```python
import torch

def _softmax(x, axis=-1):
    return torch.softmax(x, dim=axis)

def _gelu(x):
    return torch.nn.functional.gelu(x, approximate='tanh')

def _silu(x):
    return torch.nn.functional.silu(x)

def _sigmoid(x):
    return torch.sigmoid(x)

def _layernorm(x, axis=-1, eps=1e-05):
    m = x.mean(dim=axis, keepdim=True)
    v = x.var(dim=axis, keepdim=True, unbiased=False)
    return (x - m) / torch.sqrt(v + eps)

def _scatter_add(tgt, idx, src, axis=0):
    return tgt.clone().index_add_(axis, idx, src)

def _segment_sum(src, seg, n):
    out = torch.zeros((n,) + src.shape[1:], dtype=src.dtype)
    return out.index_add_(0, seg, src)

def _pad(x, pw):
    flat = tuple((v for pair in reversed(pw) for v in pair))
    return torch.nn.functional.pad(x, flat)
kernel_name = 'adaLN_cond__mha_block_cached__geglu_ffn__prompt'

def setup_inputs():

    _w = lambda a: (lambda t: t.to(torch.bfloat16) if t.is_floating_point() else t)(torch.as_tensor(a))

    def _A_setup():
        B, Sq, D = (1, 2048, 1024)
        H, Sk, Dh = (32, 2048, 128)
        return {'x': _w((lambda *s: torch.randn(*s, dtype=torch.bfloat16) * 0.05)(B, Sq, D)), 'k_cache': _w((lambda *s: torch.randn(*s, dtype=torch.bfloat16) * 3.2)(B, H, Sk, Dh)), 'v_cache': _w((lambda *s: torch.randn(*s, dtype=torch.bfloat16) * 0.4)(B, H, Sk, Dh)), 'wq': _w((lambda *s: torch.randn(*s, dtype=torch.bfloat16) * 3.2)(D, H * Dh) * D ** (-0.5)), 'wo': _w((lambda *s: torch.randn(*s, dtype=torch.bfloat16) * 0.4)(H * Dh, D) * (H * Dh) ** (-0.5))}
    _A_in = _A_setup()

    def _B_setup():
        B, S, D, F = (1, 2048, 1024, 3584)
        return {'x': _w((lambda *s: torch.randn(*s, dtype=torch.bfloat16) * 0.05)(B, S, D)), 'w_gate': _w((lambda *s: torch.randn(*s, dtype=torch.bfloat16) * 0.05)(D, F)), 'w_up': _w((lambda *s: torch.randn(*s, dtype=torch.bfloat16) * 0.05)(D, F)), 'w_down': _w((lambda *s: torch.randn(*s, dtype=torch.bfloat16) * 0.05)(F, D))}
    _B_in = _B_setup()
    return {'A_x': _A_in['x'], 'A_k_cache': _A_in['k_cache'], 'A_v_cache': _A_in['v_cache'], 'A_wq': _A_in['wq'], 'A_wo': _A_in['wo'], 'B_x': _B_in['x'], 'B_w_gate': _B_in['w_gate'], 'B_w_up': _B_in['w_up'], 'B_w_down': _B_in['w_down'], 'c': _w((lambda *s: torch.randn(*s, dtype=torch.bfloat16) * 0.1)(1, 1024) * 8.0), 'w_mod': _w((lambda *s: torch.randn(*s, dtype=torch.bfloat16) * 0.1)(1024, 6 * 1024) * 1024 ** (-0.5) * 8.0)}

def reference(A_x, A_k_cache, A_v_cache, A_wq, A_wo, B_x, B_w_gate, B_w_up, B_w_down, c, w_mod):

    def _A_ref(x, k_cache, v_cache, wq, wo):
        B, Sq, D = x.shape
        H, Dh = (32, 128)
        q = (lambda x, *d: x.permute(*d))((x @ wq).reshape(B, Sq, H, Dh), 0, 2, 1, 3)
        scores = torch.einsum('bhqd,bhkd->bhqk', q, k_cache) * Dh ** (-0.5)
        p = _softmax(scores, axis=-1)
        o = torch.einsum('bhqk,bhkd->bhqd', p, v_cache)
        return (lambda x, *d: x.permute(*d))(o, 0, 2, 1, 3).reshape(B, Sq, H * Dh) @ wo

    def _B_ref(x, w_gate, w_up, w_down):
        return _gelu(x @ w_gate) * (x @ w_up) @ w_down
    x = A_x
    mod = (_silu(c) @ w_mod).reshape(c.shape[0], 6, 1, -1)
    sa, ca, ga, sb, cb, gb = (mod[:, 0].clone(), mod[:, 1].clone(), mod[:, 2].clone(), mod[:, 3].clone(), mod[:, 4].clone(), mod[:, 5].clone())
    h = _layernorm(x, axis=-1) * (1 + ca) + sa
    x = x + ga * _A_ref(h, A_k_cache, A_v_cache, A_wq, A_wo)
    h = _layernorm(x, axis=-1) * (1 + cb) + sb
    x = x + gb * _B_ref(h, B_w_gate, B_w_up, B_w_down)
    return x
```

```cpp
#include <hip/hip_runtime.h>
#include <hip/hip_bf16.h>
typedef unsigned short u16;
__device__ inline float bf2f(u16 b){ return __uint_as_float(((unsigned)b)<<16); }
__device__ inline u16 f2bf(float f){ unsigned u=__float_as_uint(f); u += 0x7FFFu + ((u>>16)&1u); return (u16)(u>>16); }
__device__ inline float rb(float f){ return bf2f(f2bf(f)); }

__global__ void k_mod(const u16* c, const u16* wmod, float* mod){
  int n = blockIdx.x*blockDim.x+threadIdx.x; if(n>=6144) return;
  float s=0; for(int k=0;k<1024;k++){ float cv=bf2f(c[k]); float sv=rb(cv/(1.f+__expf(-cv))); s+= sv*bf2f(wmod[(size_t)k*6144+n]); }
  mod[n]=rb(s);
}
__global__ void k_ln(const u16* x, const float* shift, const float* scale, u16* h){
  int r=blockIdx.x; int t=threadIdx.x; __shared__ float red[256];
  float v[4]; float s=0; for(int i=0;i<4;i++){ v[i]=bf2f(x[(size_t)r*1024+t*4+i]); s+=v[i]; }
  red[t]=s; __syncthreads(); for(int o=128;o>0;o>>=1){ if(t<o) red[t]+=red[t+o]; __syncthreads(); }
  float m=red[0]/1024.f; __syncthreads();
  float q=0; for(int i=0;i<4;i++){ float d=v[i]-m; q+=d*d; }
  red[t]=q; __syncthreads(); for(int o=128;o>0;o>>=1){ if(t<o) red[t]+=red[t+o]; __syncthreads(); }
  float rstd=rsqrtf(red[0]/1024.f+1e-5f);
  for(int i=0;i<4;i++){ int k=t*4+i; h[(size_t)r*1024+k]=f2bf((v[i]-m)*rstd*rb(1.f+scale[k])+shift[k]); }
}
__global__ void k_gemm(const u16* A, const u16* B, u16* C, int M,int N,int K,int mode,const u16* res,const float* gate){
  __shared__ float As[16][17], Bs[16][17];
  int tx=threadIdx.x, ty=threadIdx.y; int row=blockIdx.y*16+ty, col=blockIdx.x*16+tx; float s=0;
  for(int k0=0;k0<K;k0+=16){ As[ty][tx]=bf2f(A[(size_t)row*K+k0+tx]); Bs[ty][tx]=bf2f(B[(size_t)(k0+ty)*N+col]); __syncthreads();
    for(int k=0;k<16;k++) s+=As[ty][k]*Bs[k][tx]; __syncthreads(); }
  if(mode==0) C[(size_t)row*N+col]=f2bf(s); else C[(size_t)row*N+col]=f2bf(bf2f(res[(size_t)row*N+col])+gate[col]*rb(s));
}
__global__ void k_attn(const u16* q,const u16* kc,const u16* vc,u16* o){
  int r=blockIdx.x, h=blockIdx.y, t=threadIdx.x; __shared__ float sc[2048]; __shared__ float qs[128]; __shared__ float red[256];
  if(t<128) qs[t]=bf2f(q[(size_t)r*4096+h*128+t]); __syncthreads();
  float mx=-1e30f;
  for(int j=t;j<2048;j+=256){ const u16* kr=kc+((size_t)h*2048+j)*128; float s=0; for(int d=0;d<128;d++) s+=qs[d]*bf2f(kr[d]); s*=0.08838834764831845f; sc[j]=s; mx=fmaxf(mx,s);}
  red[t]=mx; __syncthreads(); for(int o2=128;o2>0;o2>>=1){ if(t<o2) red[t]=fmaxf(red[t],red[t+o2]); __syncthreads(); }
  mx=red[0]; __syncthreads(); float sm=0;
  for(int j=t;j<2048;j+=256){ float p=__expf(sc[j]-mx); sc[j]=p; sm+=p; }
  red[t]=sm; __syncthreads(); for(int o2=128;o2>0;o2>>=1){ if(t<o2) red[t]+=red[t+o2]; __syncthreads(); }
  sm=red[0];
  if(t<128){ float a=0; for(int j=0;j<2048;j++) a+=sc[j]*bf2f(vc[((size_t)h*2048+j)*128+t]); o[(size_t)r*4096+h*128+t]=f2bf(a/sm); }
}
__global__ void k_geglu(const u16* g,const u16* u,u16* a,size_t n){
  size_t i=(size_t)blockIdx.x*blockDim.x+threadIdx.x; if(i>=n) return; float x=bf2f(g[i]);
  float y=0.5f*x*(1.f+tanhf(0.7978845608028654f*(x+0.044715f*x*x*x))); a[i]=f2bf(rb(y)*bf2f(u[i]));
}
extern "C" void kernel_launch(void* const* d_in, const int* in_sizes, int n_in, void* d_out, int out_size, void* d_ws, size_t ws_size, hipStream_t stream) {
  const u16* x=(const u16*)d_in[0]; const u16* kc=(const u16*)d_in[1]; const u16* vc=(const u16*)d_in[2]; const u16* wq=(const u16*)d_in[3]; const u16* wo=(const u16*)d_in[4];
  const u16* wg=(const u16*)d_in[6]; const u16* wu=(const u16*)d_in[7]; const u16* wd=(const u16*)d_in[8]; const u16* c=(const u16*)d_in[9]; const u16* wmod=(const u16*)d_in[10];
  char* ws=(char*)d_ws; float* mod=(float*)ws; u16* h=(u16*)(ws+(1<<20)); u16* q=(u16*)(ws+(8<<20)); u16* o=(u16*)(ws+(32<<20)); u16* x1=(u16*)(ws+(56<<20)); u16* g=(u16*)(ws+(64<<20)); u16* u=(u16*)(ws+(96<<20)); u16* a=(u16*)(ws+(128<<20));
  u16* out=(u16*)d_out;
  k_mod<<<24,256,0,stream>>>(c,wmod,mod);
  k_ln<<<2048,256,0,stream>>>(x,mod,mod+1024,h);
  k_gemm<<<dim3(4096/16,2048/16),dim3(16,16),0,stream>>>(h,wq,q,2048,4096,1024,0,nullptr,nullptr);
  k_attn<<<dim3(2048,32),256,0,stream>>>(q,kc,vc,o);
  k_gemm<<<dim3(1024/16,2048/16),dim3(16,16),0,stream>>>(o,wo,x1,2048,1024,4096,1,x,mod+2048);
  k_ln<<<2048,256,0,stream>>>(x1,mod+3072,mod+4096,h);
  k_gemm<<<dim3(3584/16,2048/16),dim3(16,16),0,stream>>>(h,wg,g,2048,3584,1024,0,nullptr,nullptr);
  k_gemm<<<dim3(3584/16,2048/16),dim3(16,16),0,stream>>>(h,wu,u,2048,3584,1024,0,nullptr,nullptr);
  k_geglu<<<(2048*3584+255)/256,256,0,stream>>>(g,u,a,(size_t)2048*3584);
  k_gemm<<<dim3(1024/16,2048/16),dim3(16,16),0,stream>>>(a,wd,out,2048,1024,3584,1,x1,mod+5120);
}
```

```cpp
#include <hip/hip_runtime.h>
#include <hip/hip_bf16.h>
#define OUTAUX 2
typedef unsigned short u16;
typedef float f32x32 __attribute__((ext_vector_type(32)));
typedef float f32x16 __attribute__((ext_vector_type(16)));
typedef float f32x8 __attribute__((ext_vector_type(8)));
typedef float f32x4 __attribute__((ext_vector_type(4)));
typedef float f32x2 __attribute__((ext_vector_type(2)));
typedef unsigned u32x16 __attribute__((ext_vector_type(16)));
typedef unsigned u32x4 __attribute__((ext_vector_type(4)));
typedef unsigned u32x2 __attribute__((ext_vector_type(2)));
typedef __bf16 bf16x2 __attribute__((ext_vector_type(2)));
typedef __attribute__((address_space(1))) unsigned gu32;
typedef __attribute__((address_space(1))) unsigned long long gu64;
#define DI __device__ __forceinline__
#define NTLD(p) __builtin_nontemporal_load(p)
DI float bf2f(u16 b){ return __uint_as_float(((unsigned)b)<<16); }
DI float bflo(unsigned w){ return __uint_as_float(w<<16); }
DI float bfhi(unsigned w){ return __uint_as_float(w & 0xffff0000u); }
DI unsigned cvtpk(float lo, float hi){ f32x2 v={lo,hi}; bf16x2 b=__builtin_convertvector(v,bf16x2); return __builtin_bit_cast(unsigned,b); }
DI float rb(float f){ return bflo(cvtpk(f,0.f)); }
DI float dot2sq(unsigned w, float acc){ return __builtin_amdgcn_fdot2_f32_bf16(__builtin_bit_cast(bf16x2,w), __builtin_bit_cast(bf16x2,w), acc, false); }
DI unsigned rfl(unsigned x){ return __builtin_amdgcn_readfirstlane(x); }
constexpr float QS=0.12751743082f;
constexpr unsigned M_=2048, MB_=1u<<20;
constexpr unsigned WS_MODF=0, WS_EP=200*1024, WS_CNT=256*1024, WS_G=768*1024, WS_HF=840*1024, WS_H=1*MB_, WS_O=8*MB_, WS_P=24*MB_, WS_X1=40*MB_, WS_XF=44*MB_, WS_H2=48*MB_, WS_ACT=52*MB_;
constexpr unsigned C_O=1, C_A=6, C_H2=10, C_X3=16, C_X5=24, C_X1=56;
#define CNT(r,k) (cnt+((r)*96+(k))*64)

DI int fpos32(int k){ const int kk=k&31; return (k&~31)+8*((kk>>2)&3)+4*(kk>>4)+(kk&3); }

DI __amdgpu_buffer_rsrc_t mkrsrc(const void* p, unsigned bytes){ return __builtin_amdgcn_make_buffer_rsrc((void*)p, (short)0, (int)bytes, 0x00020000); }
DI void st16_sc1(__amdgpu_buffer_rsrc_t r, unsigned off, u32x4 v){ __builtin_amdgcn_raw_buffer_store_b128(v, r, off, 0, 16); }
DI void st8_sc1(__amdgpu_buffer_rsrc_t r, unsigned off, u32x2 v){ __builtin_amdgcn_raw_buffer_store_b64(v, r, off, 0, 16); }
DI u32x4 ld16_sc1(__amdgpu_buffer_rsrc_t r, unsigned off){ return __builtin_amdgcn_raw_buffer_load_b128(r, off, 0, 16); }
DI void arrive_wave(unsigned* cnt){ asm volatile("s_waitcnt vmcnt(0)":::"memory");
  if((threadIdx.x&63)==0) __hip_atomic_fetch_add((gu32*)cnt, 1u, __ATOMIC_RELAXED, __HIP_MEMORY_SCOPE_AGENT); }
DI void poll(unsigned* cnt, unsigned want){ unsigned spins=0;
  while(__builtin_amdgcn_readfirstlane(__hip_atomic_load((gu32*)cnt, __ATOMIC_RELAXED, __HIP_MEMORY_SCOPE_AGENT))<want){ __builtin_amdgcn_s_sleep(1); if(++spins>(1u<<17)) break; } }
template<int C> DI float dppf(float v){ return __builtin_bit_cast(float,__builtin_amdgcn_mov_dpp(__builtin_bit_cast(int,v),C,0xf,0xf,true)); }
DI float sum16(float v){ v+=dppf<0x128>(v); v+=dppf<0x124>(v); v+=dppf<0x122>(v); v+=dppf<0x121>(v); return v; }
DI float sumx16(float v){ auto r=__builtin_amdgcn_permlane16_swap(__float_as_uint(v),__float_as_uint(v),false,false); return __uint_as_float(r[0])+__uint_as_float(r[1]); }
DI float sumx32(float v){ auto r=__builtin_amdgcn_permlane32_swap(__float_as_uint(v),__float_as_uint(v),false,false); return __uint_as_float(r[0])+__uint_as_float(r[1]); }
DI float sumg(float v){ return sumx32(sumx16(v)); }
DI void poll8(unsigned* cnt, unsigned want){ unsigned spins=0;
  while(__builtin_amdgcn_ballot_w64(__hip_atomic_load((gu32*)cnt, __ATOMIC_RELAXED, __HIP_MEMORY_SCOPE_AGENT)<want)!=0ull){ __builtin_amdgcn_s_sleep(1); if(++spins>(1u<<17)) break; } }
#define OUTST(r,off,v) __builtin_amdgcn_raw_buffer_store_b128(v, r, off, 0, OUTAUX)
#define SB_() __builtin_amdgcn_sched_barrier(0)
#define WAITVM(n) asm volatile("s_waitcnt vmcnt(" #n ")":::"memory")
#define ADD1(c) do{ if((threadIdx.x&63)==0) __hip_atomic_fetch_add((gu32*)(c), 1u, __ATOMIC_RELAXED, __HIP_MEMORY_SCOPE_AGENT); asm volatile("":::"memory"); }while(0)
#define BAR_() do{ asm volatile("s_waitcnt lgkmcnt(0)":::"memory"); __builtin_amdgcn_s_barrier(); asm volatile("":::"memory"); }while(0)
#define DRAIN_BAR() do{ asm volatile("s_waitcnt vmcnt(0) lgkmcnt(0)":::"memory"); __builtin_amdgcn_s_barrier(); asm volatile("":::"memory"); }while(0)
DI void wait_count(unsigned* cnt, unsigned want){ if(threadIdx.x<64) poll(cnt,want); BAR_(); }

#define ACC4(idx) ((idx)<32?a0[(idx)&31]:(idx)<64?a1[(idx)&31]:(idx)<96?a2[(idx)&31]:a3[(idx)&31])
#define ACC7(idx) ((idx)<32?a0[(idx)&31]:(idx)<64?a1[(idx)&31]:(idx)<96?a2[(idx)&31]:(idx)<128?a3[(idx)&31]:(idx)<160?a4[(idx)&31]:(idx)<192?a5[(idx)&31]:a6[(idx)&31])
#define LANE() unsigned tid_=threadIdx.x; asm volatile("":"+v"(tid_)); const unsigned lane=tid_&63, g=lane>>4, i16=lane&15, q=i16>>2, p=i16&3, vXl=lane*16;
#define W128_RD() const unsigned vWr0=(g>>1)*2048+(4*(g&1)+q)*64+((0^(g&1))*32)+p*8, vWr1=(g>>1)*2048+(4*(g&1)+q)*64+((1^(g&1))*32)+p*8;
#define W128_DMA(ldw) unsigned vWd[4]; { const unsigned sub=lane>>5, row=(lane>>2)&7, ps=lane&3, c=ps^(((row>>2)&1)<<1); \
    for(int i=0;i<4;i++) vWd[i]=(row+8*(i>>1))*(ldw)+(i&1)*128+sub*64+c*16; }
#define KV_DMA() unsigned vKp0,vKp1,vVp; \
  { const unsigned r=lane>>3,pos=lane&7; vKp0=r*256+((pos^((r>>1)&7))*16); vKp1=r*256+((pos^((4+(r>>1))&7))*16); \
    const unsigned sub=lane>>5,row=(lane>>2)&7,ps=lane&3,c=ps^(((row>>2)&1)<<1); vVp=row*256+sub*64+c*16; }
#define G112_DMA() unsigned vWd0, vWd1; \
  { const unsigned sub=lane>>5, row=(lane>>2)&7, ps=lane&3, c=ps^(((row>>2)&1)<<1); vWd0=(8*sub+row)*7168+c*16; vWd1=(lane>>1)*7168+(lane&1)*16; }

DI unsigned gelu_mul2(f32x2 gt, f32x2 up){
  const float c1=-2.302208198f, c2=c1*0.044715f;
  const f32x2 a=gt*((gt*gt)*c2+c1);
  const f32x2 d=(f32x2){__builtin_amdgcn_exp2f(a[0]),__builtin_amdgcn_exp2f(a[1])}+1.f;
  const f32x2 r=(gt*up)*(f32x2){__builtin_amdgcn_rcpf(d[0]),__builtin_amdgcn_rcpf(d[1])};
  return cvtpk(r[0],r[1]);
}

#define X_PUBLISH(CX) \
  const __amdgpu_buffer_rsrc_t rP=mkrsrc(ws+WS_P,4u*M_*2048u); \
  { const unsigned base=(sp*M_*1024u+(T*32+ct*4)*2048u+lane*8u)*2u; \
    _Pragma("unroll") for(int m=0;m<4;m++) _Pragma("unroll") for(int tb=0;tb<4;tb++){ \
      const int b0=((2*m)*4+tb)*4, b1=((2*m+1)*4+tb)*4; \
      u32x4 v={cvtpk(ACC4(b0),ACC4(b0+1)),cvtpk(ACC4(b0+2),ACC4(b0+3)),cvtpk(ACC4(b1),ACC4(b1+1)),cvtpk(ACC4(b1+2),ACC4(b1+3))}; \
      st16_sc1(rP, base+(m*4+tb)*1024u, v); SB_(); } } \
  arrive_wave(CNT(rt,CX+w));
#define X_FRAG() const unsigned k2=(lane>>3)&1, r8=lane&7, fo=((To*32+8*w+k2)*4+(ct>>1))*1024+(g*16+(ct&1)*8+r8)*16;
#define X_GATHER(CX) \
  wait_count(CNT(rt,CX+sp),32); \
  u32x4 pp[4][4]; \
  _Pragma("unroll") for(int j=0;j<4;j++) _Pragma("unroll") for(int s_=0;s_<4;s_++) pp[j][s_]=ld16_sc1(rP,s_*M_*2048u+fo+j*8192u); \
  float y[4][8]; \
  _Pragma("unroll") for(int j=0;j<4;j++) \
    _Pragma("unroll") for(int e=0;e<4;e++){ y[j][2*e]=(bflo(pp[j][0][e])+bflo(pp[j][1][e]))+(bflo(pp[j][2][e])+bflo(pp[j][3][e])); y[j][2*e+1]=(bfhi(pp[j][0][e])+bfhi(pp[j][1][e]))+(bfhi(pp[j][2][e])+bfhi(pp[j][3][e])); }

#define EXCHANGE4(CX) \
  u32x4* own=(u32x4*)(smem_+49152); \
  const __amdgpu_buffer_rsrc_t rP=mkrsrc(ws+WS_P,4u*M_*2048u); \
  { const unsigned base=(sp*M_*1024u+(T*32+ct*4)*2048u+lane*8u)*2u; \
    _Pragma("unroll") for(int m=0;m<4;m++) _Pragma("unroll") for(int tb=0;tb<4;tb++){ \
      const int b0=((2*m)*4+tb)*4, b1=((2*m+1)*4+tb)*4; \
      u32x4 v={cvtpk(ACC4(b0),ACC4(b0+1)),cvtpk(ACC4(b0+2),ACC4(b0+3)),cvtpk(ACC4(b1),ACC4(b1+1)),cvtpk(ACC4(b1+2),ACC4(b1+3))}; \
      if(w==sp) own[(m*4+tb)*64+lane]=v; else st16_sc1(rP, base+(m*4+tb)*1024u, v); SB_(); } } \
  unsigned* c1=CNT(rt,CX+ct*4);    \
  if(w!=sp) arrive_wave(c1+w*64); \
 \
  wait_count(c1+sp*64,3); \
  u32x4 pp[4][3]; \
  _Pragma("unroll") for(int m=0;m<4;m++) _Pragma("unroll") for(int s=0;s<3;s++){ const unsigned s2=s+(s>=(int)sp?1:0); \
      pp[m][s]=ld16_sc1(rP,(s2*M_*1024u+((To*32+ct*4+m)*4+w)*512u+lane*8u)*2u); } \
  float y[4][8]; \
  _Pragma("unroll") for(int m=0;m<4;m++){ const u32x4 o=own[(m*4+w)*64+lane]; \
    _Pragma("unroll") for(int e=0;e<4;e++){ y[m][2*e]=(bflo(o[e])+bflo(pp[m][0][e]))+(bflo(pp[m][1][e])+bflo(pp[m][2][e])); y[m][2*e+1]=(bfhi(o[e])+bfhi(pp[m][0][e]))+(bfhi(pp[m][1][e])+bfhi(pp[m][2][e])); } }

#define PRE_W128(Wp,szW,row0) { unsigned vWo[4]; \
      { const unsigned sub=lane>>5, row=(lane>>2)&7, ps=lane&3, c=ps^(((row>>2)&1)<<1); for(int i=0;i<4;i++) vWo[i]=(row+8*(i>>1))*2048+(i&1)*128+sub*64+c*16; } \
      const unsigned offWo=rfl(((row0)+16*w)*2048+ct*256), wdo=rfl(w*4096); \
      asm volatile( \
    "s_mov_b64 s[40:41], %0\n" \
    "s_mov_b32 s42, %1\n" \
    "s_mov_b32 s43, 0x20000\n" \
    "s_mov_b32 s53, %2\n" \
    "s_mov_b32 s56, %3\n" \
    "s_mov_b32 s57, 0\n" \
    "s_add_u32 m0, s57, s56\n" \
    "s_nop 0\n" \
    "buffer_load_dwordx4 %4, s[40:43], s53 offen lds\n" \
    "s_add_u32 m0, m0, 1024\n" \
    "s_nop 0\n" \
    "buffer_load_dwordx4 %5, s[40:43], s53 offen lds\n" \
    "s_add_u32 m0, m0, 1024\n" \
    "s_nop 0\n" \
    "buffer_load_dwordx4 %6, s[40:43], s53 offen lds\n" \
    "s_add_u32 m0, m0, 1024\n" \
    "s_nop 0\n" \
    "buffer_load_dwordx4 %7, s[40:43], s53 offen lds\n" \
    "s_add_u32 s53, s53, 131072\n" \
    "s_add_u32 s57, s57, 16384\n" \
    "s_cmp_ge_u32 s57, 49152\n" \
    "s_cselect_b32 s57, 0, s57\n" \
    "s_add_u32 m0, s57, s56\n" \
    "s_nop 0\n" \
    "buffer_load_dwordx4 %4, s[40:43], s53 offen lds\n" \
    "s_add_u32 m0, m0, 1024\n" \
    "s_nop 0\n" \
    "buffer_load_dwordx4 %5, s[40:43], s53 offen lds\n" \
    "s_add_u32 m0, m0, 1024\n" \
    "s_nop 0\n" \
    "buffer_load_dwordx4 %6, s[40:43], s53 offen lds\n" \
    "s_add_u32 m0, m0, 1024\n" \
    "s_nop 0\n" \
    "buffer_load_dwordx4 %7, s[40:43], s53 offen lds\n" \
    "s_add_u32 s53, s53, 131072\n" \
    "s_add_u32 s57, s57, 16384\n" \
    "s_cmp_ge_u32 s57, 49152\n" \
    "s_cselect_b32 s57, 0, s57\n" \
    "s_add_u32 m0, s57, s56\n" \
    "s_nop 0\n" \
    "buffer_load_dwordx4 %4, s[40:43], s53 offen lds\n" \
    "s_add_u32 m0, m0, 1024\n" \
    "s_nop 0\n" \
    "buffer_load_dwordx4 %5, s[40:43], s53 offen lds\n" \
        :: "s"(Wp), "s"(szW), "s"(offWo), "s"(wdo), "v"(vWo[0]), "v"(vWo[1]), "v"(vWo[2]), "v"(vWo[3]) : "memory", "vcc", "scc", "m0", "s36", "s37", "s38", "s39", "s40", "s41", "s42", "s43", "s44", "s45", "s46", "s47", "s48", "s49", "s50", "s51", "s52", "s53", "s54", "s55", "s56", "s57", "s58", "s59", "s60", "s61", "s62", "s63", "s64", "s65", "s66", "s67", "s68", "s69", "s70"); }
#define PRE_G112() { G112_DMA() \
      const unsigned paircol=112*ct4+(odd4?16:0), lonecol=112*ct4+(odd4?0:96); \
      const unsigned offW4=rfl(16*w*7168+paircol*2), offL4=rfl(32*(w&1)*7168+lonecol*2), wdst4=rfl(w*3072), ldst4=rfl(24576+w*1024); \
      const u16* Wl=(w>>1)?Wu:Wg; \
      asm volatile( \
    "s_mov_b64 s[40:41], %0\n" \
    "s_mov_b32 s42, %3\n" \
    "s_mov_b32 s43, 0x20000\n" \
    "s_mov_b64 s[44:45], %1\n" \
    "s_mov_b32 s46, %3\n" \
    "s_mov_b32 s47, 0x20000\n" \
    "s_mov_b64 s[48:49], %2\n" \
    "s_mov_b32 s50, %3\n" \
    "s_mov_b32 s51, 0x20000\n" \
    "s_mov_b32 s53, %4\n" \
    "s_mov_b32 s56, %6\n" \
    "s_mov_b32 s54, %5\n" \
    "s_mov_b32 s61, %7\n" \
    "s_mov_b32 s57, 73728\n" \
    "s_add_u32 m0, s57, s56\n" \
    "s_nop 0\n" \
    "buffer_load_dwordx4 %8, s[40:43], s53 offen lds\n" \
    "s_add_u32 m0, m0, 960\n" \
    "s_nop 0\n" \
    "buffer_load_dwordx4 %8, s[40:43], s53 offen offset:64 lds\n" \
    "s_add_u32 m0, m0, 960\n" \
    "s_nop 0\n" \
    "buffer_load_dwordx4 %8, s[40:43], s53 offen offset:128 lds\n" \
    "s_add_u32 m0, m0, 10368\n" \
    "s_nop 0\n" \
    "buffer_load_dwordx4 %8, s[44:47], s53 offen lds\n" \
    "s_add_u32 m0, m0, 960\n" \
    "s_nop 0\n" \
    "buffer_load_dwordx4 %8, s[44:47], s53 offen offset:64 lds\n" \
    "s_add_u32 m0, m0, 960\n" \
    "s_nop 0\n" \
    "buffer_load_dwordx4 %8, s[44:47], s53 offen offset:128 lds\n" \
    "s_add_u32 m0, s57, s61\n" \
    "s_nop 0\n" \
    "buffer_load_dwordx4 %9, s[48:51], s54 offen lds\n" \
    "s_add_u32 s53, s53, 458752\n" \
    "s_add_u32 s54, s54, 458752\n" \
    "s_add_u32 s57, s57, 28672\n" \
    "s_cmp_ge_u32 s57, 159744\n" \
    "s_cselect_b32 s57, 73728, s57\n" \
    "s_add_u32 m0, s57, s56\n" \
    "s_nop 0\n" \
    "buffer_load_dwordx4 %8, s[40:43], s53 offen lds\n" \
    "s_add_u32 m0, m0, 960\n" \
    "s_nop 0\n" \
    "buffer_load_dwordx4 %8, s[40:43], s53 offen offset:64 lds\n" \
    "s_add_u32 m0, m0, 960\n" \
    "s_nop 0\n" \
    "buffer_load_dwordx4 %8, s[40:43], s53 offen offset:128 lds\n" \
    "s_add_u32 m0, m0, 10368\n" \
    "s_nop 0\n" \
    "buffer_load_dwordx4 %8, s[44:47], s53 offen lds\n" \
    "s_add_u32 m0, m0, 960\n" \
    "s_nop 0\n" \
    "buffer_load_dwordx4 %8, s[44:47], s53 offen offset:64 lds\n" \
    "s_add_u32 m0, m0, 960\n" \
    "s_nop 0\n" \
    "buffer_load_dwordx4 %8, s[44:47], s53 offen offset:128 lds\n" \
    "s_add_u32 m0, s57, s61\n" \
    "s_nop 0\n" \
    "buffer_load_dwordx4 %9, s[48:51], s54 offen lds\n" \
    "s_add_u32 s53, s53, 458752\n" \
    "s_add_u32 s54, s54, 458752\n" \
    "s_add_u32 s57, s57, 28672\n" \
    "s_cmp_ge_u32 s57, 159744\n" \
    "s_cselect_b32 s57, 73728, s57\n" \
    "s_add_u32 m0, s57, s56\n" \
    "s_nop 0\n" \
    "buffer_load_dwordx4 %8, s[40:43], s53 offen lds\n" \
    "s_add_u32 m0, m0, 960\n" \
    "s_nop 0\n" \
    "buffer_load_dwordx4 %8, s[40:43], s53 offen offset:64 lds\n" \
    "s_add_u32 m0, m0, 960\n" \
    "s_nop 0\n" \
    "buffer_load_dwordx4 %8, s[40:43], s53 offen offset:128 lds\n" \
    "s_add_u32 m0, m0, 10368\n" \
    "s_nop 0\n" \
    "buffer_load_dwordx4 %8, s[44:47], s53 offen lds\n" \
        :: "s"(Wg), "s"(Wu), "s"(Wl), "s"(1024u*7168u), "s"(offW4), "s"(offL4), "s"(wdst4), "s"(ldst4), "v"(vWd0), "v"(vWd1) : "memory", "vcc", "scc", "m0", "s36", "s37", "s38", "s39", "s40", "s41", "s42", "s43", "s44", "s45", "s46", "s47", "s48", "s49", "s50", "s51", "s52", "s53", "s54", "s55", "s56", "s57", "s58", "s59", "s60", "s61", "s62", "s63", "s64", "s65", "s66", "s67", "s68", "s69", "s70"); }

struct Args { const u16 *x, *Kc, *Vc, *Wq, *Wo, *Wg, *Wu, *Wd, *cv, *wmod; char* ws; u16* out; };
typedef const __attribute__((address_space(4))) Args* kargs_t;
#define PHASE() kargs_t ka_=(kargs_t)__builtin_amdgcn_kernarg_segment_ptr(); unsigned bid_=blockIdx.x; asm volatile("":"+s"(ka_),"+s"(bid_)); \
  const u16 *cv=ka_->cv, *wmod=ka_->wmod; (void)cv; (void)wmod; const u16 *x=ka_->x, *Kc=ka_->Kc, *Vc=ka_->Vc, *Wq=ka_->Wq, *Wo=ka_->Wo, *Wg=ka_->Wg, *Wu=ka_->Wu, *Wd=ka_->Wd; char* ws=ka_->ws; u16* out=ka_->out; \
  (void)x;(void)Kc;(void)Vc;(void)Wq;(void)Wo;(void)Wg;(void)Wu;(void)Wd;(void)out; \
  const unsigned w=rfl(threadIdx.x>>6), xcd=bid_&7, loc=bid_>>3; unsigned* cnt=(unsigned*)(ws+WS_CNT); (void)cnt; \
    \
  const unsigned hh=((xcd&1)^1)*16+(xcd>>1)*4+(loc>>3), qblk=loc&7, sp=(xcd&1)*2+((xcd>>1)&1), rt=(xcd>>2)*4+(loc>>3), ct=loc&7, To=rt*4+sp, rt4=(xcd&1)*4+(loc&3), ct4=(xcd>>1)*8+(loc>>2), odd4=ct4&1; \
  const unsigned sp5=xcd>>1, rt5=((xcd&1)^1)*4+(loc>>3); (void)sp5; (void)rt5; \
  (void)hh;(void)qblk;(void)sp;(void)rt;(void)ct;(void)To;(void)rt4;(void)ct4;(void)odd4;
__global__ __launch_bounds__(256,1) void mega(const u16* x_, const u16* Kc_, const u16* Vc_, const u16* Wq_, const u16* Wo_, const u16* Wg_, const u16* Wu_, const u16* Wd_, const u16* cv_, const u16* wmod_, char* ws_, u16* out_){
  extern __shared__ __attribute__((aligned(16))) char smem_[];
  unsigned tagLo, tagHi;
  { PHASE() LANE() W128_DMA(8192u) KV_DMA() (void)g;(void)i16;(void)q;(void)p;
    if(tid_<3) __hip_atomic_store((gu32*)CNT(qblk,hh*3+tid_), 0u, __ATOMIC_RELAXED, __HIP_MEMORY_SCOPE_AGENT);
    const unsigned r=tid_>>4, c16=tid_&15, row0=qblk*256+(hh>>1)*16, ch=hh&1;
    const unsigned s4=xcd&3, cg=(xcd>>2)*32+loc, grow=s4*256+(tid_>>2);
    f32x32 rawv; u32x16 wmv; u32x4 cvv; u32x2 ep;
    { const unsigned offW=rfl(16*w*8192+hh*256), wdst=rfl(w*4096), cofs=rfl((loc*4+w)*1024);
      const char* pK=(const char*)Kc+(size_t)hh*2048*256-1024; const char* pV=(const char*)Vc+(size_t)hh*2048*256-1024;
      const unsigned vX=(row0+r)*2048+c16*16+ch*1024, vX2=(row0+r)*2048+c16*16+(ch^1)*1024, vWm=grow*12288+cg*64+(tid_&3)*16, vC=grow*2;
      const unsigned pfo=rfl((qblk*128+w*32)*8192+hh*256), pfv=(lane>>4)*8192+(lane&15)*16;
      asm volatile(
    "L_mega_code:\n"
    "s_getpc_b64 s[96:97]\n"
    "L_pc5:\n"
    "s_sub_u32 s96, s96, L_pc5-L_mega_code\n"
    "s_subb_u32 s97, s97, 0\n"
    "s_mov_b32 s98, L_text_end-L_mega_code\n"
    "s_mov_b32 s99, 0x20000\n"
    "s_and_b32 s100, %23, 3072\n"
    "s_add_u32 m0, s100, 159744\n"
    "s_nop 0\n"
    "buffer_load_dwordx4 %22, s[96:99], %23 offen lds\n"
    "s_mov_b64 s[84:85], %17\n"
    "s_mov_b32 s86, 8\n"
    "s_mov_b32 s87, 0x20000\n"
    "s_mov_b64 s[80:81], %16\n"
    "s_mov_b32 s82, 2048\n"
    "s_mov_b32 s83, 0x20000\n"
    "s_mov_b64 s[92:93], %15\n"
    "s_mov_b32 s94, 12582912\n"
    "s_mov_b32 s95, 0x20000\n"
    "s_mov_b64 s[88:89], %14\n"
    "s_mov_b32 s90, 4194304\n"
    "s_mov_b32 s91, 0x20000\n"
    "s_mov_b32 s79, 0\n"
    "buffer_load_dwordx2 v[52:53], off, s[84:87], s79 sc1\n"
    "buffer_load_ushort v48, %21, s[80:83], s79 offen\n"
    "buffer_load_ushort v49, %21, s[80:83], s79 offen offset:128\n"
    "buffer_load_ushort v50, %21, s[80:83], s79 offen offset:256\n"
    "buffer_load_ushort v51, %21, s[80:83], s79 offen offset:384\n"
    "buffer_load_dwordx4 v[32:35], %20, s[92:95], s79 offen nt\n"
    "s_add_u32 s79, s79, 786432\n"
    "buffer_load_dwordx4 v[36:39], %20, s[92:95], s79 offen nt\n"
    "s_add_u32 s79, s79, 786432\n"
    "buffer_load_dwordx4 v[40:43], %20, s[92:95], s79 offen nt\n"
    "s_add_u32 s79, s79, 786432\n"
    "buffer_load_dwordx4 v[44:47], %20, s[92:95], s79 offen nt\n"
    "s_add_u32 s79, s79, 786432\n"
    "s_mov_b32 s79, 0\n"
    "buffer_load_dwordx4 v[0:3], %18, s[88:91], s79 offen nt\n"
    "buffer_load_dwordx4 v[4:7], %18, s[88:91], s79 offen offset:256 nt\n"
    "buffer_load_dwordx4 v[8:11], %18, s[88:91], s79 offen offset:512 nt\n"
    "buffer_load_dwordx4 v[12:15], %18, s[88:91], s79 offen offset:768 nt\n"
    "buffer_load_dwordx4 v[16:19], %19, s[88:91], s79 offen nt\n"
    "buffer_load_dwordx4 v[20:23], %19, s[88:91], s79 offen offset:256 nt\n"
    "buffer_load_dwordx4 v[24:27], %19, s[88:91], s79 offen offset:512 nt\n"
    "buffer_load_dwordx4 v[28:31], %19, s[88:91], s79 offen offset:768 nt\n"
    "s_mov_b64 s[64:65], %6\n"
    "s_mov_b32 s66, 525312\n"
    "s_mov_b32 s67, 0x20000\n"
    "s_mov_b64 s[68:69], %7\n"
    "s_mov_b32 s70, 525312\n"
    "s_mov_b32 s71, 0x20000\n"
    "s_mov_b32 s79, %9\n"
    "s_mov_b32 s72, %9\n"
    "s_mov_b32 s73, 49152\n"
    "s_mov_b32 s74, 98304\n"
    "s_mov_b32 s75, 49152\n"
    "s_mov_b32 s76, 98304\n"
    "v_add_u32 v204, 1024, %11\n"
    "v_add_u32 v205, 128, %11\n"
    "v_add_u32 v206, 1024, %12\n"
    "v_add_u32 v207, 128, %12\n"
    "v_add_u32 v208, 1024, %13\n"
    "v_add_u32 v209, 128, %13\n"
    "v_mov_b32 v200, %10\n"
    "v_add_u32 v201, 128, %10\n"
    "v_add_u32 v202, 65536, %10\n"
    "v_add_u32 v203, 65664, %10\n"
    "v_add_u32 v210, 49152, %10\n"
    "v_add_u32 v211, 49152, %10\n"
    "v_add_u32 v212, 98304, %10\n"
    "v_add_u32 v213, 98304, %10\n"
    "s_mov_b64 s[40:41], %4\n"
    "s_mov_b32 s42, %5\n"
    "s_mov_b32 s43, 0x20000\n"
    "s_mov_b32 s53, %8\n"
    "s_mov_b32 s56, %9\n"
    "s_mov_b32 s57, 0\n"
    "s_add_u32 m0, s57, s56\n"
    "s_nop 0\n"
    "buffer_load_dwordx4 v200, s[40:43], s53 offen lds\n"
    "s_add_u32 m0, m0, 1024\n"
    "s_nop 0\n"
    "buffer_load_dwordx4 v201, s[40:43], s53 offen lds\n"
    "s_add_u32 m0, m0, 1024\n"
    "s_nop 0\n"
    "buffer_load_dwordx4 v202, s[40:43], s53 offen lds\n"
    "s_add_u32 m0, m0, 1024\n"
    "s_nop 0\n"
    "buffer_load_dwordx4 v203, s[40:43], s53 offen lds\n"
    "s_add_u32 s53, s53, 524288\n"
    "s_add_u32 s57, s57, 16384\n"
    "s_cmp_ge_u32 s57, 49152\n"
    "s_cselect_b32 s57, 0, s57\n"
    "s_add_u32 m0, s57, s56\n"
    "s_nop 0\n"
    "buffer_load_dwordx4 v200, s[40:43], s53 offen lds\n"
    "s_add_u32 m0, m0, 1024\n"
    "s_nop 0\n"
    "buffer_load_dwordx4 v201, s[40:43], s53 offen lds\n"
    "s_add_u32 m0, m0, 1024\n"
    "s_nop 0\n"
    "buffer_load_dwordx4 v202, s[40:43], s53 offen lds\n"
    "s_add_u32 m0, m0, 1024\n"
    "s_nop 0\n"
    "buffer_load_dwordx4 v203, s[40:43], s53 offen lds\n"
    "s_add_u32 s53, s53, 524288\n"
    "s_add_u32 s57, s57, 16384\n"
    "s_cmp_ge_u32 s57, 49152\n"
    "s_cselect_b32 s57, 0, s57\n"
    "s_add_u32 m0, s57, s56\n"
    "s_nop 0\n"
    "buffer_load_dwordx4 v200, s[40:43], s53 offen lds\n"
    "s_add_u32 m0, m0, 1024\n"
    "s_nop 0\n"
    "buffer_load_dwordx4 v201, s[40:43], s53 offen lds\n"
    "s_add_u32 m0, s73, s79\n"
    "s_nop 0\n"
    "buffer_load_dwordx4 v204, s[64:67], s72 offen nt lds\n"
    "buffer_load_dwordx4 v205, s[64:67], s72 offen offset:1024 nt lds\n"
    "buffer_load_dwordx4 v206, s[64:67], s72 offen offset:2048 nt lds\n"
    "buffer_load_dwordx4 v207, s[64:67], s72 offen offset:3072 nt lds\n"
    "s_add_u32 m0, s74, s79\n"
    "s_nop 0\n"
    "buffer_load_dwordx4 v208, s[68:71], s72 offen nt lds\n"
    "buffer_load_dwordx4 v209, s[68:71], s72 offen offset:1024 nt lds\n"
    "buffer_load_dwordx4 v208, s[68:71], s72 offen offset:2048 nt lds\n"
    "buffer_load_dwordx4 v209, s[68:71], s72 offen offset:3072 nt lds\n"
    "s_add_u32 s72, s72, 16384\n"
    "s_add_u32 s73, s73, 16384\n"
    "s_cmp_ge_u32 s73, 98304\n"
    "s_cselect_b32 s73, 49152, s73\n"
    "s_add_u32 s74, s74, 16384\n"
    "s_cmp_ge_u32 s74, 163840\n"
    "s_cselect_b32 s74, 98304, s74\n"
    "s_mov_b64 s[88:89], %4\n"
    "s_mov_b32 s90, 8388608\n"
    "s_mov_b32 s91, 0x20000\n"
    "s_mov_b32 s92, %24\n"
    "s_mov_b32 m0, s100\n"
    "s_add_u32 m0, m0, 159744\n"
    "s_nop 0\n"
    "buffer_load_dwordx4 %25, s[88:91], s92 offen lds\n"
    "s_add_u32 s92, s92, 32768\n"
    "buffer_load_dwordx4 %25, s[88:91], s92 offen lds\n"
    "s_add_u32 s92, s92, 32768\n"
    "buffer_load_dwordx4 %25, s[88:91], s92 offen lds\n"
    "s_add_u32 s92, s92, 32768\n"
    "buffer_load_dwordx4 %25, s[88:91], s92 offen lds\n"
    "s_add_u32 s92, s92, 32768\n"
    "buffer_load_dwordx4 %25, s[88:91], s92 offen lds\n"
    "s_add_u32 s92, s92, 32768\n"
    "buffer_load_dwordx4 %25, s[88:91], s92 offen lds\n"
    "s_add_u32 s92, s92, 32768\n"
    "buffer_load_dwordx4 %25, s[88:91], s92 offen lds\n"
    "s_add_u32 s92, s92, 32768\n"
    "buffer_load_dwordx4 %25, s[88:91], s92 offen lds\n"
    "s_add_u32 s92, s92, 32768\n"
    "s_waitcnt vmcnt(26)\n"
        : "=&{v[0:31]}"(rawv), "=&{v[32:47]}"(wmv), "=&{v[48:51]}"(cvv), "=&{v[52:53]}"(ep)
        : "s"(Wq), "s"(1024u*8192u), "s"(pK), "s"(pV), "s"(offW), "s"(wdst), "v"(vWd[0]), "v"(vKp0), "v"(vKp1), "v"(vVp),
          "s"(x), "s"(wmod), "s"(cv), "s"(ws+WS_EP), "v"(vX), "v"(vX2), "v"(vWm), "v"(vC), "v"(vXl), "s"(cofs), "s"(pfo), "v"(pfv)
        : "memory", "vcc", "scc", "m0", "s36", "s37", "s38", "s39", "s40", "s41", "s42", "s43", "s44", "s45", "s46", "s47", "s48", "s49", "s50", "s51", "s52", "s53", "s54", "s55", "s56", "s57", "s58", "s59", "s60", "s61", "s62", "s63", "s64", "s65", "s66", "s67", "s68", "s69", "s70", "s71", "s72", "s73", "s74", "s75", "s76", "s77", "s78", "s79", "s80", "s81", "s82", "s83", "s84", "s85", "s86", "s87", "s88", "s89", "s90", "s91", "s92", "s93", "s94", "s95", "s96", "s97", "s98", "s99", "s100", "s101", "v60", "v61", "v200", "v201", "v202", "v203", "v204", "v205", "v206", "v207", "v208", "v209", "v210", "v211", "v212", "v213");
    }
    const unsigned long long TAG=(((unsigned long long)ep[1]<<32)|ep[0])+1ull;
    tagLo=rfl((unsigned)TAG); tagHi=rfl((unsigned)(TAG>>32));
    u32x4 raw[8], wm[4];
    #pragma unroll
    for(int j=0;j<8;j++) raw[j]=(u32x4){__float_as_uint(rawv[4*j]),__float_as_uint(rawv[4*j+1]),__float_as_uint(rawv[4*j+2]),__float_as_uint(rawv[4*j+3])};
    #pragma unroll
    for(int j=0;j<4;j++) wm[j]=(u32x4){wmv[4*j],wmv[4*j+1],wmv[4*j+2],wmv[4*j+3]};
    float* mA=(float*)(smem_+114688); char* imH=smem_+122880; char* imX=smem_+139264;
    { float a[8]={0,0,0,0,0,0,0,0};
      #pragma unroll
      for(int i=0;i<4;i++){ const float c_=__uint_as_float(cvv[i]<<16), sv=rb(c_*__builtin_amdgcn_rcpf(1.f+__expf(-c_)));
        #pragma unroll
        for(int e=0;e<4;e++){ a[2*e]+=sv*bflo(wm[i][e]); a[2*e+1]+=sv*bfhi(wm[i][e]); } }
      #pragma unroll
      for(int e=0;e<8;e++){ a[e]+=dppf<0x124>(a[e]); a[e]+=dppf<0x128>(a[e]); a[e]=sumg(a[e]); }
      if(lane<4){ *(f32x4*)(mA+w*32+lane*8)=(f32x4){a[0],a[1],a[2],a[3]}; *(f32x4*)(mA+w*32+lane*8+4)=(f32x4){a[4],a[5],a[6],a[7]}; } }
    BAR_();
    if(tid_<32){ const float v=(mA[tid_]+mA[32+tid_])+(mA[64+tid_]+mA[96+tid_]);
      const unsigned hi=((__float_as_uint(v)+0x80u)&0xffffff00u)|((unsigned)(TAG>>32)&0xffu);
      __hip_atomic_store((gu64*)(ws+WS_G)+s4*2048+cg*32+tid_, ((unsigned long long)hi<<32)|(unsigned)TAG, __ATOMIC_RELAXED, __HIP_MEMORY_SCOPE_AGENT); }

    float xv[64]; f32x2 s2[4]={{0,0},{0,0},{0,0},{0,0}};
    #pragma unroll
    for(int j=0;j<8;j++)
      #pragma unroll
      for(int e=0;e<4;e++){ xv[j*8+2*e]=bflo(raw[j][e]); xv[j*8+2*e+1]=bfhi(raw[j][e]); s2[e]+=(f32x2){xv[j*8+2*e],xv[j*8+2*e+1]}; }
    const f32x2 st=(s2[0]+s2[1])+(s2[2]+s2[3]);
    const float mean=sum16(st[0]+st[1])*(1.f/1024.f);
    f32x2 q2[4]={{0,0},{0,0},{0,0},{0,0}};
    #pragma unroll
    for(int j=0;j<8;j++)
      #pragma unroll
      for(int e=0;e<4;e++){ const f32x2 d=(f32x2){xv[j*8+2*e],xv[j*8+2*e+1]}-mean; xv[j*8+2*e]=d[0]; xv[j*8+2*e+1]=d[1]; q2[e]+=d*d; }
    const f32x2 qt=(q2[0]+q2[1])+(q2[2]+q2[3]);
    const float rstd=rsqrtf(sum16(qt[0]+qt[1])*(1.f/1024.f)+1e-5f);

    { const __amdgpu_buffer_rsrc_t rG=mkrsrc(ws+WS_G,65536u); const unsigned go=((tid_>>7)*1024+ch*512+(tid_&127)*4)*8;
      const unsigned tlo=(unsigned)TAG, thi=(unsigned)(TAG>>32); u32x4 gv[8]; unsigned spins=0;
      for(;;){
        #pragma unroll
        for(int k=0;k<8;k++) gv[k]=ld16_sc1(rG,go+(k>>1)*16384+(k&1)*16);
        unsigned bad=0;
        #pragma unroll
        for(int k=0;k<8;k++) bad|=(gv[k][0]^tlo)|(gv[k][2]^tlo)|(((gv[k][1]^thi)|(gv[k][3]^thi))&0xffu);
        if(__builtin_amdgcn_ballot_w64(bad!=0)==0ull || ++spins>(1u<<16)) break;
        __builtin_amdgcn_s_sleep(1); }
      f32x4 o;
      #pragma unroll
      for(int e=0;e<4;e++){ const int k=e>>1, c=1+2*(e&1);
        float v=(__uint_as_float(gv[k][c]&0xffffff00u)+__uint_as_float(gv[2+k][c]&0xffffff00u))+(__uint_as_float(gv[4+k][c]&0xffffff00u)+__uint_as_float(gv[6+k][c]&0xffffff00u));
        v=rb(v); o[e]=(tid_>>7)?rb(1.f+v):v; }
      *(f32x4*)(mA+128+tid_*4)=o; }
    BAR_();

    #pragma unroll
    for(int jj=0;jj<4;jj++){ const u32x4 rw=raw[jj]; const int ci=jj*16+c16, ksl=ci>>2, a=ci&3; unsigned n[4];
      const f32x4 sa0=*(const f32x4*)(mA+128+ci*8), sa1=*(const f32x4*)(mA+128+ci*8+4), sc0=*(const f32x4*)(mA+640+ci*8), sc1=*(const f32x4*)(mA+640+ci*8+4);
      const float* d=xv+jj*8;
      n[0]=cvtpk((d[0]*rstd*sc0[0]+sa0[0])*QS,(d[1]*rstd*sc0[1]+sa0[1])*QS);
      n[1]=cvtpk((d[2]*rstd*sc0[2]+sa0[2])*QS,(d[3]*rstd*sc0[3]+sa0[3])*QS);
      n[2]=cvtpk((d[4]*rstd*sc1[0]+sa1[0])*QS,(d[5]*rstd*sc1[1]+sa1[1])*QS);
      n[3]=cvtpk((d[6]*rstd*sc1[2]+sa1[2])*QS,(d[7]*rstd*sc1[3]+sa1[3])*QS);
      const int rs=r^((ksl&3)<<2), o0=((ksl*4+2*(a&1))*16+rs)*16+(a>>1)*8, o1=o0+256;
      *(u32x2*)(imH+o0)=(u32x2){n[0],n[1]}; *(u32x2*)(imH+o1)=(u32x2){n[2],n[3]};
      *(u32x2*)(imX+o0)=(u32x2){rw[0],rw[1]}; *(u32x2*)(imX+o1)=(u32x2){rw[2],rw[3]}; }
    BAR_();

    const unsigned T=row0>>6, tb=(row0>>4)&3; const __amdgpu_buffer_rsrc_t rH=mkrsrc(ws+WS_H,M_*2048u), rX=mkrsrc(ws+WS_XF,M_*2048u);
    #pragma unroll
    for(int j=0;j<4;j++){ const unsigned f=w*4+j; st16_sc1(rH,((T*32+ch*16+f)*4+tb)*1024+lane*16,*(const u32x4*)(imH+f*1024+((lane&48)+((lane&15)^((f&3)<<2)))*16)); }

    DRAIN_BAR();
    if(tid_<4) __hip_atomic_store((gu64*)(ws+WS_HF)+(tid_*8+qblk)*32+hh, TAG, __ATOMIC_RELAXED, __HIP_MEMORY_SCOPE_AGENT);

    #pragma unroll
    for(int j=0;j<4;j++){ const unsigned f=w*4+j; st16_sc1(rX,((T*32+ch*16+f)*4+tb)*1024+lane*16,*(const u32x4*)(imX+f*1024+((lane&48)+((lane&15)^((f&3)<<2)))*16)); }

  }
  unsigned needO;
  for(unsigned pass=0;;pass++){
  { PHASE() LANE() W128_RD() W128_DMA(8192u) KV_DMA()
    const unsigned T=qblk*4+w;
    const unsigned vKr0=((lane>>3)&1)*2048+(lane&7)*128+(((0+g)^((i16>>1)&7))*16), vKr1=((lane>>3)&1)*2048+(lane&7)*128+(((4+g)^((i16>>1)&7))*16);
    const unsigned offX=rfl(T*32*4096), offW=rfl(16*w*8192+hh*256), wdst=rfl(w*4096);
    const char* pK=(const char*)Kc+(size_t)hh*2048*256-1024; const char* pV=(const char*)Vc+(size_t)hh*2048*256-1024;
    if(pass){
      asm volatile(
    "s_mov_b64 s[64:65], %2\n"
    "s_mov_b32 s66, 525312\n"
    "s_mov_b32 s67, 0x20000\n"
    "s_mov_b64 s[68:69], %3\n"
    "s_mov_b32 s70, 525312\n"
    "s_mov_b32 s71, 0x20000\n"
    "s_mov_b32 s79, %5\n"
    "s_mov_b32 s72, %5\n"
    "s_mov_b32 s73, 49152\n"
    "s_mov_b32 s74, 98304\n"
    "s_mov_b32 s75, 49152\n"
    "s_mov_b32 s76, 98304\n"
    "v_add_u32 v204, 1024, %7\n"
    "v_add_u32 v205, 128, %7\n"
    "v_add_u32 v206, 1024, %8\n"
    "v_add_u32 v207, 128, %8\n"
    "v_add_u32 v208, 1024, %9\n"
    "v_add_u32 v209, 128, %9\n"
    "v_mov_b32 v200, %6\n"
    "v_add_u32 v201, 128, %6\n"
    "v_add_u32 v202, 65536, %6\n"
    "v_add_u32 v203, 65664, %6\n"
    "v_add_u32 v210, 49152, %6\n"
    "v_add_u32 v211, 49152, %6\n"
    "v_add_u32 v212, 98304, %6\n"
    "v_add_u32 v213, 98304, %6\n"
    "s_mov_b64 s[40:41], %0\n"
    "s_mov_b32 s42, %1\n"
    "s_mov_b32 s43, 0x20000\n"
    "s_mov_b32 s53, %4\n"
    "s_mov_b32 s56, %5\n"
    "s_mov_b32 s57, 0\n"
    "s_add_u32 m0, s57, s56\n"
    "s_nop 0\n"
    "buffer_load_dwordx4 v200, s[40:43], s53 offen lds\n"
    "s_add_u32 m0, m0, 1024\n"
    "s_nop 0\n"
    "buffer_load_dwordx4 v201, s[40:43], s53 offen lds\n"
    "s_add_u32 m0, m0, 1024\n"
    "s_nop 0\n"
    "buffer_load_dwordx4 v202, s[40:43], s53 offen lds\n"
    "s_add_u32 m0, m0, 1024\n"
    "s_nop 0\n"
    "buffer_load_dwordx4 v203, s[40:43], s53 offen lds\n"
    "s_add_u32 s53, s53, 524288\n"
    "s_add_u32 s57, s57, 16384\n"
    "s_cmp_ge_u32 s57, 49152\n"
    "s_cselect_b32 s57, 0, s57\n"
    "s_add_u32 m0, s57, s56\n"
    "s_nop 0\n"
    "buffer_load_dwordx4 v200, s[40:43], s53 offen lds\n"
    "s_add_u32 m0, m0, 1024\n"
    "s_nop 0\n"
    "buffer_load_dwordx4 v201, s[40:43], s53 offen lds\n"
    "s_add_u32 m0, m0, 1024\n"
    "s_nop 0\n"
    "buffer_load_dwordx4 v202, s[40:43], s53 offen lds\n"
    "s_add_u32 m0, m0, 1024\n"
    "s_nop 0\n"
    "buffer_load_dwordx4 v203, s[40:43], s53 offen lds\n"
    "s_add_u32 s53, s53, 524288\n"
    "s_add_u32 s57, s57, 16384\n"
    "s_cmp_ge_u32 s57, 49152\n"
    "s_cselect_b32 s57, 0, s57\n"
    "s_add_u32 m0, s57, s56\n"
    "s_nop 0\n"
    "buffer_load_dwordx4 v200, s[40:43], s53 offen lds\n"
    "s_add_u32 m0, m0, 1024\n"
    "s_nop 0\n"
    "buffer_load_dwordx4 v201, s[40:43], s53 offen lds\n"
    "s_add_u32 m0, s73, s79\n"
    "s_nop 0\n"
    "buffer_load_dwordx4 v204, s[64:67], s72 offen nt lds\n"
    "buffer_load_dwordx4 v205, s[64:67], s72 offen offset:1024 nt lds\n"
    "buffer_load_dwordx4 v206, s[64:67], s72 offen offset:2048 nt lds\n"
    "buffer_load_dwordx4 v207, s[64:67], s72 offen offset:3072 nt lds\n"
    "s_add_u32 m0, s74, s79\n"
    "s_nop 0\n"
    "buffer_load_dwordx4 v208, s[68:71], s72 offen nt lds\n"
    "buffer_load_dwordx4 v209, s[68:71], s72 offen offset:1024 nt lds\n"
    "buffer_load_dwordx4 v208, s[68:71], s72 offen offset:2048 nt lds\n"
    "buffer_load_dwordx4 v209, s[68:71], s72 offen offset:3072 nt lds\n"
    "s_add_u32 s72, s72, 16384\n"
    "s_add_u32 s73, s73, 16384\n"
    "s_cmp_ge_u32 s73, 98304\n"
    "s_cselect_b32 s73, 49152, s73\n"
    "s_add_u32 s74, s74, 16384\n"
    "s_cmp_ge_u32 s74, 163840\n"
    "s_cselect_b32 s74, 98304, s74\n"
        :: "s"(Wq), "s"(1024u*8192u), "s"(pK), "s"(pV), "s"(offW), "s"(wdst), "v"(vWd[0]), "v"(vKp0), "v"(vKp1), "v"(vVp)
        : "memory", "vcc", "scc", "m0", "s36", "s37", "s38", "s39", "s40", "s41", "s42", "s43", "s44", "s45", "s46", "s47", "s48", "s49", "s50", "s51", "s52", "s53", "s54", "s55", "s56", "s57", "s58", "s59", "s60", "s61", "s62", "s63", "s64", "s65", "s66", "s67", "s68", "s69", "s70", "s71", "s72", "s73", "s74", "s75", "s76", "s77", "s78", "s79", "s80", "s81", "s82", "s83", "s84", "s85", "s86", "s87", "s88", "s89", "s90", "s91", "s92", "s93", "s94", "s95", "s96", "s97", "s98", "s99", "v200", "v201", "v202", "v203", "v204", "v205", "v206", "v207", "v208", "v209", "v210", "v211", "v212", "v213");
    }
    const unsigned gw=bid_*4+w, pm=gw>>8, pi=gw&255, pn=pm==0?32u:28u;
    const char* pPF=(const char*)(pm==0?Wo:pm==1?Wg:pm==2?Wu:Wd)+(size_t)pi*pn*1024; const unsigned szPF=rfl(pn*1024);
    const unsigned mofs=(loc&1)*512*12288+4096+(xcd*16+(loc>>1))*64;
    const char* pMw=(const char*)wmod+mofs; const unsigned szMw=rfl(1024*12288-mofs), vMw2=(w*16+(lane>>2))*8*12288+(lane&3)*16;
    f32x8 ls; const unsigned cofs=rfl(((loc*4+w+96)&127)*1024), mode=rfl(pass);
    const unsigned vWq0=(g>>1)*2048+(4*(g&1)+q)*64+((p^(2*(g&1)))*16);
    const char* pHF=ws+WS_HF+((hh&3)*8+qblk)*256;
    const unsigned vOb=((T*128+hh*4)*4*64+lane)*16, vC8=((loc&1)*512+(w*16+(lane>>2))*8)*2; u32x4 c8; unsigned cO0;
    asm volatile(
    "s_mov_b64 s[64:65], %5\n"
    "s_mov_b32 s66, 525312\n"
    "s_mov_b32 s67, 0x20000\n"
    "s_mov_b64 s[68:69], %6\n"
    "s_mov_b32 s70, 525312\n"
    "s_mov_b32 s71, 0x20000\n"
    "s_mov_b64 s[84:85], %19\n"
    "s_mov_b32 s86, %20\n"
    "s_mov_b32 s87, 0x20000\n"
    "s_mov_b32 s81, 0\n"
    "s_getpc_b64 s[88:89]\n"
    "L_pc4:\n"
    "s_sub_u32 s88, s88, L_pc4-L_mega_code\n"
    "s_subb_u32 s89, s89, 0\n"
    "s_mov_b32 s90, L_text_end-L_mega_code\n"
    "s_mov_b32 s91, 0x20000\n"
    "s_mov_b32 s91, %21\n"
    "s_mov_b32 s79, %9\n"
    "s_mov_b32 s72, %9\n"
    "s_mov_b32 s73, 49152\n"
    "s_mov_b32 s74, 98304\n"
    "s_mov_b32 s75, 49152\n"
    "s_mov_b32 s76, 98304\n"
    "v_add_u32 v204, 1024, %16\n"
    "v_add_u32 v205, 128, %16\n"
    "v_add_u32 v206, 1024, %17\n"
    "v_add_u32 v207, 128, %17\n"
    "v_add_u32 v208, 1024, %18\n"
    "v_add_u32 v209, 128, %18\n"
    "v_add_u32 v215, 8, %34\n"
    "v_mov_b32 v200, %13\n"
    "v_add_u32 v201, 128, %13\n"
    "v_add_u32 v202, 65536, %13\n"
    "v_add_u32 v203, 65664, %13\n"
    "v_add_u32 v210, 49152, %14\n"
    "v_add_u32 v211, 49152, %15\n"
    "v_add_u32 v212, 98304, %11\n"
    "v_add_u32 v213, 98304, %12\n"
    "s_mov_b64 s[36:37], %3\n"
    "s_mov_b32 s38, 4194304\n"
    "s_mov_b32 s39, 0x20000\n"
    "s_mov_b64 s[40:41], %4\n"
    "s_mov_b32 s42, 8388608\n"
    "s_mov_b32 s43, 0x20000\n"
    "s_mov_b32 s52, %7\n"
    "s_mov_b32 s53, %8\n"
    "s_mov_b32 s56, %9\n"
    "s_mov_b32 s57, 0\n"
    "s_mov_b32 s58, 0\n"
    "v_add_u32 v192, 0, %34\n"
    "v_add_u32 v193, 0, v215\n"
    "v_mov_b32 v0, 0\n"
    "v_mov_b32 v1, 0\n"
    "v_mov_b32 v2, 0\n"
    "v_mov_b32 v3, 0\n"
    "v_mov_b32 v4, 0\n"
    "v_mov_b32 v5, 0\n"
    "v_mov_b32 v6, 0\n"
    "v_mov_b32 v7, 0\n"
    "v_mov_b32 v8, 0\n"
    "v_mov_b32 v9, 0\n"
    "v_mov_b32 v10, 0\n"
    "v_mov_b32 v11, 0\n"
    "v_mov_b32 v12, 0\n"
    "v_mov_b32 v13, 0\n"
    "v_mov_b32 v14, 0\n"
    "v_mov_b32 v15, 0\n"
    "v_mov_b32 v16, 0\n"
    "v_mov_b32 v17, 0\n"
    "v_mov_b32 v18, 0\n"
    "v_mov_b32 v19, 0\n"
    "v_mov_b32 v20, 0\n"
    "v_mov_b32 v21, 0\n"
    "v_mov_b32 v22, 0\n"
    "v_mov_b32 v23, 0\n"
    "v_mov_b32 v24, 0\n"
    "v_mov_b32 v25, 0\n"
    "v_mov_b32 v26, 0\n"
    "v_mov_b32 v27, 0\n"
    "v_mov_b32 v28, 0\n"
    "v_mov_b32 v29, 0\n"
    "v_mov_b32 v30, 0\n"
    "v_mov_b32 v31, 0\n"
    "v_mov_b32 v32, 0\n"
    "v_mov_b32 v33, 0\n"
    "v_mov_b32 v34, 0\n"
    "v_mov_b32 v35, 0\n"
    "v_mov_b32 v36, 0\n"
    "v_mov_b32 v37, 0\n"
    "v_mov_b32 v38, 0\n"
    "v_mov_b32 v39, 0\n"
    "v_mov_b32 v40, 0\n"
    "v_mov_b32 v41, 0\n"
    "v_mov_b32 v42, 0\n"
    "v_mov_b32 v43, 0\n"
    "v_mov_b32 v44, 0\n"
    "v_mov_b32 v45, 0\n"
    "v_mov_b32 v46, 0\n"
    "v_mov_b32 v47, 0\n"
    "v_mov_b32 v48, 0\n"
    "v_mov_b32 v49, 0\n"
    "v_mov_b32 v50, 0\n"
    "v_mov_b32 v51, 0\n"
    "v_mov_b32 v52, 0\n"
    "v_mov_b32 v53, 0\n"
    "v_mov_b32 v54, 0\n"
    "v_mov_b32 v55, 0\n"
    "v_mov_b32 v56, 0\n"
    "v_mov_b32 v57, 0\n"
    "v_mov_b32 v58, 0\n"
    "v_mov_b32 v59, 0\n"
    "v_mov_b32 v60, 0\n"
    "v_mov_b32 v61, 0\n"
    "v_mov_b32 v62, 0\n"
    "v_mov_b32 v63, 0\n"
    "v_mov_b32 v64, 0\n"
    "v_mov_b32 v65, 0\n"
    "v_mov_b32 v66, 0\n"
    "v_mov_b32 v67, 0\n"
    "v_mov_b32 v68, 0\n"
    "v_mov_b32 v69, 0\n"
    "v_mov_b32 v70, 0\n"
    "v_mov_b32 v71, 0\n"
    "v_mov_b32 v72, 0\n"
    "v_mov_b32 v73, 0\n"
    "v_mov_b32 v74, 0\n"
    "v_mov_b32 v75, 0\n"
    "v_mov_b32 v76, 0\n"
    "v_mov_b32 v77, 0\n"
    "v_mov_b32 v78, 0\n"
    "v_mov_b32 v79, 0\n"
    "v_mov_b32 v80, 0\n"
    "v_mov_b32 v81, 0\n"
    "v_mov_b32 v82, 0\n"
    "v_mov_b32 v83, 0\n"
    "v_mov_b32 v84, 0\n"
    "v_mov_b32 v85, 0\n"
    "v_mov_b32 v86, 0\n"
    "v_mov_b32 v87, 0\n"
    "v_mov_b32 v88, 0\n"
    "v_mov_b32 v89, 0\n"
    "v_mov_b32 v90, 0\n"
    "v_mov_b32 v91, 0\n"
    "v_mov_b32 v92, 0\n"
    "v_mov_b32 v93, 0\n"
    "v_mov_b32 v94, 0\n"
    "v_mov_b32 v95, 0\n"
    "v_mov_b32 v96, 0\n"
    "v_mov_b32 v97, 0\n"
    "v_mov_b32 v98, 0\n"
    "v_mov_b32 v99, 0\n"
    "v_mov_b32 v100, 0\n"
    "v_mov_b32 v101, 0\n"
    "v_mov_b32 v102, 0\n"
    "v_mov_b32 v103, 0\n"
    "v_mov_b32 v104, 0\n"
    "v_mov_b32 v105, 0\n"
    "v_mov_b32 v106, 0\n"
    "v_mov_b32 v107, 0\n"
    "v_mov_b32 v108, 0\n"
    "v_mov_b32 v109, 0\n"
    "v_mov_b32 v110, 0\n"
    "v_mov_b32 v111, 0\n"
    "v_mov_b32 v112, 0\n"
    "v_mov_b32 v113, 0\n"
    "v_mov_b32 v114, 0\n"
    "v_mov_b32 v115, 0\n"
    "v_mov_b32 v116, 0\n"
    "v_mov_b32 v117, 0\n"
    "v_mov_b32 v118, 0\n"
    "v_mov_b32 v119, 0\n"
    "v_mov_b32 v120, 0\n"
    "v_mov_b32 v121, 0\n"
    "v_mov_b32 v122, 0\n"
    "v_mov_b32 v123, 0\n"
    "v_mov_b32 v124, 0\n"
    "v_mov_b32 v125, 0\n"
    "v_mov_b32 v126, 0\n"
    "v_mov_b32 v127, 0\n"
    "v_accvgpr_write_b32 a0, 0\n"
    "v_accvgpr_write_b32 a1, 0\n"
    "v_accvgpr_write_b32 a2, 0\n"
    "v_accvgpr_write_b32 a3, 0\n"
    "v_accvgpr_write_b32 a4, 0\n"
    "v_accvgpr_write_b32 a5, 0\n"
    "v_accvgpr_write_b32 a6, 0\n"
    "v_accvgpr_write_b32 a7, 0\n"
    "v_accvgpr_write_b32 a8, 0\n"
    "v_accvgpr_write_b32 a9, 0\n"
    "v_accvgpr_write_b32 a10, 0\n"
    "v_accvgpr_write_b32 a11, 0\n"
    "v_accvgpr_write_b32 a12, 0\n"
    "v_accvgpr_write_b32 a13, 0\n"
    "v_accvgpr_write_b32 a14, 0\n"
    "v_accvgpr_write_b32 a15, 0\n"
    "v_accvgpr_write_b32 a16, 0\n"
    "v_accvgpr_write_b32 a17, 0\n"
    "v_accvgpr_write_b32 a18, 0\n"
    "v_accvgpr_write_b32 a19, 0\n"
    "v_accvgpr_write_b32 a20, 0\n"
    "v_accvgpr_write_b32 a21, 0\n"
    "v_accvgpr_write_b32 a22, 0\n"
    "v_accvgpr_write_b32 a23, 0\n"
    "v_accvgpr_write_b32 a24, 0\n"
    "v_accvgpr_write_b32 a25, 0\n"
    "v_accvgpr_write_b32 a26, 0\n"
    "v_accvgpr_write_b32 a27, 0\n"
    "v_accvgpr_write_b32 a28, 0\n"
    "v_accvgpr_write_b32 a29, 0\n"
    "v_accvgpr_write_b32 a30, 0\n"
    "v_accvgpr_write_b32 a31, 0\n"
    "v_accvgpr_write_b32 a32, 0\n"
    "v_accvgpr_write_b32 a33, 0\n"
    "v_accvgpr_write_b32 a34, 0\n"
    "v_accvgpr_write_b32 a35, 0\n"
    "v_accvgpr_write_b32 a36, 0\n"
    "v_accvgpr_write_b32 a37, 0\n"
    "v_accvgpr_write_b32 a38, 0\n"
    "v_accvgpr_write_b32 a39, 0\n"
    "v_accvgpr_write_b32 a40, 0\n"
    "v_accvgpr_write_b32 a41, 0\n"
    "v_accvgpr_write_b32 a42, 0\n"
    "v_accvgpr_write_b32 a43, 0\n"
    "v_accvgpr_write_b32 a44, 0\n"
    "v_accvgpr_write_b32 a45, 0\n"
    "v_accvgpr_write_b32 a46, 0\n"
    "v_accvgpr_write_b32 a47, 0\n"
    "v_accvgpr_write_b32 a48, 0\n"
    "v_accvgpr_write_b32 a49, 0\n"
    "v_accvgpr_write_b32 a50, 0\n"
    "v_accvgpr_write_b32 a51, 0\n"
    "v_accvgpr_write_b32 a52, 0\n"
    "v_accvgpr_write_b32 a53, 0\n"
    "v_accvgpr_write_b32 a54, 0\n"
    "v_accvgpr_write_b32 a55, 0\n"
    "v_accvgpr_write_b32 a56, 0\n"
    "v_accvgpr_write_b32 a57, 0\n"
    "v_accvgpr_write_b32 a58, 0\n"
    "v_accvgpr_write_b32 a59, 0\n"
    "v_accvgpr_write_b32 a60, 0\n"
    "v_accvgpr_write_b32 a61, 0\n"
    "v_accvgpr_write_b32 a62, 0\n"
    "v_accvgpr_write_b32 a63, 0\n"
    "v_accvgpr_write_b32 a64, 0\n"
    "v_accvgpr_write_b32 a65, 0\n"
    "v_accvgpr_write_b32 a66, 0\n"
    "v_accvgpr_write_b32 a67, 0\n"
    "v_accvgpr_write_b32 a68, 0\n"
    "v_accvgpr_write_b32 a69, 0\n"
    "v_accvgpr_write_b32 a70, 0\n"
    "v_accvgpr_write_b32 a71, 0\n"
    "v_accvgpr_write_b32 a72, 0\n"
    "v_accvgpr_write_b32 a73, 0\n"
    "v_accvgpr_write_b32 a74, 0\n"
    "v_accvgpr_write_b32 a75, 0\n"
    "v_accvgpr_write_b32 a76, 0\n"
    "v_accvgpr_write_b32 a77, 0\n"
    "v_accvgpr_write_b32 a78, 0\n"
    "v_accvgpr_write_b32 a79, 0\n"
    "v_accvgpr_write_b32 a80, 0\n"
    "v_accvgpr_write_b32 a81, 0\n"
    "v_accvgpr_write_b32 a82, 0\n"
    "v_accvgpr_write_b32 a83, 0\n"
    "v_accvgpr_write_b32 a84, 0\n"
    "v_accvgpr_write_b32 a85, 0\n"
    "v_accvgpr_write_b32 a86, 0\n"
    "v_accvgpr_write_b32 a87, 0\n"
    "v_accvgpr_write_b32 a88, 0\n"
    "v_accvgpr_write_b32 a89, 0\n"
    "v_accvgpr_write_b32 a90, 0\n"
    "v_accvgpr_write_b32 a91, 0\n"
    "v_accvgpr_write_b32 a92, 0\n"
    "v_accvgpr_write_b32 a93, 0\n"
    "v_accvgpr_write_b32 a94, 0\n"
    "v_accvgpr_write_b32 a95, 0\n"
    "v_accvgpr_write_b32 a96, 0\n"
    "v_accvgpr_write_b32 a97, 0\n"
    "v_accvgpr_write_b32 a98, 0\n"
    "v_accvgpr_write_b32 a99, 0\n"
    "v_accvgpr_write_b32 a100, 0\n"
    "v_accvgpr_write_b32 a101, 0\n"
    "v_accvgpr_write_b32 a102, 0\n"
    "v_accvgpr_write_b32 a103, 0\n"
    "v_accvgpr_write_b32 a104, 0\n"
    "v_accvgpr_write_b32 a105, 0\n"
    "v_accvgpr_write_b32 a106, 0\n"
    "v_accvgpr_write_b32 a107, 0\n"
    "v_accvgpr_write_b32 a108, 0\n"
    "v_accvgpr_write_b32 a109, 0\n"
    "v_accvgpr_write_b32 a110, 0\n"
    "v_accvgpr_write_b32 a111, 0\n"
    "v_accvgpr_write_b32 a112, 0\n"
    "v_accvgpr_write_b32 a113, 0\n"
    "v_accvgpr_write_b32 a114, 0\n"
    "v_accvgpr_write_b32 a115, 0\n"
    "v_accvgpr_write_b32 a116, 0\n"
    "v_accvgpr_write_b32 a117, 0\n"
    "v_accvgpr_write_b32 a118, 0\n"
    "v_accvgpr_write_b32 a119, 0\n"
    "v_accvgpr_write_b32 a120, 0\n"
    "v_accvgpr_write_b32 a121, 0\n"
    "v_accvgpr_write_b32 a122, 0\n"
    "v_accvgpr_write_b32 a123, 0\n"
    "v_accvgpr_write_b32 a124, 0\n"
    "v_accvgpr_write_b32 a125, 0\n"
    "v_accvgpr_write_b32 a126, 0\n"
    "v_accvgpr_write_b32 a127, 0\n"
    "s_cmp_lg_u32 %9, 0\n"
    "s_cbranch_scc1 L_h_done\n"
    "s_mov_b64 s[92:93], %31\n"
    "s_mov_b32 s94, 256\n"
    "s_mov_b32 s95, 0x20000\n"
    "s_mov_b32 s63, 131072\n"
    "v_lshrrev_b32 v214, 1, %10\n"
    "v_and_b32 v214, 0xf8, v214\n"
    "L_h_poll:\n"
    "buffer_load_dwordx2 v[216:217], v214, s[92:95], 0 offen sc1\n"
    "s_waitcnt vmcnt(0)\n"
    "v_cmp_ne_u32 vcc, %32, v216\n"
    "v_cmp_ne_u32_e64 s[96:97], %33, v217\n"
    "s_nop 1\n"
    "s_or_b64 vcc, vcc, s[96:97]\n"
    "s_cbranch_vccz L_h_done\n"
    "s_sleep 1\n"
    "s_sub_u32 s63, s63, 1\n"
    "s_cmp_lg_u32 s63, 0\n"
    "s_cbranch_scc1 L_h_poll\n"
    "L_h_done:\n"
    "s_barrier\n"
    "s_add_u32 m0, s57, s56\n"
    "s_add_u32 m0, m0, 1024\n"
    "s_add_u32 m0, m0, 1024\n"
    "s_add_u32 m0, m0, 1024\n"
    "s_add_u32 s53, s53, 524288\n"
    "s_add_u32 s57, s57, 16384\n"
    "s_cmp_ge_u32 s57, 49152\n"
    "s_cselect_b32 s57, 0, s57\n"
    "buffer_load_dwordx4 a[192:195], %10, s[36:39], s52 offen sc1\n"
    "buffer_load_dwordx4 a[196:199], %10, s[36:39], s52 offen offset:1024 sc1\n"
    "buffer_load_dwordx4 a[200:203], %10, s[36:39], s52 offen offset:2048 sc1\n"
    "buffer_load_dwordx4 a[204:207], %10, s[36:39], s52 offen offset:3072 sc1\n"
    "s_add_u32 s52, s52, 4096\n"
    "buffer_load_dwordx4 a[208:211], %10, s[36:39], s52 offen sc1\n"
    "buffer_load_dwordx4 a[212:215], %10, s[36:39], s52 offen offset:1024 sc1\n"
    "buffer_load_dwordx4 a[216:219], %10, s[36:39], s52 offen offset:2048 sc1\n"
    "buffer_load_dwordx4 a[220:223], %10, s[36:39], s52 offen offset:3072 sc1\n"
    "s_add_u32 s52, s52, 4096\n"
    "s_add_u32 m0, s73, s79\n"
    "s_add_u32 m0, s74, s79\n"
    "s_add_u32 s72, s72, 16384\n"
    "s_add_u32 s73, s73, 16384\n"
    "s_cmp_ge_u32 s73, 98304\n"
    "s_cselect_b32 s73, 49152, s73\n"
    "s_add_u32 s74, s74, 16384\n"
    "s_cmp_ge_u32 s74, 163840\n"
    "s_cselect_b32 s74, 98304, s74\n"
    "s_add_u32 m0, s57, s56\n"
    "s_add_u32 m0, m0, 1024\n"
    "s_add_u32 m0, m0, 1024\n"
    "s_add_u32 m0, m0, 1024\n"
    "s_add_u32 s53, s53, 524288\n"
    "s_add_u32 s57, s57, 16384\n"
    "s_cmp_ge_u32 s57, 49152\n"
    "s_cselect_b32 s57, 0, s57\n"
    "buffer_load_dwordx4 a[224:227], %10, s[36:39], s52 offen sc1\n"
    "buffer_load_dwordx4 a[228:231], %10, s[36:39], s52 offen offset:1024 sc1\n"
    "buffer_load_dwordx4 a[232:235], %10, s[36:39], s52 offen offset:2048 sc1\n"
    "buffer_load_dwordx4 a[236:239], %10, s[36:39], s52 offen offset:3072 sc1\n"
    "s_add_u32 s52, s52, 4096\n"
    "s_add_u32 m0, s57, s56\n"
    "s_add_u32 m0, m0, 1024\n"
    "s_mov_b32 s60, 14\n"
    "s_mov_b32 s59, 30\n"
    "s_mov_b32 s55, 8\n"
    "s_waitcnt vmcnt(8)\n"
    "s_barrier\n"
    "ds_read_b64_tr_b16 a[128:129], v192 offset:0\n"
    "ds_read_b64_tr_b16 a[130:131], v192 offset:4096\n"
    "ds_read_b64_tr_b16 a[132:133], v193 offset:0\n"
    "ds_read_b64_tr_b16 a[134:135], v193 offset:4096\n"
    "ds_read_b64_tr_b16 a[136:137], v192 offset:512\n"
    "ds_read_b64_tr_b16 a[138:139], v192 offset:4608\n"
    "ds_read_b64_tr_b16 a[140:141], v193 offset:512\n"
    "ds_read_b64_tr_b16 a[142:143], v193 offset:4608\n"
    "ds_read_b64_tr_b16 a[144:145], v192 offset:1024\n"
    "ds_read_b64_tr_b16 a[146:147], v192 offset:5120\n"
    "ds_read_b64_tr_b16 a[148:149], v193 offset:1024\n"
    "ds_read_b64_tr_b16 a[150:151], v193 offset:5120\n"
    "ds_read_b64_tr_b16 a[152:153], v192 offset:1536\n"
    "ds_read_b64_tr_b16 a[154:155], v192 offset:5632\n"
    "ds_read_b64_tr_b16 a[156:157], v193 offset:1536\n"
    "ds_read_b64_tr_b16 a[158:159], v193 offset:5632\n"
    "s_waitcnt lgkmcnt(0)\n"
    ".p2align 6\n"
    "L_qp_loop:\n"
    "v_mfma_f32_16x16x32_bf16 v[0:3], a[128:131], a[192:195], v[0:3]\n"
    "s_sub_u32 s59, s59, 1\n"
    "ds_read_b64_tr_b16 a[160:161], v192 offset:8192\n"
    "v_mfma_f32_16x16x32_bf16 v[4:7], a[128:131], a[196:199], v[4:7]\n"
    "s_cmp_gt_i32 s59, 0\n"
    "ds_read_b64_tr_b16 a[162:163], v192 offset:12288\n"
    "v_mfma_f32_16x16x32_bf16 v[8:11], a[128:131], a[200:203], v[8:11]\n"
    "s_cselect_b32 s38, s38, 0\n"
    "ds_read_b64_tr_b16 a[164:165], v193 offset:8192\n"
    "v_mfma_f32_16x16x32_bf16 v[12:15], a[128:131], a[204:207], v[12:15]\n"
    "ds_read_b64_tr_b16 a[166:167], v193 offset:12288\n"
    "v_mfma_f32_16x16x32_bf16 v[28:31], a[132:135], a[204:207], v[28:31]\n"
    "buffer_load_dwordx4 a[240:243], %10, s[36:39], s52 offen sc1\n"
    "v_mfma_f32_16x16x32_bf16 v[24:27], a[132:135], a[200:203], v[24:27]\n"
    "ds_read_b64_tr_b16 a[168:169], v192 offset:8704\n"
    "v_mfma_f32_16x16x32_bf16 v[20:23], a[132:135], a[196:199], v[20:23]\n"
    "ds_read_b64_tr_b16 a[170:171], v192 offset:12800\n"
    "v_mfma_f32_16x16x32_bf16 v[16:19], a[132:135], a[192:195], v[16:19]\n"
    "s_add_u32 m0, m0, 1024\n"
    "ds_read_b64_tr_b16 a[172:173], v193 offset:8704\n"
    "v_mfma_f32_16x16x32_bf16 v[32:35], a[136:139], a[192:195], v[32:35]\n"
    "buffer_load_dwordx4 v202, s[40:43], s53 offen lds\n"
    "v_mfma_f32_16x16x32_bf16 v[36:39], a[136:139], a[196:199], v[36:39]\n"
    "ds_read_b64_tr_b16 a[174:175], v193 offset:12800\n"
    "v_mfma_f32_16x16x32_bf16 v[40:43], a[136:139], a[200:203], v[40:43]\n"
    "ds_read_b64_tr_b16 a[176:177], v192 offset:9216\n"
    "v_mfma_f32_16x16x32_bf16 v[44:47], a[136:139], a[204:207], v[44:47]\n"
    "ds_read_b64_tr_b16 a[178:179], v192 offset:13312\n"
    "v_mfma_f32_16x16x32_bf16 v[60:63], a[140:143], a[204:207], v[60:63]\n"
    "buffer_load_dwordx4 a[244:247], %10, s[36:39], s52 offen offset:1024 sc1\n"
    "v_mfma_f32_16x16x32_bf16 v[56:59], a[140:143], a[200:203], v[56:59]\n"
    "ds_read_b64_tr_b16 a[180:181], v193 offset:9216\n"
    "v_mfma_f32_16x16x32_bf16 v[52:55], a[140:143], a[196:199], v[52:55]\n"
    "ds_read_b64_tr_b16 a[182:183], v193 offset:13312\n"
    "v_mfma_f32_16x16x32_bf16 v[48:51], a[140:143], a[192:195], v[48:51]\n"
    "s_add_u32 m0, m0, 1024\n"
    "ds_read_b64_tr_b16 a[184:185], v192 offset:9728\n"
    "v_mfma_f32_16x16x32_bf16 v[64:67], a[144:147], a[192:195], v[64:67]\n"
    "buffer_load_dwordx4 v203, s[40:43], s53 offen lds\n"
    "v_mfma_f32_16x16x32_bf16 v[68:71], a[144:147], a[196:199], v[68:71]\n"
    "s_add_u32 s53, s53, 524288\n"
    "ds_read_b64_tr_b16 a[186:187], v192 offset:13824\n"
    "v_mfma_f32_16x16x32_bf16 v[72:75], a[144:147], a[200:203], v[72:75]\n"
    "s_add_u32 s57, s57, 16384\n"
    "ds_read_b64_tr_b16 a[188:189], v193 offset:9728\n"
    "v_mfma_f32_16x16x32_bf16 v[76:79], a[144:147], a[204:207], v[76:79]\n"
    "s_cmp_ge_u32 s57, 49152\n"
    "ds_read_b64_tr_b16 a[190:191], v193 offset:13824\n"
    "v_mfma_f32_16x16x32_bf16 v[92:95], a[148:151], a[204:207], v[92:95]\n"
    "s_cselect_b32 s57, 0, s57\n"
    "buffer_load_dwordx4 a[248:251], %10, s[36:39], s52 offen offset:2048 sc1\n"
    "v_mfma_f32_16x16x32_bf16 v[88:91], a[148:151], a[200:203], v[88:91]\n"
    "s_add_u32 s58, s58, 16384\n"
    "v_mfma_f32_16x16x32_bf16 v[84:87], a[148:151], a[196:199], v[84:87]\n"
    "s_cmp_ge_u32 s58, 49152\n"
    "v_mfma_f32_16x16x32_bf16 v[80:83], a[148:151], a[192:195], v[80:83]\n"
    "s_cselect_b32 s58, 0, s58\n"
    "v_mfma_f32_16x16x32_bf16 v[96:99], a[152:155], a[192:195], v[96:99]\n"
    "buffer_load_dwordx4 a[252:255], %10, s[36:39], s52 offen offset:3072 sc1\n"
    "v_add_u32 v192, s58, %34\n"
    "v_add_u32 v193, s58, v215\n"
    "v_mfma_f32_16x16x32_bf16 v[100:103], a[152:155], a[196:199], v[100:103]\n"
    "s_add_u32 s52, s52, 4096\n"
    "v_mfma_f32_16x16x32_bf16 v[104:107], a[152:155], a[200:203], v[104:107]\n"
    "v_mfma_f32_16x16x32_bf16 v[108:111], a[152:155], a[204:207], v[108:111]\n"
    "v_mfma_f32_16x16x32_bf16 v[124:127], a[156:159], a[204:207], v[124:127]\n"
    "v_mfma_f32_16x16x32_bf16 v[120:123], a[156:159], a[200:203], v[120:123]\n"
    "v_mfma_f32_16x16x32_bf16 v[116:119], a[156:159], a[196:199], v[116:119]\n"
    "v_mfma_f32_16x16x32_bf16 v[112:115], a[156:159], a[192:195], v[112:115]\n"
    "s_waitcnt vmcnt(10) lgkmcnt(0)\n"
    "v_mfma_f32_16x16x32_bf16 v[0:3], a[160:163], a[208:211], v[0:3]\n"
    "s_barrier\n"
    "v_mfma_f32_16x16x32_bf16 v[4:7], a[160:163], a[212:215], v[4:7]\n"
    "s_sub_u32 s59, s59, 1\n"
    "ds_read_b64_tr_b16 a[128:129], v192 offset:0\n"
    "v_mfma_f32_16x16x32_bf16 v[8:11], a[160:163], a[216:219], v[8:11]\n"
    "s_cmp_gt_i32 s59, 0\n"
    "ds_read_b64_tr_b16 a[130:131], v192 offset:4096\n"
    "v_mfma_f32_16x16x32_bf16 v[12:15], a[160:163], a[220:223], v[12:15]\n"
    "s_cselect_b32 s38, s38, 0\n"
    "ds_read_b64_tr_b16 a[132:133], v193 offset:0\n"
    "v_mfma_f32_16x16x32_bf16 v[28:31], a[164:167], a[220:223], v[28:31]\n"
    "s_sub_u32 s60, s60, 1\n"
    "ds_read_b64_tr_b16 a[134:135], v193 offset:4096\n"
    "v_mfma_f32_16x16x32_bf16 v[24:27], a[164:167], a[216:219], v[24:27]\n"
    "s_cmp_gt_i32 s60, 0\n"
    "ds_read_b64_tr_b16 a[136:137], v192 offset:512\n"
    "v_mfma_f32_16x16x32_bf16 v[20:23], a[164:167], a[212:215], v[20:23]\n"
    "s_cselect_b32 s42, s42, 0\n"
    "ds_read_b64_tr_b16 a[138:139], v192 offset:4608\n"
    "v_mfma_f32_16x16x32_bf16 v[16:19], a[164:167], a[208:211], v[16:19]\n"
    "ds_read_b64_tr_b16 a[140:141], v193 offset:512\n"
    "v_mfma_f32_16x16x32_bf16 v[32:35], a[168:171], a[208:211], v[32:35]\n"
    "buffer_load_dwordx4 a[192:195], %10, s[36:39], s52 offen sc1\n"
    "v_mfma_f32_16x16x32_bf16 v[36:39], a[168:171], a[212:215], v[36:39]\n"
    "ds_read_b64_tr_b16 a[142:143], v193 offset:4608\n"
    "v_mfma_f32_16x16x32_bf16 v[40:43], a[168:171], a[216:219], v[40:43]\n"
    "s_add_u32 m0, s57, s56\n"
    "ds_read_b64_tr_b16 a[144:145], v192 offset:1024\n"
    "v_mfma_f32_16x16x32_bf16 v[44:47], a[168:171], a[220:223], v[44:47]\n"
    "buffer_load_dwordx4 v200, s[40:43], s53 offen lds\n"
    "v_mfma_f32_16x16x32_bf16 v[60:63], a[172:175], a[220:223], v[60:63]\n"
    "ds_read_b64_tr_b16 a[146:147], v192 offset:5120\n"
    "v_mfma_f32_16x16x32_bf16 v[56:59], a[172:175], a[216:219], v[56:59]\n"
    "ds_read_b64_tr_b16 a[148:149], v193 offset:1024\n"
    "v_mfma_f32_16x16x32_bf16 v[52:55], a[172:175], a[212:215], v[52:55]\n"
    "buffer_load_dwordx4 a[196:199], %10, s[36:39], s52 offen offset:1024 sc1\n"
    "v_mfma_f32_16x16x32_bf16 v[48:51], a[172:175], a[208:211], v[48:51]\n"
    "ds_read_b64_tr_b16 a[150:151], v193 offset:5120\n"
    "v_mfma_f32_16x16x32_bf16 v[64:67], a[176:179], a[208:211], v[64:67]\n"
    "s_add_u32 m0, m0, 1024\n"
    "ds_read_b64_tr_b16 a[152:153], v192 offset:1536\n"
    "v_mfma_f32_16x16x32_bf16 v[68:71], a[176:179], a[212:215], v[68:71]\n"
    "buffer_load_dwordx4 v201, s[40:43], s53 offen lds\n"
    "v_mfma_f32_16x16x32_bf16 v[72:75], a[176:179], a[216:219], v[72:75]\n"
    "ds_read_b64_tr_b16 a[154:155], v192 offset:5632\n"
    "v_mfma_f32_16x16x32_bf16 v[76:79], a[176:179], a[220:223], v[76:79]\n"
    "ds_read_b64_tr_b16 a[156:157], v193 offset:1536\n"
    "v_mfma_f32_16x16x32_bf16 v[92:95], a[180:183], a[220:223], v[92:95]\n"
    "buffer_load_dwordx4 a[200:203], %10, s[36:39], s52 offen offset:2048 sc1\n"
    "v_mfma_f32_16x16x32_bf16 v[88:91], a[180:183], a[216:219], v[88:91]\n"
    "ds_read_b64_tr_b16 a[158:159], v193 offset:5632\n"
    "v_mfma_f32_16x16x32_bf16 v[84:87], a[180:183], a[212:215], v[84:87]\n"
    "v_mfma_f32_16x16x32_bf16 v[80:83], a[180:183], a[208:211], v[80:83]\n"
    "buffer_load_dwordx4 a[204:207], %10, s[36:39], s52 offen offset:3072 sc1\n"
    "v_mfma_f32_16x16x32_bf16 v[96:99], a[184:187], a[208:211], v[96:99]\n"
    "s_add_u32 s52, s52, 4096\n"
    "v_mfma_f32_16x16x32_bf16 v[100:103], a[184:187], a[212:215], v[100:103]\n"
    "v_mfma_f32_16x16x32_bf16 v[104:107], a[184:187], a[216:219], v[104:107]\n"
    "v_mfma_f32_16x16x32_bf16 v[108:111], a[184:187], a[220:223], v[108:111]\n"
    "v_mfma_f32_16x16x32_bf16 v[124:127], a[188:191], a[220:223], v[124:127]\n"
    "v_mfma_f32_16x16x32_bf16 v[120:123], a[188:191], a[216:219], v[120:123]\n"
    "v_mfma_f32_16x16x32_bf16 v[116:119], a[188:191], a[212:215], v[116:119]\n"
    "v_mfma_f32_16x16x32_bf16 v[112:115], a[188:191], a[208:211], v[112:115]\n"
    "s_waitcnt vmcnt(12) lgkmcnt(0)\n"
    "v_mfma_f32_16x16x32_bf16 v[0:3], a[128:131], a[224:227], v[0:3]\n"
    "s_sub_u32 s59, s59, 1\n"
    "ds_read_b64_tr_b16 a[160:161], v192 offset:8192\n"
    "v_mfma_f32_16x16x32_bf16 v[4:7], a[128:131], a[228:231], v[4:7]\n"
    "s_cmp_gt_i32 s59, 0\n"
    "ds_read_b64_tr_b16 a[162:163], v192 offset:12288\n"
    "v_mfma_f32_16x16x32_bf16 v[8:11], a[128:131], a[232:235], v[8:11]\n"
    "s_cselect_b32 s38, s38, 0\n"
    "ds_read_b64_tr_b16 a[164:165], v193 offset:8192\n"
    "v_mfma_f32_16x16x32_bf16 v[12:15], a[128:131], a[236:239], v[12:15]\n"
    "ds_read_b64_tr_b16 a[166:167], v193 offset:12288\n"
    "v_mfma_f32_16x16x32_bf16 v[28:31], a[132:135], a[236:239], v[28:31]\n"
    "buffer_load_dwordx4 a[208:211], %10, s[36:39], s52 offen sc1\n"
    "v_mfma_f32_16x16x32_bf16 v[24:27], a[132:135], a[232:235], v[24:27]\n"
    "ds_read_b64_tr_b16 a[168:169], v192 offset:8704\n"
    "v_mfma_f32_16x16x32_bf16 v[20:23], a[132:135], a[228:231], v[20:23]\n"
    "ds_read_b64_tr_b16 a[170:171], v192 offset:12800\n"
    "v_mfma_f32_16x16x32_bf16 v[16:19], a[132:135], a[224:227], v[16:19]\n"
    "s_add_u32 m0, m0, 1024\n"
    "ds_read_b64_tr_b16 a[172:173], v193 offset:8704\n"
    "v_mfma_f32_16x16x32_bf16 v[32:35], a[136:139], a[224:227], v[32:35]\n"
    "buffer_load_dwordx4 v202, s[40:43], s53 offen lds\n"
    "v_mfma_f32_16x16x32_bf16 v[36:39], a[136:139], a[228:231], v[36:39]\n"
    "ds_read_b64_tr_b16 a[174:175], v193 offset:12800\n"
    "v_mfma_f32_16x16x32_bf16 v[40:43], a[136:139], a[232:235], v[40:43]\n"
    "ds_read_b64_tr_b16 a[176:177], v192 offset:9216\n"
    "v_mfma_f32_16x16x32_bf16 v[44:47], a[136:139], a[236:239], v[44:47]\n"
    "ds_read_b64_tr_b16 a[178:179], v192 offset:13312\n"
    "v_mfma_f32_16x16x32_bf16 v[60:63], a[140:143], a[236:239], v[60:63]\n"
    "buffer_load_dwordx4 a[212:215], %10, s[36:39], s52 offen offset:1024 sc1\n"
    "v_mfma_f32_16x16x32_bf16 v[56:59], a[140:143], a[232:235], v[56:59]\n"
    "ds_read_b64_tr_b16 a[180:181], v193 offset:9216\n"
    "v_mfma_f32_16x16x32_bf16 v[52:55], a[140:143], a[228:231], v[52:55]\n"
    "ds_read_b64_tr_b16 a[182:183], v193 offset:13312\n"
    "v_mfma_f32_16x16x32_bf16 v[48:51], a[140:143], a[224:227], v[48:51]\n"
    "s_add_u32 m0, m0, 1024\n"
    "ds_read_b64_tr_b16 a[184:185], v192 offset:9728\n"
    "v_mfma_f32_16x16x32_bf16 v[64:67], a[144:147], a[224:227], v[64:67]\n"
    "buffer_load_dwordx4 v203, s[40:43], s53 offen lds\n"
    "v_mfma_f32_16x16x32_bf16 v[68:71], a[144:147], a[228:231], v[68:71]\n"
    "s_add_u32 s53, s53, 524288\n"
    "ds_read_b64_tr_b16 a[186:187], v192 offset:13824\n"
    "v_mfma_f32_16x16x32_bf16 v[72:75], a[144:147], a[232:235], v[72:75]\n"
    "s_add_u32 s57, s57, 16384\n"
    "ds_read_b64_tr_b16 a[188:189], v193 offset:9728\n"
    "v_mfma_f32_16x16x32_bf16 v[76:79], a[144:147], a[236:239], v[76:79]\n"
    "s_cmp_ge_u32 s57, 49152\n"
    "ds_read_b64_tr_b16 a[190:191], v193 offset:13824\n"
    "v_mfma_f32_16x16x32_bf16 v[92:95], a[148:151], a[236:239], v[92:95]\n"
    "s_cselect_b32 s57, 0, s57\n"
    "buffer_load_dwordx4 a[216:219], %10, s[36:39], s52 offen offset:2048 sc1\n"
    "v_mfma_f32_16x16x32_bf16 v[88:91], a[148:151], a[232:235], v[88:91]\n"
    "s_add_u32 s58, s58, 16384\n"
    "v_mfma_f32_16x16x32_bf16 v[84:87], a[148:151], a[228:231], v[84:87]\n"
    "s_cmp_ge_u32 s58, 49152\n"
    "v_mfma_f32_16x16x32_bf16 v[80:83], a[148:151], a[224:227], v[80:83]\n"
    "s_cselect_b32 s58, 0, s58\n"
    "v_mfma_f32_16x16x32_bf16 v[96:99], a[152:155], a[224:227], v[96:99]\n"
    "buffer_load_dwordx4 a[220:223], %10, s[36:39], s52 offen offset:3072 sc1\n"
    "v_add_u32 v192, s58, %34\n"
    "v_add_u32 v193, s58, v215\n"
    "v_mfma_f32_16x16x32_bf16 v[100:103], a[152:155], a[228:231], v[100:103]\n"
    "s_add_u32 s52, s52, 4096\n"
    "v_mfma_f32_16x16x32_bf16 v[104:107], a[152:155], a[232:235], v[104:107]\n"
    "v_mfma_f32_16x16x32_bf16 v[108:111], a[152:155], a[236:239], v[108:111]\n"
    "v_mfma_f32_16x16x32_bf16 v[124:127], a[156:159], a[236:239], v[124:127]\n"
    "v_mfma_f32_16x16x32_bf16 v[120:123], a[156:159], a[232:235], v[120:123]\n"
    "v_mfma_f32_16x16x32_bf16 v[116:119], a[156:159], a[228:231], v[116:119]\n"
    "v_mfma_f32_16x16x32_bf16 v[112:115], a[156:159], a[224:227], v[112:115]\n"
    "s_waitcnt vmcnt(12) lgkmcnt(0)\n"
    "v_mfma_f32_16x16x32_bf16 v[0:3], a[160:163], a[240:243], v[0:3]\n"
    "s_barrier\n"
    "v_mfma_f32_16x16x32_bf16 v[4:7], a[160:163], a[244:247], v[4:7]\n"
    "s_sub_u32 s59, s59, 1\n"
    "ds_read_b64_tr_b16 a[128:129], v192 offset:0\n"
    "v_mfma_f32_16x16x32_bf16 v[8:11], a[160:163], a[248:251], v[8:11]\n"
    "s_cmp_gt_i32 s59, 0\n"
    "ds_read_b64_tr_b16 a[130:131], v192 offset:4096\n"
    "v_mfma_f32_16x16x32_bf16 v[12:15], a[160:163], a[252:255], v[12:15]\n"
    "s_cselect_b32 s38, s38, 0\n"
    "ds_read_b64_tr_b16 a[132:133], v193 offset:0\n"
    "v_mfma_f32_16x16x32_bf16 v[28:31], a[164:167], a[252:255], v[28:31]\n"
    "s_sub_u32 s60, s60, 1\n"
    "ds_read_b64_tr_b16 a[134:135], v193 offset:4096\n"
    "v_mfma_f32_16x16x32_bf16 v[24:27], a[164:167], a[248:251], v[24:27]\n"
    "s_cmp_gt_i32 s60, 0\n"
    "ds_read_b64_tr_b16 a[136:137], v192 offset:512\n"
    "v_mfma_f32_16x16x32_bf16 v[20:23], a[164:167], a[244:247], v[20:23]\n"
    "s_cselect_b32 s42, s42, 0\n"
    "ds_read_b64_tr_b16 a[138:139], v192 offset:4608\n"
    "v_mfma_f32_16x16x32_bf16 v[16:19], a[164:167], a[240:243], v[16:19]\n"
    "ds_read_b64_tr_b16 a[140:141], v193 offset:512\n"
    "v_mfma_f32_16x16x32_bf16 v[32:35], a[168:171], a[240:243], v[32:35]\n"
    "buffer_load_dwordx4 a[224:227], %10, s[36:39], s52 offen sc1\n"
    "v_mfma_f32_16x16x32_bf16 v[36:39], a[168:171], a[244:247], v[36:39]\n"
    "ds_read_b64_tr_b16 a[142:143], v193 offset:4608\n"
    "v_mfma_f32_16x16x32_bf16 v[40:43], a[168:171], a[248:251], v[40:43]\n"
    "s_add_u32 m0, s57, s56\n"
    "ds_read_b64_tr_b16 a[144:145], v192 offset:1024\n"
    "v_mfma_f32_16x16x32_bf16 v[44:47], a[168:171], a[252:255], v[44:47]\n"
    "buffer_load_dwordx4 v200, s[40:43], s53 offen lds\n"
    "v_mfma_f32_16x16x32_bf16 v[60:63], a[172:175], a[252:255], v[60:63]\n"
    "ds_read_b64_tr_b16 a[146:147], v192 offset:5120\n"
    "v_mfma_f32_16x16x32_bf16 v[56:59], a[172:175], a[248:251], v[56:59]\n"
    "ds_read_b64_tr_b16 a[148:149], v193 offset:1024\n"
    "v_mfma_f32_16x16x32_bf16 v[52:55], a[172:175], a[244:247], v[52:55]\n"
    "buffer_load_dwordx4 a[228:231], %10, s[36:39], s52 offen offset:1024 sc1\n"
    "v_mfma_f32_16x16x32_bf16 v[48:51], a[172:175], a[240:243], v[48:51]\n"
    "ds_read_b64_tr_b16 a[150:151], v193 offset:5120\n"
    "v_mfma_f32_16x16x32_bf16 v[64:67], a[176:179], a[240:243], v[64:67]\n"
    "s_add_u32 m0, m0, 1024\n"
    "ds_read_b64_tr_b16 a[152:153], v192 offset:1536\n"
    "v_mfma_f32_16x16x32_bf16 v[68:71], a[176:179], a[244:247], v[68:71]\n"
    "buffer_load_dwordx4 v201, s[40:43], s53 offen lds\n"
    "v_mfma_f32_16x16x32_bf16 v[72:75], a[176:179], a[248:251], v[72:75]\n"
    "ds_read_b64_tr_b16 a[154:155], v192 offset:5632\n"
    "v_mfma_f32_16x16x32_bf16 v[76:79], a[176:179], a[252:255], v[76:79]\n"
    "ds_read_b64_tr_b16 a[156:157], v193 offset:1536\n"
    "v_mfma_f32_16x16x32_bf16 v[92:95], a[180:183], a[252:255], v[92:95]\n"
    "buffer_load_dwordx4 a[232:235], %10, s[36:39], s52 offen offset:2048 sc1\n"
    "v_mfma_f32_16x16x32_bf16 v[88:91], a[180:183], a[248:251], v[88:91]\n"
    "ds_read_b64_tr_b16 a[158:159], v193 offset:5632\n"
    "v_mfma_f32_16x16x32_bf16 v[84:87], a[180:183], a[244:247], v[84:87]\n"
    "v_mfma_f32_16x16x32_bf16 v[80:83], a[180:183], a[240:243], v[80:83]\n"
    "buffer_load_dwordx4 a[236:239], %10, s[36:39], s52 offen offset:3072 sc1\n"
    "v_mfma_f32_16x16x32_bf16 v[96:99], a[184:187], a[240:243], v[96:99]\n"
    "s_add_u32 s52, s52, 4096\n"
    "v_mfma_f32_16x16x32_bf16 v[100:103], a[184:187], a[244:247], v[100:103]\n"
    "v_mfma_f32_16x16x32_bf16 v[104:107], a[184:187], a[248:251], v[104:107]\n"
    "v_mfma_f32_16x16x32_bf16 v[108:111], a[184:187], a[252:255], v[108:111]\n"
    "v_mfma_f32_16x16x32_bf16 v[124:127], a[188:191], a[252:255], v[124:127]\n"
    "v_mfma_f32_16x16x32_bf16 v[120:123], a[188:191], a[248:251], v[120:123]\n"
    "v_mfma_f32_16x16x32_bf16 v[116:119], a[188:191], a[244:247], v[116:119]\n"
    "v_mfma_f32_16x16x32_bf16 v[112:115], a[188:191], a[240:243], v[112:115]\n"
    "s_waitcnt vmcnt(12) lgkmcnt(0)\n"
    "s_sub_u32 s55, s55, 1\n"
    "s_cmp_lg_u32 s55, 0\n"
    "s_cbranch_scc1 L_qp_loop\n"
    "s_cmp_lg_u32 %25, 0\n"
    "s_cbranch_scc1 L_attn_slow\n"
    "s_add_u32 m0, s73, s79\n"
    "s_nop 0\n"
    "buffer_load_dwordx4 v204, s[64:67], s72 offen nt lds\n"
    "buffer_load_dwordx4 v205, s[64:67], s72 offen offset:1024 nt lds\n"
    "buffer_load_dwordx4 v206, s[64:67], s72 offen offset:2048 nt lds\n"
    "buffer_load_dwordx4 v207, s[64:67], s72 offen offset:3072 nt lds\n"
    "s_add_u32 m0, s74, s79\n"
    "s_nop 0\n"
    "buffer_load_dwordx4 v208, s[68:71], s72 offen nt lds\n"
    "buffer_load_dwordx4 v209, s[68:71], s72 offen offset:1024 nt lds\n"
    "buffer_load_dwordx4 v208, s[68:71], s72 offen offset:2048 nt lds\n"
    "buffer_load_dwordx4 v209, s[68:71], s72 offen offset:3072 nt lds\n"
    "s_add_u32 s72, s72, 16384\n"
    "s_add_u32 s73, s73, 16384\n"
    "s_cmp_ge_u32 s73, 98304\n"
    "s_cselect_b32 s73, 49152, s73\n"
    "s_add_u32 m0, s73, s79\n"
    "s_nop 0\n"
    "buffer_load_dwordx4 v204, s[64:67], s72 offen nt lds\n"
    "buffer_load_dwordx4 v205, s[64:67], s72 offen offset:1024 nt lds\n"
    "s_mov_b32 s78, 30\n"
    "s_mov_b32 s77, 16\n"
    "s_mov_b32 s76, 147456\n"
    "s_nop 7\n"
    "v_cvt_pk_bf16_f32 v128, v0, v1\n"
    "v_cvt_pk_bf16_f32 v129, v2, v3\n"
    "v_cvt_pk_bf16_f32 v130, v16, v17\n"
    "v_cvt_pk_bf16_f32 v131, v18, v19\n"
    "v_cvt_pk_bf16_f32 v132, v32, v33\n"
    "v_cvt_pk_bf16_f32 v133, v34, v35\n"
    "v_cvt_pk_bf16_f32 v134, v48, v49\n"
    "v_cvt_pk_bf16_f32 v135, v50, v51\n"
    "v_cvt_pk_bf16_f32 v136, v64, v65\n"
    "v_cvt_pk_bf16_f32 v137, v66, v67\n"
    "v_cvt_pk_bf16_f32 v138, v80, v81\n"
    "v_cvt_pk_bf16_f32 v139, v82, v83\n"
    "v_cvt_pk_bf16_f32 v140, v96, v97\n"
    "v_cvt_pk_bf16_f32 v141, v98, v99\n"
    "v_cvt_pk_bf16_f32 v142, v112, v113\n"
    "v_cvt_pk_bf16_f32 v143, v114, v115\n"
    "v_cvt_pk_bf16_f32 v144, v4, v5\n"
    "v_cvt_pk_bf16_f32 v145, v6, v7\n"
    "v_cvt_pk_bf16_f32 v146, v20, v21\n"
    "v_cvt_pk_bf16_f32 v147, v22, v23\n"
    "v_cvt_pk_bf16_f32 v148, v36, v37\n"
    "v_cvt_pk_bf16_f32 v149, v38, v39\n"
    "v_cvt_pk_bf16_f32 v150, v52, v53\n"
    "v_cvt_pk_bf16_f32 v151, v54, v55\n"
    "v_cvt_pk_bf16_f32 v152, v68, v69\n"
    "v_cvt_pk_bf16_f32 v153, v70, v71\n"
    "v_cvt_pk_bf16_f32 v154, v84, v85\n"
    "v_cvt_pk_bf16_f32 v155, v86, v87\n"
    "v_cvt_pk_bf16_f32 v156, v100, v101\n"
    "v_cvt_pk_bf16_f32 v157, v102, v103\n"
    "v_cvt_pk_bf16_f32 v158, v116, v117\n"
    "v_cvt_pk_bf16_f32 v159, v118, v119\n"
    "v_cvt_pk_bf16_f32 v160, v8, v9\n"
    "v_cvt_pk_bf16_f32 v161, v10, v11\n"
    "v_cvt_pk_bf16_f32 v162, v24, v25\n"
    "v_cvt_pk_bf16_f32 v163, v26, v27\n"
    "v_cvt_pk_bf16_f32 v164, v40, v41\n"
    "v_cvt_pk_bf16_f32 v165, v42, v43\n"
    "v_cvt_pk_bf16_f32 v166, v56, v57\n"
    "v_cvt_pk_bf16_f32 v167, v58, v59\n"
    "v_cvt_pk_bf16_f32 v168, v72, v73\n"
    "v_cvt_pk_bf16_f32 v169, v74, v75\n"
    "v_cvt_pk_bf16_f32 v170, v88, v89\n"
    "v_cvt_pk_bf16_f32 v171, v90, v91\n"
    "v_cvt_pk_bf16_f32 v172, v104, v105\n"
    "v_cvt_pk_bf16_f32 v173, v106, v107\n"
    "v_cvt_pk_bf16_f32 v174, v120, v121\n"
    "v_cvt_pk_bf16_f32 v175, v122, v123\n"
    "v_cvt_pk_bf16_f32 v176, v12, v13\n"
    "v_cvt_pk_bf16_f32 v177, v14, v15\n"
    "v_cvt_pk_bf16_f32 v178, v28, v29\n"
    "v_cvt_pk_bf16_f32 v179, v30, v31\n"
    "v_cvt_pk_bf16_f32 v180, v44, v45\n"
    "v_cvt_pk_bf16_f32 v181, v46, v47\n"
    "v_cvt_pk_bf16_f32 v182, v60, v61\n"
    "v_cvt_pk_bf16_f32 v183, v62, v63\n"
    "v_cvt_pk_bf16_f32 v184, v76, v77\n"
    "v_cvt_pk_bf16_f32 v185, v78, v79\n"
    "v_cvt_pk_bf16_f32 v186, v92, v93\n"
    "v_cvt_pk_bf16_f32 v187, v94, v95\n"
    "v_cvt_pk_bf16_f32 v188, v108, v109\n"
    "v_cvt_pk_bf16_f32 v189, v110, v111\n"
    "v_cvt_pk_bf16_f32 v190, v124, v125\n"
    "v_cvt_pk_bf16_f32 v191, v126, v127\n"
    "s_nop 1\n"
    "s_waitcnt vmcnt(10)\n"
    "s_barrier\n"
    "s_mov_b64 s[92:93], %22\n"
    "s_mov_b32 s94, %23\n"
    "s_mov_b32 s95, 0x20000\n"
    "s_mov_b32 s82, 0\n"
    "s_lshl_b32 s96, s79, 1\n"
    "s_lshr_b32 s97, s79, 2\n"
    "s_add_u32 s97, s97, 32768\n"
    "ds_read_b128 a[128:131], v210 offset:0\n"
    "ds_read_b128 a[132:135], v211 offset:0\n"
    "ds_read_b128 a[136:139], v210 offset:1024\n"
    "ds_read_b128 a[140:143], v211 offset:1024\n"
    "s_waitcnt lgkmcnt(0)\n"
    "v_mfma_f32_16x16x32_bf16 v[0:3], a[128:131], v[128:131], 0\n"
    "v_mfma_f32_16x16x32_bf16 v[0:3], a[132:135], v[132:135], v[0:3]\n"
    "v_mfma_f32_16x16x32_bf16 v[0:3], a[136:139], v[136:139], v[0:3]\n"
    "v_mfma_f32_16x16x32_bf16 v[0:3], a[140:143], v[140:143], v[0:3]\n"
    "v_mfma_f32_16x16x32_bf16 v[4:7], a[128:131], v[144:147], 0\n"
    "v_mfma_f32_16x16x32_bf16 v[4:7], a[132:135], v[148:151], v[4:7]\n"
    "v_mfma_f32_16x16x32_bf16 v[4:7], a[136:139], v[152:155], v[4:7]\n"
    "v_mfma_f32_16x16x32_bf16 v[4:7], a[140:143], v[156:159], v[4:7]\n"
    "v_mfma_f32_16x16x32_bf16 v[8:11], a[128:131], v[160:163], 0\n"
    "v_mfma_f32_16x16x32_bf16 v[8:11], a[132:135], v[164:167], v[8:11]\n"
    "v_mfma_f32_16x16x32_bf16 v[8:11], a[136:139], v[168:171], v[8:11]\n"
    "v_mfma_f32_16x16x32_bf16 v[8:11], a[140:143], v[172:175], v[8:11]\n"
    "v_mfma_f32_16x16x32_bf16 v[12:15], a[128:131], v[176:179], 0\n"
    "v_mfma_f32_16x16x32_bf16 v[12:15], a[132:135], v[180:183], v[12:15]\n"
    "v_mfma_f32_16x16x32_bf16 v[12:15], a[136:139], v[184:187], v[12:15]\n"
    "v_mfma_f32_16x16x32_bf16 v[12:15], a[140:143], v[188:191], v[12:15]\n"
    "v_mov_b32 v16, 0xff800000\n"
    "v_mov_b32 v32, 0\n"
    "v_mov_b32 v48, 0\n"
    "v_mov_b32 v17, 0xff800000\n"
    "v_mov_b32 v33, 0\n"
    "v_mov_b32 v49, 0\n"
    "v_mov_b32 v18, 0xff800000\n"
    "v_mov_b32 v34, 0\n"
    "v_mov_b32 v50, 0\n"
    "v_mov_b32 v19, 0xff800000\n"
    "v_mov_b32 v35, 0\n"
    "v_mov_b32 v51, 0\n"
    "v_mov_b32 v80, 0\n"
    "v_mov_b32 v84, 0\n"
    "v_mov_b32 v20, 0xff800000\n"
    "v_mov_b32 v36, 0\n"
    "v_mov_b32 v52, 0\n"
    "v_mov_b32 v21, 0xff800000\n"
    "v_mov_b32 v37, 0\n"
    "v_mov_b32 v53, 0\n"
    "v_mov_b32 v22, 0xff800000\n"
    "v_mov_b32 v38, 0\n"
    "v_mov_b32 v54, 0\n"
    "v_mov_b32 v23, 0xff800000\n"
    "v_mov_b32 v39, 0\n"
    "v_mov_b32 v55, 0\n"
    "v_mov_b32 v81, 0\n"
    "v_mov_b32 v85, 0\n"
    "v_mov_b32 v24, 0xff800000\n"
    "v_mov_b32 v40, 0\n"
    "v_mov_b32 v56, 0\n"
    "v_mov_b32 v25, 0xff800000\n"
    "v_mov_b32 v41, 0\n"
    "v_mov_b32 v57, 0\n"
    "v_mov_b32 v26, 0xff800000\n"
    "v_mov_b32 v42, 0\n"
    "v_mov_b32 v58, 0\n"
    "v_mov_b32 v27, 0xff800000\n"
    "v_mov_b32 v43, 0\n"
    "v_mov_b32 v59, 0\n"
    "v_mov_b32 v82, 0\n"
    "v_mov_b32 v86, 0\n"
    "v_mov_b32 v28, 0xff800000\n"
    "v_mov_b32 v44, 0\n"
    "v_mov_b32 v60, 0\n"
    "v_mov_b32 v29, 0xff800000\n"
    "v_mov_b32 v45, 0\n"
    "v_mov_b32 v61, 0\n"
    "v_mov_b32 v30, 0xff800000\n"
    "v_mov_b32 v46, 0\n"
    "v_mov_b32 v62, 0\n"
    "v_mov_b32 v31, 0xff800000\n"
    "v_mov_b32 v47, 0\n"
    "v_mov_b32 v63, 0\n"
    "v_mov_b32 v83, 0\n"
    "v_mov_b32 v87, 0\n"
    "v_accvgpr_write_b32 a160, 0\n"
    "v_accvgpr_write_b32 a161, 0\n"
    "v_accvgpr_write_b32 a162, 0\n"
    "v_accvgpr_write_b32 a163, 0\n"
    "v_accvgpr_write_b32 a164, 0\n"
    "v_accvgpr_write_b32 a165, 0\n"
    "v_accvgpr_write_b32 a166, 0\n"
    "v_accvgpr_write_b32 a167, 0\n"
    "v_accvgpr_write_b32 a168, 0\n"
    "v_accvgpr_write_b32 a169, 0\n"
    "v_accvgpr_write_b32 a170, 0\n"
    "v_accvgpr_write_b32 a171, 0\n"
    "v_accvgpr_write_b32 a172, 0\n"
    "v_accvgpr_write_b32 a173, 0\n"
    "v_accvgpr_write_b32 a174, 0\n"
    "v_accvgpr_write_b32 a175, 0\n"
    "v_accvgpr_write_b32 a176, 0\n"
    "v_accvgpr_write_b32 a177, 0\n"
    "v_accvgpr_write_b32 a178, 0\n"
    "v_accvgpr_write_b32 a179, 0\n"
    "v_accvgpr_write_b32 a180, 0\n"
    "v_accvgpr_write_b32 a181, 0\n"
    "v_accvgpr_write_b32 a182, 0\n"
    "v_accvgpr_write_b32 a183, 0\n"
    "v_accvgpr_write_b32 a184, 0\n"
    "v_accvgpr_write_b32 a185, 0\n"
    "v_accvgpr_write_b32 a186, 0\n"
    "v_accvgpr_write_b32 a187, 0\n"
    "v_accvgpr_write_b32 a188, 0\n"
    "v_accvgpr_write_b32 a189, 0\n"
    "v_accvgpr_write_b32 a190, 0\n"
    "v_accvgpr_write_b32 a191, 0\n"
    "v_mov_b32 v108, 0\n"
    "v_mov_b32 v109, 0\n"
    "v_mov_b32 v110, 0\n"
    "v_mov_b32 v111, 0\n"
    "v_mov_b32 v112, 0\n"
    "v_mov_b32 v113, 0\n"
    "v_mov_b32 v114, 0\n"
    "v_mov_b32 v115, 0\n"
    "v_mov_b32 v116, 0\n"
    "v_mov_b32 v117, 0\n"
    "v_mov_b32 v118, 0\n"
    "v_mov_b32 v119, 0\n"
    "v_mov_b32 v124, 0x3f803f80\n"
    "v_mov_b32 v125, 0x3f803f80\n"
    "v_mov_b32 v126, 0x3f803f80\n"
    "v_mov_b32 v127, 0x3f803f80\n"
    "v_max3_f32 v90, v0, v1, v2\n"
    "v_max3_f32 v91, v4, v5, v6\n"
    "v_max3_f32 v92, v8, v9, v10\n"
    "v_max3_f32 v93, v12, v13, v14\n"
    "v_max_f32 v90, v90, v3\n"
    "v_max_f32 v91, v91, v7\n"
    "v_max_f32 v92, v92, v11\n"
    "v_max_f32 v93, v93, v15\n"
    "v_mov_b32 v94, v90\n"
    "v_mov_b32 v95, v91\n"
    "v_mov_b32 v96, v92\n"
    "v_mov_b32 v97, v93\n"
    "s_nop 0\n"
    "v_permlane32_swap_b32 v90, v94\n"
    "v_permlane32_swap_b32 v91, v95\n"
    "v_permlane32_swap_b32 v92, v96\n"
    "v_permlane32_swap_b32 v93, v97\n"
    "s_nop 0\n"
    "v_max_f32 v90, v90, v94\n"
    "v_max_f32 v91, v91, v95\n"
    "v_max_f32 v92, v92, v96\n"
    "v_max_f32 v93, v93, v97\n"
    "v_mov_b32 v94, v90\n"
    "v_mov_b32 v95, v91\n"
    "v_mov_b32 v96, v92\n"
    "v_mov_b32 v97, v93\n"
    "s_nop 0\n"
    "v_permlane16_swap_b32 v90, v94\n"
    "v_permlane16_swap_b32 v91, v95\n"
    "v_permlane16_swap_b32 v92, v96\n"
    "v_permlane16_swap_b32 v93, v97\n"
    "s_nop 0\n"
    "v_max_f32 v90, v90, v94\n"
    "v_max_f32 v91, v91, v95\n"
    "v_max_f32 v92, v92, v96\n"
    "v_max_f32 v93, v93, v97\n"
    "v_sub_f32 v64, 0xc2cc0000, v90\n"
    "v_sub_f32 v65, 0xc2cc0000, v90\n"
    "v_sub_f32 v66, 0xc2cc0000, v90\n"
    "v_sub_f32 v67, 0xc2cc0000, v90\n"
    "v_sub_f32 v68, 0xc2cc0000, v91\n"
    "v_sub_f32 v69, 0xc2cc0000, v91\n"
    "v_sub_f32 v70, 0xc2cc0000, v91\n"
    "v_sub_f32 v71, 0xc2cc0000, v91\n"
    "v_sub_f32 v72, 0xc2cc0000, v92\n"
    "v_sub_f32 v73, 0xc2cc0000, v92\n"
    "v_sub_f32 v74, 0xc2cc0000, v92\n"
    "v_sub_f32 v75, 0xc2cc0000, v92\n"
    "v_sub_f32 v76, 0xc2cc0000, v93\n"
    "v_sub_f32 v77, 0xc2cc0000, v93\n"
    "v_sub_f32 v78, 0xc2cc0000, v93\n"
    "v_sub_f32 v79, 0xc2cc0000, v93\n"
    "s_nop 1\n"
    "L_attn_entry:\n"
    ".p2align 6\n"
    "L_attn_loop:\n"
    "v_mfma_f32_16x16x32_bf16 v[0:3], a[128:131], v[128:131], v[64:67]\n"
    "v_exp_f32 v24, v24\n"
    "v_mfma_f32_16x16x32_bf16 v[0:3], a[132:135], v[132:135], v[0:3]\n"
    "s_add_u32 s74, s74, 16384\n"
    "ds_read_b128 a[144:147], v210 offset:4096\n"
    "ds_read_b128 a[148:151], v211 offset:4096\n"
    "v_mfma_f32_16x16x32_bf16 v[0:3], a[136:139], v[136:139], v[0:3]\n"
    "s_cmp_ge_u32 s74, 163840\n"
    "v_exp_f32 v25, v25\n"
    "v_mfma_f32_16x16x32_bf16 v[0:3], a[140:143], v[140:143], v[0:3]\n"
    "s_cselect_b32 s74, 98304, s74\n"
    "v_exp_f32 v26, v26\n"
    "v_mfma_f32_16x16x32_bf16 v[4:7], a[140:143], v[156:159], v[68:71]\n"
    "v_exp_f32 v27, v27\n"
    "v_mfma_f32_16x16x32_bf16 v[4:7], a[136:139], v[152:155], v[4:7]\n"
    "ds_read_b128 a[152:155], v210 offset:5120\n"
    "ds_read_b128 a[156:159], v211 offset:5120\n"
    "v_mfma_f32_16x16x32_bf16 v[4:7], a[132:135], v[148:151], v[4:7]\n"
    "v_exp_f32 v28, v28\n"
    "v_mfma_f32_16x16x32_bf16 v[4:7], a[128:131], v[144:147], v[4:7]\n"
    "v_exp_f32 v29, v29\n"
    "v_mfma_f32_16x16x32_bf16 v[8:11], a[128:131], v[160:163], v[72:75]\n"
    "v_exp_f32 v30, v30\n"
    "v_mfma_f32_16x16x32_bf16 v[8:11], a[132:135], v[164:167], v[8:11]\n"
    "v_exp_f32 v31, v31\n"
    "v_mfma_f32_16x16x32_bf16 v[8:11], a[136:139], v[168:171], v[8:11]\n"
    "v_cvt_pk_bf16_f32 v58, v24, v25\n"
    "v_cvt_pk_bf16_f32 v59, v26, v27\n"
    "v_mfma_f32_16x16x32_bf16 v[8:11], a[140:143], v[172:175], v[8:11]\n"
    "buffer_load_dwordx4 v206, s[64:67], s72 offen offset:2048 nt lds\n"
    "v_mfma_f32_16x16x32_bf16 v[12:15], a[140:143], v[188:191], v[76:79]\n"
    "v_cvt_pk_bf16_f32 v62, v28, v29\n"
    "v_cvt_pk_bf16_f32 v63, v30, v31\n"
    "v_mfma_f32_16x16x32_bf16 v[12:15], a[136:139], v[184:187], v[12:15]\n"
    "v_add_f32 v83, v83, v28\n"
    "v_add_f32 v87, v87, v29\n"
    "v_mfma_f32_16x16x32_bf16 v[12:15], a[132:135], v[180:183], v[12:15]\n"
    "v_add_f32 v83, v83, v30\n"
    "v_add_f32 v87, v87, v31\n"
    "v_mfma_f32_16x16x32_bf16 v[12:15], a[128:131], v[176:179], v[12:15]\n"
    "s_waitcnt lgkmcnt(4)\n"
    "v_mfma_f32_16x16x32_bf16 a[0:3], a[160:163], v[48:51], a[0:3]\n"
    "ds_read_b64_tr_b16 a[176:177], v212 offset:9216\n"
    "ds_read_b64_tr_b16 a[178:179], v212 offset:13312\n"
    "v_mfma_f32_16x16x32_bf16 a[4:7], a[160:163], v[52:55], a[4:7]\n"
    "s_add_u32 s76, s76, 16384\n"
    "v_exp_f32 v0, v0\n"
    "v_mfma_f32_16x16x32_bf16 a[8:11], a[160:163], v[56:59], a[8:11]\n"
    "s_cmp_ge_u32 s76, 163840\n"
    "v_exp_f32 v1, v1\n"
    "v_mfma_f32_16x16x32_bf16 a[12:15], a[160:163], v[60:63], a[12:15]\n"
    "s_cselect_b32 s76, 98304, s76\n"
    "ds_read_b64_tr_b16 a[180:181], v213 offset:9216\n"
    "ds_read_b64_tr_b16 a[182:183], v213 offset:13312\n"
    "v_mfma_f32_16x16x32_bf16 a[28:31], a[164:167], v[60:63], a[28:31]\n"
    "v_exp_f32 v2, v2\n"
    "v_mfma_f32_16x16x32_bf16 a[24:27], a[164:167], v[56:59], a[24:27]\n"
    "v_exp_f32 v3, v3\n"
    "v_mfma_f32_16x16x32_bf16 a[20:23], a[164:167], v[52:55], a[20:23]\n"
    "ds_read_b64_tr_b16 a[184:185], v212 offset:9728\n"
    "ds_read_b64_tr_b16 a[186:187], v212 offset:13824\n"
    "v_mfma_f32_16x16x32_bf16 a[16:19], a[164:167], v[48:51], a[16:19]\n"
    "v_exp_f32 v4, v4\n"
    "v_mfma_f32_16x16x32_bf16 a[32:35], a[168:171], v[48:51], a[32:35]\n"
    "v_exp_f32 v5, v5\n"
    "v_mfma_f32_16x16x32_bf16 a[36:39], a[168:171], v[52:55], a[36:39]\n"
    "ds_read_b64_tr_b16 a[188:189], v213 offset:9728\n"
    "ds_read_b64_tr_b16 a[190:191], v213 offset:13824\n"
    "v_mfma_f32_16x16x32_bf16 a[40:43], a[168:171], v[56:59], a[40:43]\n"
    "v_exp_f32 v6, v6\n"
    "v_mfma_f32_16x16x32_bf16 a[44:47], a[168:171], v[60:63], a[44:47]\n"
    "v_exp_f32 v7, v7\n"
    "v_mfma_f32_16x16x32_bf16 a[60:63], a[172:175], v[60:63], a[60:63]\n"
    "buffer_load_dwordx4 v207, s[64:67], s72 offen offset:3072 nt lds\n"
    "v_mfma_f32_16x16x32_bf16 a[56:59], a[172:175], v[56:59], a[56:59]\n"
    "s_mov_b32 m0, s96\n"
    "v_mfma_f32_16x16x32_bf16 a[52:55], a[172:175], v[52:55], a[52:55]\n"
    "buffer_load_dwordx4 %24, s[92:95], s82 offen lds\n"
    "v_mfma_f32_16x16x32_bf16 a[48:51], a[172:175], v[48:51], a[48:51]\n"
    "v_add_u32 v212, s76, %11\n"
    "v_add_u32 v213, s76, %12\n"
    "v_mfma_f32_16x16x32_bf16 v[108:111], v[124:127], v[48:51], v[108:111]\n"
    "v_mfma_f32_16x16x32_bf16 v[112:115], v[124:127], v[52:55], v[112:115]\n"
    "v_cvt_pk_bf16_f32 v32, v0, v1\n"
    "v_cvt_pk_bf16_f32 v33, v2, v3\n"
    "v_mfma_f32_16x16x32_bf16 v[116:119], v[124:127], v[56:59], v[116:119]\n"
    "s_waitcnt lgkmcnt(8)\n"
    "v_cvt_pk_bf16_f32 v36, v4, v5\n"
    "v_cvt_pk_bf16_f32 v37, v6, v7\n"
    "v_mfma_f32_16x16x32_bf16 v[16:19], a[144:147], v[128:131], v[64:67]\n"
    "v_exp_f32 v8, v8\n"
    "v_mfma_f32_16x16x32_bf16 v[16:19], a[148:151], v[132:135], v[16:19]\n"
    "ds_read_b128 a[128:131], v210 offset:8192\n"
    "ds_read_b128 a[132:135], v211 offset:8192\n"
    "v_mfma_f32_16x16x32_bf16 v[16:19], a[152:155], v[136:139], v[16:19]\n"
    "v_exp_f32 v9, v9\n"
    "v_mfma_f32_16x16x32_bf16 v[16:19], a[156:159], v[140:143], v[16:19]\n"
    "v_exp_f32 v10, v10\n"
    "v_mfma_f32_16x16x32_bf16 v[20:23], a[156:159], v[156:159], v[68:71]\n"
    "v_exp_f32 v11, v11\n"
    "v_mfma_f32_16x16x32_bf16 v[20:23], a[152:155], v[152:155], v[20:23]\n"
    "s_add_u32 s73, s73, 16384\n"
    "ds_read_b128 a[136:139], v210 offset:9216\n"
    "ds_read_b128 a[140:143], v211 offset:9216\n"
    "v_mfma_f32_16x16x32_bf16 v[20:23], a[148:151], v[148:151], v[20:23]\n"
    "s_cmp_ge_u32 s73, 98304\n"
    "v_exp_f32 v12, v12\n"
    "v_mfma_f32_16x16x32_bf16 v[20:23], a[144:147], v[144:147], v[20:23]\n"
    "s_cselect_b32 s73, 49152, s73\n"
    "v_exp_f32 v13, v13\n"
    "v_mfma_f32_16x16x32_bf16 v[24:27], a[144:147], v[160:163], v[72:75]\n"
    "v_exp_f32 v14, v14\n"
    "v_mfma_f32_16x16x32_bf16 v[24:27], a[148:151], v[164:167], v[24:27]\n"
    "v_exp_f32 v15, v15\n"
    "v_mfma_f32_16x16x32_bf16 v[24:27], a[152:155], v[168:171], v[24:27]\n"
    "s_add_u32 m0, s74, s79\n"
    "v_cvt_pk_bf16_f32 v40, v8, v9\n"
    "v_cvt_pk_bf16_f32 v41, v10, v11\n"
    "v_mfma_f32_16x16x32_bf16 v[24:27], a[156:159], v[172:175], v[24:27]\n"
    "buffer_load_dwordx4 v208, s[68:71], s72 offen nt lds\n"
    "v_mfma_f32_16x16x32_bf16 v[28:31], a[156:159], v[188:191], v[76:79]\n"
    "v_cvt_pk_bf16_f32 v44, v12, v13\n"
    "v_cvt_pk_bf16_f32 v45, v14, v15\n"
    "v_mfma_f32_16x16x32_bf16 v[28:31], a[152:155], v[184:187], v[28:31]\n"
    "v_add_f32 v83, v83, v12\n"
    "v_add_f32 v87, v87, v13\n"
    "v_mfma_f32_16x16x32_bf16 v[28:31], a[148:151], v[180:183], v[28:31]\n"
    "v_add_f32 v83, v83, v14\n"
    "v_add_f32 v87, v87, v15\n"
    "v_mfma_f32_16x16x32_bf16 v[28:31], a[144:147], v[176:179], v[28:31]\n"
    "s_waitcnt lgkmcnt(4)\n"
    "v_mfma_f32_16x16x32_bf16 a[64:67], a[176:179], v[48:51], a[64:67]\n"
    "s_add_u32 s82, s82, 12288\n"
    "ds_read_b64_tr_b16 a[160:161], v212 offset:0\n"
    "ds_read_b64_tr_b16 a[162:163], v212 offset:4096\n"
    "v_mfma_f32_16x16x32_bf16 a[68:71], a[176:179], v[52:55], a[68:71]\n"
    "s_add_u32 s96, s96, 1024\n"
    "v_exp_f32 v16, v16\n"
    "v_mfma_f32_16x16x32_bf16 a[72:75], a[176:179], v[56:59], a[72:75]\n"
    "s_cmp_ge_u32 s82, 98304\n"
    "v_exp_f32 v17, v17\n"
    "v_mfma_f32_16x16x32_bf16 a[76:79], a[176:179], v[60:63], a[76:79]\n"
    "s_cselect_b32 s96, s97, s96\n"
    "ds_read_b64_tr_b16 a[164:165], v213 offset:0\n"
    "ds_read_b64_tr_b16 a[166:167], v213 offset:4096\n"
    "v_mfma_f32_16x16x32_bf16 a[92:95], a[180:183], v[60:63], a[92:95]\n"
    "s_cselect_b32 s94, 0, s94\n"
    "v_exp_f32 v18, v18\n"
    "v_mfma_f32_16x16x32_bf16 a[88:91], a[180:183], v[56:59], a[88:91]\n"
    "v_exp_f32 v19, v19\n"
    "v_mfma_f32_16x16x32_bf16 a[84:87], a[180:183], v[52:55], a[84:87]\n"
    "ds_read_b64_tr_b16 a[168:169], v212 offset:512\n"
    "ds_read_b64_tr_b16 a[170:171], v212 offset:4608\n"
    "v_mfma_f32_16x16x32_bf16 a[80:83], a[180:183], v[48:51], a[80:83]\n"
    "v_exp_f32 v20, v20\n"
    "v_mfma_f32_16x16x32_bf16 a[96:99], a[184:187], v[48:51], a[96:99]\n"
    "v_exp_f32 v21, v21\n"
    "v_mfma_f32_16x16x32_bf16 a[100:103], a[184:187], v[52:55], a[100:103]\n"
    "ds_read_b64_tr_b16 a[172:173], v213 offset:512\n"
    "ds_read_b64_tr_b16 a[174:175], v213 offset:4608\n"
    "v_mfma_f32_16x16x32_bf16 a[104:107], a[184:187], v[56:59], a[104:107]\n"
    "v_exp_f32 v22, v22\n"
    "v_mfma_f32_16x16x32_bf16 a[108:111], a[184:187], v[60:63], a[108:111]\n"
    "v_exp_f32 v23, v23\n"
    "v_mfma_f32_16x16x32_bf16 a[124:127], a[188:191], v[60:63], a[124:127]\n"
    "buffer_load_dwordx4 v209, s[68:71], s72 offen offset:1024 nt lds\n"
    "v_mfma_f32_16x16x32_bf16 a[120:123], a[188:191], v[56:59], a[120:123]\n"
    "v_mfma_f32_16x16x32_bf16 a[116:119], a[188:191], v[52:55], a[116:119]\n"
    "v_cvt_pk_bf16_f32 v34, v16, v17\n"
    "v_cvt_pk_bf16_f32 v35, v18, v19\n"
    "v_mfma_f32_16x16x32_bf16 a[112:115], a[188:191], v[48:51], a[112:115]\n"
    "s_waitcnt lgkmcnt(8)\n"
    "v_cvt_pk_bf16_f32 v38, v20, v21\n"
    "v_cvt_pk_bf16_f32 v39, v22, v23\n"
    "v_mfma_f32_16x16x32_bf16 v[0:3], a[128:131], v[128:131], v[64:67]\n"
    "v_exp_f32 v24, v24\n"
    "v_mfma_f32_16x16x32_bf16 v[0:3], a[132:135], v[132:135], v[0:3]\n"
    "ds_read_b128 a[144:147], v210 offset:12288\n"
    "ds_read_b128 a[148:151], v211 offset:12288\n"
    "v_mfma_f32_16x16x32_bf16 v[0:3], a[136:139], v[136:139], v[0:3]\n"
    "v_exp_f32 v25, v25\n"
    "v_mfma_f32_16x16x32_bf16 v[0:3], a[140:143], v[140:143], v[0:3]\n"
    "v_exp_f32 v26, v26\n"
    "v_mfma_f32_16x16x32_bf16 v[4:7], a[140:143], v[156:159], v[68:71]\n"
    "v_exp_f32 v27, v27\n"
    "v_mfma_f32_16x16x32_bf16 v[4:7], a[136:139], v[152:155], v[4:7]\n"
    "ds_read_b128 a[152:155], v210 offset:13312\n"
    "ds_read_b128 a[156:159], v211 offset:13312\n"
    "v_mfma_f32_16x16x32_bf16 v[4:7], a[132:135], v[148:151], v[4:7]\n"
    "v_exp_f32 v28, v28\n"
    "v_mfma_f32_16x16x32_bf16 v[4:7], a[128:131], v[144:147], v[4:7]\n"
    "v_exp_f32 v29, v29\n"
    "v_mfma_f32_16x16x32_bf16 v[8:11], a[128:131], v[160:163], v[72:75]\n"
    "v_exp_f32 v30, v30\n"
    "v_mfma_f32_16x16x32_bf16 v[8:11], a[132:135], v[164:167], v[8:11]\n"
    "v_exp_f32 v31, v31\n"
    "v_mfma_f32_16x16x32_bf16 v[8:11], a[136:139], v[168:171], v[8:11]\n"
    "v_cvt_pk_bf16_f32 v42, v24, v25\n"
    "v_cvt_pk_bf16_f32 v43, v26, v27\n"
    "v_mfma_f32_16x16x32_bf16 v[8:11], a[140:143], v[172:175], v[8:11]\n"
    "buffer_load_dwordx4 v208, s[68:71], s72 offen offset:2048 nt lds\n"
    "v_mfma_f32_16x16x32_bf16 v[12:15], a[140:143], v[188:191], v[76:79]\n"
    "v_cvt_pk_bf16_f32 v46, v28, v29\n"
    "v_cvt_pk_bf16_f32 v47, v30, v31\n"
    "v_mfma_f32_16x16x32_bf16 v[12:15], a[136:139], v[184:187], v[12:15]\n"
    "v_add_f32 v83, v83, v28\n"
    "v_add_f32 v87, v87, v29\n"
    "v_mfma_f32_16x16x32_bf16 v[12:15], a[132:135], v[180:183], v[12:15]\n"
    "v_add_f32 v83, v83, v30\n"
    "v_add_f32 v87, v87, v31\n"
    "v_mfma_f32_16x16x32_bf16 v[12:15], a[128:131], v[176:179], v[12:15]\n"
    "s_waitcnt lgkmcnt(4)\n"
    "v_mfma_f32_16x16x32_bf16 a[0:3], a[160:163], v[32:35], a[0:3]\n"
    "ds_read_b64_tr_b16 a[176:177], v212 offset:1024\n"
    "ds_read_b64_tr_b16 a[178:179], v212 offset:5120\n"
    "v_mfma_f32_16x16x32_bf16 a[4:7], a[160:163], v[36:39], a[4:7]\n"
    "s_add_u32 s75, s75, 16384\n"
    "v_exp_f32 v0, v0\n"
    "v_mfma_f32_16x16x32_bf16 a[8:11], a[160:163], v[40:43], a[8:11]\n"
    "s_cmp_ge_u32 s75, 98304\n"
    "v_exp_f32 v1, v1\n"
    "v_mfma_f32_16x16x32_bf16 a[12:15], a[160:163], v[44:47], a[12:15]\n"
    "s_cselect_b32 s75, 49152, s75\n"
    "ds_read_b64_tr_b16 a[180:181], v213 offset:1024\n"
    "ds_read_b64_tr_b16 a[182:183], v213 offset:5120\n"
    "v_mfma_f32_16x16x32_bf16 a[28:31], a[164:167], v[44:47], a[28:31]\n"
    "v_add_u32 v210, s75, %14\n"
    "v_add_u32 v211, s75, %15\n"
    "v_mfma_f32_16x16x32_bf16 a[24:27], a[164:167], v[40:43], a[24:27]\n"
    "v_exp_f32 v2, v2\n"
    "v_mfma_f32_16x16x32_bf16 a[20:23], a[164:167], v[36:39], a[20:23]\n"
    "ds_read_b64_tr_b16 a[184:185], v212 offset:1536\n"
    "ds_read_b64_tr_b16 a[186:187], v212 offset:5632\n"
    "v_mfma_f32_16x16x32_bf16 a[16:19], a[164:167], v[32:35], a[16:19]\n"
    "v_exp_f32 v3, v3\n"
    "v_mfma_f32_16x16x32_bf16 a[32:35], a[168:171], v[32:35], a[32:35]\n"
    "v_exp_f32 v4, v4\n"
    "v_mfma_f32_16x16x32_bf16 a[36:39], a[168:171], v[36:39], a[36:39]\n"
    "ds_read_b64_tr_b16 a[188:189], v213 offset:1536\n"
    "ds_read_b64_tr_b16 a[190:191], v213 offset:5632\n"
    "v_mfma_f32_16x16x32_bf16 a[40:43], a[168:171], v[40:43], a[40:43]\n"
    "v_exp_f32 v5, v5\n"
    "v_mfma_f32_16x16x32_bf16 a[44:47], a[168:171], v[44:47], a[44:47]\n"
    "v_exp_f32 v6, v6\n"
    "v_mfma_f32_16x16x32_bf16 a[60:63], a[172:175], v[44:47], a[60:63]\n"
    "buffer_load_dwordx4 v209, s[68:71], s72 offen offset:3072 nt lds\n"
    "v_mfma_f32_16x16x32_bf16 a[56:59], a[172:175], v[40:43], a[56:59]\n"
    "s_add_u32 s72, s72, 16384\n"
    "v_exp_f32 v7, v7\n"
    "v_mfma_f32_16x16x32_bf16 a[52:55], a[172:175], v[36:39], a[52:55]\n"
    "v_mfma_f32_16x16x32_bf16 a[48:51], a[172:175], v[32:35], a[48:51]\n"
    "buffer_load_dwordx4 v[216:219], %10, s[84:87], s81 offen\n"
    "v_mfma_f32_16x16x32_bf16 v[108:111], v[124:127], v[32:35], v[108:111]\n"
    "v_mfma_f32_16x16x32_bf16 v[112:115], v[124:127], v[36:39], v[112:115]\n"
    "v_cvt_pk_bf16_f32 v48, v0, v1\n"
    "v_cvt_pk_bf16_f32 v49, v2, v3\n"
    "v_mfma_f32_16x16x32_bf16 v[116:119], v[124:127], v[40:43], v[116:119]\n"
    "s_waitcnt lgkmcnt(8)\n"
    "v_cvt_pk_bf16_f32 v52, v4, v5\n"
    "v_cvt_pk_bf16_f32 v53, v6, v7\n"
    "v_mfma_f32_16x16x32_bf16 v[16:19], a[144:147], v[128:131], v[64:67]\n"
    "s_waitcnt vmcnt(10)\n"
    "v_exp_f32 v8, v8\n"
    "v_mfma_f32_16x16x32_bf16 v[16:19], a[148:151], v[132:135], v[16:19]\n"
    "s_barrier\n"
    "v_exp_f32 v9, v9\n"
    "v_mfma_f32_16x16x32_bf16 v[16:19], a[152:155], v[136:139], v[16:19]\n"
    "s_sub_u32 s78, s78, 1\n"
    "ds_read_b128 a[128:131], v210 offset:0\n"
    "ds_read_b128 a[132:135], v211 offset:0\n"
    "v_mfma_f32_16x16x32_bf16 v[16:19], a[156:159], v[140:143], v[16:19]\n"
    "s_cmp_gt_i32 s78, 0\n"
    "v_exp_f32 v10, v10\n"
    "v_mfma_f32_16x16x32_bf16 v[20:23], a[156:159], v[156:159], v[68:71]\n"
    "s_cselect_b32 s66, s66, 0\n"
    "v_exp_f32 v11, v11\n"
    "v_mfma_f32_16x16x32_bf16 v[20:23], a[152:155], v[152:155], v[20:23]\n"
    "s_cselect_b32 s70, s70, 0\n"
    "ds_read_b128 a[136:139], v210 offset:1024\n"
    "ds_read_b128 a[140:143], v211 offset:1024\n"
    "v_mfma_f32_16x16x32_bf16 v[20:23], a[148:151], v[148:151], v[20:23]\n"
    "v_exp_f32 v12, v12\n"
    "v_mfma_f32_16x16x32_bf16 v[20:23], a[144:147], v[144:147], v[20:23]\n"
    "v_exp_f32 v13, v13\n"
    "v_mfma_f32_16x16x32_bf16 v[24:27], a[144:147], v[160:163], v[72:75]\n"
    "v_exp_f32 v14, v14\n"
    "v_mfma_f32_16x16x32_bf16 v[24:27], a[148:151], v[164:167], v[24:27]\n"
    "v_exp_f32 v15, v15\n"
    "v_mfma_f32_16x16x32_bf16 v[24:27], a[152:155], v[168:171], v[24:27]\n"
    "s_add_u32 m0, s73, s79\n"
    "v_cvt_pk_bf16_f32 v56, v8, v9\n"
    "v_cvt_pk_bf16_f32 v57, v10, v11\n"
    "v_mfma_f32_16x16x32_bf16 v[24:27], a[156:159], v[172:175], v[24:27]\n"
    "buffer_load_dwordx4 v204, s[64:67], s72 offen nt lds\n"
    "v_mfma_f32_16x16x32_bf16 v[28:31], a[156:159], v[188:191], v[76:79]\n"
    "v_cvt_pk_bf16_f32 v60, v12, v13\n"
    "v_cvt_pk_bf16_f32 v61, v14, v15\n"
    "v_mfma_f32_16x16x32_bf16 v[28:31], a[152:155], v[184:187], v[28:31]\n"
    "v_add_f32 v83, v83, v12\n"
    "v_add_f32 v87, v87, v13\n"
    "v_mfma_f32_16x16x32_bf16 v[28:31], a[148:151], v[180:183], v[28:31]\n"
    "v_add_f32 v83, v83, v14\n"
    "v_add_f32 v87, v87, v15\n"
    "v_mfma_f32_16x16x32_bf16 v[28:31], a[144:147], v[176:179], v[28:31]\n"
    "s_waitcnt lgkmcnt(4)\n"
    "v_mfma_f32_16x16x32_bf16 a[64:67], a[176:179], v[32:35], a[64:67]\n"
    "s_add_u32 s81, s81, 1024\n"
    "ds_read_b64_tr_b16 a[160:161], v212 offset:8192\n"
    "ds_read_b64_tr_b16 a[162:163], v212 offset:12288\n"
    "v_mfma_f32_16x16x32_bf16 a[68:71], a[176:179], v[36:39], a[68:71]\n"
    "s_cmp_eq_u32 s81, s86\n"
    "v_exp_f32 v16, v16\n"
    "v_mfma_f32_16x16x32_bf16 a[72:75], a[176:179], v[40:43], a[72:75]\n"
    "s_cselect_b32 s84, s88, s84\n"
    "v_exp_f32 v17, v17\n"
    "v_mfma_f32_16x16x32_bf16 a[76:79], a[176:179], v[44:47], a[76:79]\n"
    "s_cselect_b32 s85, s89, s85\n"
    "ds_read_b64_tr_b16 a[164:165], v213 offset:8192\n"
    "ds_read_b64_tr_b16 a[166:167], v213 offset:12288\n"
    "v_mfma_f32_16x16x32_bf16 a[92:95], a[180:183], v[44:47], a[92:95]\n"
    "s_cselect_b32 s86, s90, s86\n"
    "v_exp_f32 v18, v18\n"
    "v_mfma_f32_16x16x32_bf16 a[88:91], a[180:183], v[40:43], a[88:91]\n"
    "s_cselect_b32 s81, s91, s81\n"
    "v_exp_f32 v19, v19\n"
    "v_mfma_f32_16x16x32_bf16 a[84:87], a[180:183], v[36:39], a[84:87]\n"
    "ds_read_b64_tr_b16 a[168:169], v212 offset:8704\n"
    "ds_read_b64_tr_b16 a[170:171], v212 offset:12800\n"
    "v_mfma_f32_16x16x32_bf16 a[80:83], a[180:183], v[32:35], a[80:83]\n"
    "v_exp_f32 v20, v20\n"
    "v_mfma_f32_16x16x32_bf16 a[96:99], a[184:187], v[32:35], a[96:99]\n"
    "v_exp_f32 v21, v21\n"
    "v_mfma_f32_16x16x32_bf16 a[100:103], a[184:187], v[36:39], a[100:103]\n"
    "ds_read_b64_tr_b16 a[172:173], v213 offset:8704\n"
    "ds_read_b64_tr_b16 a[174:175], v213 offset:12800\n"
    "v_mfma_f32_16x16x32_bf16 a[104:107], a[184:187], v[40:43], a[104:107]\n"
    "v_exp_f32 v22, v22\n"
    "v_mfma_f32_16x16x32_bf16 a[108:111], a[184:187], v[44:47], a[108:111]\n"
    "v_exp_f32 v23, v23\n"
    "v_mfma_f32_16x16x32_bf16 a[124:127], a[188:191], v[44:47], a[124:127]\n"
    "buffer_load_dwordx4 v205, s[64:67], s72 offen offset:1024 nt lds\n"
    "v_mfma_f32_16x16x32_bf16 a[120:123], a[188:191], v[40:43], a[120:123]\n"
    "v_mfma_f32_16x16x32_bf16 a[116:119], a[188:191], v[36:39], a[116:119]\n"
    "v_cvt_pk_bf16_f32 v50, v16, v17\n"
    "v_cvt_pk_bf16_f32 v51, v18, v19\n"
    "v_mfma_f32_16x16x32_bf16 a[112:115], a[188:191], v[32:35], a[112:115]\n"
    "s_waitcnt lgkmcnt(8)\n"
    "v_cvt_pk_bf16_f32 v54, v20, v21\n"
    "v_cvt_pk_bf16_f32 v55, v22, v23\n"
    "v_mfma_f32_16x16x32_bf16 v[0:3], a[128:131], v[128:131], v[64:67]\n"
    "v_exp_f32 v24, v24\n"
    "v_mfma_f32_16x16x32_bf16 v[0:3], a[132:135], v[132:135], v[0:3]\n"
    "s_add_u32 s74, s74, 16384\n"
    "ds_read_b128 a[144:147], v210 offset:4096\n"
    "ds_read_b128 a[148:151], v211 offset:4096\n"
    "v_mfma_f32_16x16x32_bf16 v[0:3], a[136:139], v[136:139], v[0:3]\n"
    "s_cmp_ge_u32 s74, 163840\n"
    "v_exp_f32 v25, v25\n"
    "v_mfma_f32_16x16x32_bf16 v[0:3], a[140:143], v[140:143], v[0:3]\n"
    "s_cselect_b32 s74, 98304, s74\n"
    "v_exp_f32 v26, v26\n"
    "v_mfma_f32_16x16x32_bf16 v[4:7], a[140:143], v[156:159], v[68:71]\n"
    "v_exp_f32 v27, v27\n"
    "v_mfma_f32_16x16x32_bf16 v[4:7], a[136:139], v[152:155], v[4:7]\n"
    "ds_read_b128 a[152:155], v210 offset:5120\n"
    "ds_read_b128 a[156:159], v211 offset:5120\n"
    "v_mfma_f32_16x16x32_bf16 v[4:7], a[132:135], v[148:151], v[4:7]\n"
    "v_exp_f32 v28, v28\n"
    "v_mfma_f32_16x16x32_bf16 v[4:7], a[128:131], v[144:147], v[4:7]\n"
    "v_exp_f32 v29, v29\n"
    "v_mfma_f32_16x16x32_bf16 v[8:11], a[128:131], v[160:163], v[72:75]\n"
    "v_exp_f32 v30, v30\n"
    "v_mfma_f32_16x16x32_bf16 v[8:11], a[132:135], v[164:167], v[8:11]\n"
    "v_exp_f32 v31, v31\n"
    "v_mfma_f32_16x16x32_bf16 v[8:11], a[136:139], v[168:171], v[8:11]\n"
    "v_cvt_pk_bf16_f32 v58, v24, v25\n"
    "v_cvt_pk_bf16_f32 v59, v26, v27\n"
    "v_mfma_f32_16x16x32_bf16 v[8:11], a[140:143], v[172:175], v[8:11]\n"
    "buffer_load_dwordx4 v206, s[64:67], s72 offen offset:2048 nt lds\n"
    "v_mfma_f32_16x16x32_bf16 v[12:15], a[140:143], v[188:191], v[76:79]\n"
    "v_cvt_pk_bf16_f32 v62, v28, v29\n"
    "v_cvt_pk_bf16_f32 v63, v30, v31\n"
    "v_mfma_f32_16x16x32_bf16 v[12:15], a[136:139], v[184:187], v[12:15]\n"
    "v_add_f32 v83, v83, v28\n"
    "v_add_f32 v87, v87, v29\n"
    "v_mfma_f32_16x16x32_bf16 v[12:15], a[132:135], v[180:183], v[12:15]\n"
    "v_add_f32 v83, v83, v30\n"
    "v_add_f32 v87, v87, v31\n"
    "v_mfma_f32_16x16x32_bf16 v[12:15], a[128:131], v[176:179], v[12:15]\n"
    "s_waitcnt lgkmcnt(4)\n"
    "v_mfma_f32_16x16x32_bf16 a[0:3], a[160:163], v[48:51], a[0:3]\n"
    "ds_read_b64_tr_b16 a[176:177], v212 offset:9216\n"
    "ds_read_b64_tr_b16 a[178:179], v212 offset:13312\n"
    "v_mfma_f32_16x16x32_bf16 a[4:7], a[160:163], v[52:55], a[4:7]\n"
    "s_add_u32 s76, s76, 16384\n"
    "v_exp_f32 v0, v0\n"
    "v_mfma_f32_16x16x32_bf16 a[8:11], a[160:163], v[56:59], a[8:11]\n"
    "s_cmp_ge_u32 s76, 163840\n"
    "v_exp_f32 v1, v1\n"
    "v_mfma_f32_16x16x32_bf16 a[12:15], a[160:163], v[60:63], a[12:15]\n"
    "s_cselect_b32 s76, 98304, s76\n"
    "ds_read_b64_tr_b16 a[180:181], v213 offset:9216\n"
    "ds_read_b64_tr_b16 a[182:183], v213 offset:13312\n"
    "v_mfma_f32_16x16x32_bf16 a[28:31], a[164:167], v[60:63], a[28:31]\n"
    "v_exp_f32 v2, v2\n"
    "v_mfma_f32_16x16x32_bf16 a[24:27], a[164:167], v[56:59], a[24:27]\n"
    "v_exp_f32 v3, v3\n"
    "v_mfma_f32_16x16x32_bf16 a[20:23], a[164:167], v[52:55], a[20:23]\n"
    "ds_read_b64_tr_b16 a[184:185], v212 offset:9728\n"
    "ds_read_b64_tr_b16 a[186:187], v212 offset:13824\n"
    "v_mfma_f32_16x16x32_bf16 a[16:19], a[164:167], v[48:51], a[16:19]\n"
    "v_exp_f32 v4, v4\n"
    "v_mfma_f32_16x16x32_bf16 a[32:35], a[168:171], v[48:51], a[32:35]\n"
    "v_exp_f32 v5, v5\n"
    "v_mfma_f32_16x16x32_bf16 a[36:39], a[168:171], v[52:55], a[36:39]\n"
    "ds_read_b64_tr_b16 a[188:189], v213 offset:9728\n"
    "ds_read_b64_tr_b16 a[190:191], v213 offset:13824\n"
    "v_mfma_f32_16x16x32_bf16 a[40:43], a[168:171], v[56:59], a[40:43]\n"
    "v_exp_f32 v6, v6\n"
    "v_mfma_f32_16x16x32_bf16 a[44:47], a[168:171], v[60:63], a[44:47]\n"
    "v_exp_f32 v7, v7\n"
    "v_mfma_f32_16x16x32_bf16 a[60:63], a[172:175], v[60:63], a[60:63]\n"
    "buffer_load_dwordx4 v207, s[64:67], s72 offen offset:3072 nt lds\n"
    "v_mfma_f32_16x16x32_bf16 a[56:59], a[172:175], v[56:59], a[56:59]\n"
    "s_mov_b32 m0, s96\n"
    "v_mfma_f32_16x16x32_bf16 a[52:55], a[172:175], v[52:55], a[52:55]\n"
    "buffer_load_dwordx4 %24, s[92:95], s82 offen lds\n"
    "v_mfma_f32_16x16x32_bf16 a[48:51], a[172:175], v[48:51], a[48:51]\n"
    "v_add_u32 v212, s76, %11\n"
    "v_add_u32 v213, s76, %12\n"
    "v_mfma_f32_16x16x32_bf16 v[108:111], v[124:127], v[48:51], v[108:111]\n"
    "v_mfma_f32_16x16x32_bf16 v[112:115], v[124:127], v[52:55], v[112:115]\n"
    "v_cvt_pk_bf16_f32 v32, v0, v1\n"
    "v_cvt_pk_bf16_f32 v33, v2, v3\n"
    "v_mfma_f32_16x16x32_bf16 v[116:119], v[124:127], v[56:59], v[116:119]\n"
    "s_waitcnt lgkmcnt(8)\n"
    "v_cvt_pk_bf16_f32 v36, v4, v5\n"
    "v_cvt_pk_bf16_f32 v37, v6, v7\n"
    "v_mfma_f32_16x16x32_bf16 v[16:19], a[144:147], v[128:131], v[64:67]\n"
    "v_exp_f32 v8, v8\n"
    "v_mfma_f32_16x16x32_bf16 v[16:19], a[148:151], v[132:135], v[16:19]\n"
    "ds_read_b128 a[128:131], v210 offset:8192\n"
    "ds_read_b128 a[132:135], v211 offset:8192\n"
    "v_mfma_f32_16x16x32_bf16 v[16:19], a[152:155], v[136:139], v[16:19]\n"
    "v_exp_f32 v9, v9\n"
    "v_mfma_f32_16x16x32_bf16 v[16:19], a[156:159], v[140:143], v[16:19]\n"
    "v_exp_f32 v10, v10\n"
    "v_mfma_f32_16x16x32_bf16 v[20:23], a[156:159], v[156:159], v[68:71]\n"
    "v_exp_f32 v11, v11\n"
    "v_mfma_f32_16x16x32_bf16 v[20:23], a[152:155], v[152:155], v[20:23]\n"
    "s_add_u32 s73, s73, 16384\n"
    "ds_read_b128 a[136:139], v210 offset:9216\n"
    "ds_read_b128 a[140:143], v211 offset:9216\n"
    "v_mfma_f32_16x16x32_bf16 v[20:23], a[148:151], v[148:151], v[20:23]\n"
    "s_cmp_ge_u32 s73, 98304\n"
    "v_exp_f32 v12, v12\n"
    "v_mfma_f32_16x16x32_bf16 v[20:23], a[144:147], v[144:147], v[20:23]\n"
    "s_cselect_b32 s73, 49152, s73\n"
    "v_exp_f32 v13, v13\n"
    "v_mfma_f32_16x16x32_bf16 v[24:27], a[144:147], v[160:163], v[72:75]\n"
    "v_exp_f32 v14, v14\n"
    "v_mfma_f32_16x16x32_bf16 v[24:27], a[148:151], v[164:167], v[24:27]\n"
    "v_exp_f32 v15, v15\n"
    "v_mfma_f32_16x16x32_bf16 v[24:27], a[152:155], v[168:171], v[24:27]\n"
    "s_add_u32 m0, s74, s79\n"
    "v_cvt_pk_bf16_f32 v40, v8, v9\n"
    "v_cvt_pk_bf16_f32 v41, v10, v11\n"
    "v_mfma_f32_16x16x32_bf16 v[24:27], a[156:159], v[172:175], v[24:27]\n"
    "buffer_load_dwordx4 v208, s[68:71], s72 offen nt lds\n"
    "v_mfma_f32_16x16x32_bf16 v[28:31], a[156:159], v[188:191], v[76:79]\n"
    "v_cvt_pk_bf16_f32 v44, v12, v13\n"
    "v_cvt_pk_bf16_f32 v45, v14, v15\n"
    "v_mfma_f32_16x16x32_bf16 v[28:31], a[152:155], v[184:187], v[28:31]\n"
    "v_add_f32 v83, v83, v12\n"
    "v_add_f32 v87, v87, v13\n"
    "v_mfma_f32_16x16x32_bf16 v[28:31], a[148:151], v[180:183], v[28:31]\n"
    "v_add_f32 v83, v83, v14\n"
    "v_add_f32 v87, v87, v15\n"
    "v_mfma_f32_16x16x32_bf16 v[28:31], a[144:147], v[176:179], v[28:31]\n"
    "s_waitcnt lgkmcnt(4)\n"
    "v_mfma_f32_16x16x32_bf16 a[64:67], a[176:179], v[48:51], a[64:67]\n"
    "s_add_u32 s82, s82, 12288\n"
    "ds_read_b64_tr_b16 a[160:161], v212 offset:0\n"
    "ds_read_b64_tr_b16 a[162:163], v212 offset:4096\n"
    "v_mfma_f32_16x16x32_bf16 a[68:71], a[176:179], v[52:55], a[68:71]\n"
    "s_add_u32 s96, s96, 1024\n"
    "v_exp_f32 v16, v16\n"
    "v_mfma_f32_16x16x32_bf16 a[72:75], a[176:179], v[56:59], a[72:75]\n"
    "s_cmp_ge_u32 s82, 98304\n"
    "v_exp_f32 v17, v17\n"
    "v_mfma_f32_16x16x32_bf16 a[76:79], a[176:179], v[60:63], a[76:79]\n"
    "s_cselect_b32 s96, s97, s96\n"
    "ds_read_b64_tr_b16 a[164:165], v213 offset:0\n"
    "ds_read_b64_tr_b16 a[166:167], v213 offset:4096\n"
    "v_mfma_f32_16x16x32_bf16 a[92:95], a[180:183], v[60:63], a[92:95]\n"
    "s_cselect_b32 s94, 0, s94\n"
    "v_exp_f32 v18, v18\n"
    "v_mfma_f32_16x16x32_bf16 a[88:91], a[180:183], v[56:59], a[88:91]\n"
    "v_exp_f32 v19, v19\n"
    "v_mfma_f32_16x16x32_bf16 a[84:87], a[180:183], v[52:55], a[84:87]\n"
    "ds_read_b64_tr_b16 a[168:169], v212 offset:512\n"
    "ds_read_b64_tr_b16 a[170:171], v212 offset:4608\n"
    "v_mfma_f32_16x16x32_bf16 a[80:83], a[180:183], v[48:51], a[80:83]\n"
    "v_exp_f32 v20, v20\n"
    "v_mfma_f32_16x16x32_bf16 a[96:99], a[184:187], v[48:51], a[96:99]\n"
    "v_exp_f32 v21, v21\n"
    "v_mfma_f32_16x16x32_bf16 a[100:103], a[184:187], v[52:55], a[100:103]\n"
    "ds_read_b64_tr_b16 a[172:173], v213 offset:512\n"
    "ds_read_b64_tr_b16 a[174:175], v213 offset:4608\n"
    "v_mfma_f32_16x16x32_bf16 a[104:107], a[184:187], v[56:59], a[104:107]\n"
    "v_exp_f32 v22, v22\n"
    "v_mfma_f32_16x16x32_bf16 a[108:111], a[184:187], v[60:63], a[108:111]\n"
    "v_exp_f32 v23, v23\n"
    "v_mfma_f32_16x16x32_bf16 a[124:127], a[188:191], v[60:63], a[124:127]\n"
    "buffer_load_dwordx4 v209, s[68:71], s72 offen offset:1024 nt lds\n"
    "v_mfma_f32_16x16x32_bf16 a[120:123], a[188:191], v[56:59], a[120:123]\n"
    "v_mfma_f32_16x16x32_bf16 a[116:119], a[188:191], v[52:55], a[116:119]\n"
    "v_cvt_pk_bf16_f32 v34, v16, v17\n"
    "v_cvt_pk_bf16_f32 v35, v18, v19\n"
    "v_mfma_f32_16x16x32_bf16 a[112:115], a[188:191], v[48:51], a[112:115]\n"
    "s_waitcnt lgkmcnt(8)\n"
    "v_cvt_pk_bf16_f32 v38, v20, v21\n"
    "v_cvt_pk_bf16_f32 v39, v22, v23\n"
    "v_mfma_f32_16x16x32_bf16 v[0:3], a[128:131], v[128:131], v[64:67]\n"
    "v_exp_f32 v24, v24\n"
    "v_mfma_f32_16x16x32_bf16 v[0:3], a[132:135], v[132:135], v[0:3]\n"
    "ds_read_b128 a[144:147], v210 offset:12288\n"
    "ds_read_b128 a[148:151], v211 offset:12288\n"
    "v_mfma_f32_16x16x32_bf16 v[0:3], a[136:139], v[136:139], v[0:3]\n"
    "v_exp_f32 v25, v25\n"
    "v_mfma_f32_16x16x32_bf16 v[0:3], a[140:143], v[140:143], v[0:3]\n"
    "v_exp_f32 v26, v26\n"
    "v_mfma_f32_16x16x32_bf16 v[4:7], a[140:143], v[156:159], v[68:71]\n"
    "v_exp_f32 v27, v27\n"
    "v_mfma_f32_16x16x32_bf16 v[4:7], a[136:139], v[152:155], v[4:7]\n"
    "ds_read_b128 a[152:155], v210 offset:13312\n"
    "ds_read_b128 a[156:159], v211 offset:13312\n"
    "v_mfma_f32_16x16x32_bf16 v[4:7], a[132:135], v[148:151], v[4:7]\n"
    "v_exp_f32 v28, v28\n"
    "v_mfma_f32_16x16x32_bf16 v[4:7], a[128:131], v[144:147], v[4:7]\n"
    "v_exp_f32 v29, v29\n"
    "v_mfma_f32_16x16x32_bf16 v[8:11], a[128:131], v[160:163], v[72:75]\n"
    "v_exp_f32 v30, v30\n"
    "v_mfma_f32_16x16x32_bf16 v[8:11], a[132:135], v[164:167], v[8:11]\n"
    "v_exp_f32 v31, v31\n"
    "v_mfma_f32_16x16x32_bf16 v[8:11], a[136:139], v[168:171], v[8:11]\n"
    "v_cvt_pk_bf16_f32 v42, v24, v25\n"
    "v_cvt_pk_bf16_f32 v43, v26, v27\n"
    "v_mfma_f32_16x16x32_bf16 v[8:11], a[140:143], v[172:175], v[8:11]\n"
    "buffer_load_dwordx4 v208, s[68:71], s72 offen offset:2048 nt lds\n"
    "v_mfma_f32_16x16x32_bf16 v[12:15], a[140:143], v[188:191], v[76:79]\n"
    "v_cvt_pk_bf16_f32 v46, v28, v29\n"
    "v_cvt_pk_bf16_f32 v47, v30, v31\n"
    "v_mfma_f32_16x16x32_bf16 v[12:15], a[136:139], v[184:187], v[12:15]\n"
    "v_add_f32 v83, v83, v28\n"
    "v_add_f32 v87, v87, v29\n"
    "v_mfma_f32_16x16x32_bf16 v[12:15], a[132:135], v[180:183], v[12:15]\n"
    "v_add_f32 v83, v83, v30\n"
    "v_add_f32 v87, v87, v31\n"
    "v_mfma_f32_16x16x32_bf16 v[12:15], a[128:131], v[176:179], v[12:15]\n"
    "s_waitcnt lgkmcnt(4)\n"
    "v_mfma_f32_16x16x32_bf16 a[0:3], a[160:163], v[32:35], a[0:3]\n"
    "ds_read_b64_tr_b16 a[176:177], v212 offset:1024\n"
    "ds_read_b64_tr_b16 a[178:179], v212 offset:5120\n"
    "v_mfma_f32_16x16x32_bf16 a[4:7], a[160:163], v[36:39], a[4:7]\n"
    "s_add_u32 s75, s75, 16384\n"
    "v_exp_f32 v0, v0\n"
    "v_mfma_f32_16x16x32_bf16 a[8:11], a[160:163], v[40:43], a[8:11]\n"
    "s_cmp_ge_u32 s75, 98304\n"
    "v_exp_f32 v1, v1\n"
    "v_mfma_f32_16x16x32_bf16 a[12:15], a[160:163], v[44:47], a[12:15]\n"
    "s_cselect_b32 s75, 49152, s75\n"
    "ds_read_b64_tr_b16 a[180:181], v213 offset:1024\n"
    "ds_read_b64_tr_b16 a[182:183], v213 offset:5120\n"
    "v_mfma_f32_16x16x32_bf16 a[28:31], a[164:167], v[44:47], a[28:31]\n"
    "v_add_u32 v210, s75, %14\n"
    "v_add_u32 v211, s75, %15\n"
    "v_mfma_f32_16x16x32_bf16 a[24:27], a[164:167], v[40:43], a[24:27]\n"
    "v_exp_f32 v2, v2\n"
    "v_mfma_f32_16x16x32_bf16 a[20:23], a[164:167], v[36:39], a[20:23]\n"
    "ds_read_b64_tr_b16 a[184:185], v212 offset:1536\n"
    "ds_read_b64_tr_b16 a[186:187], v212 offset:5632\n"
    "v_mfma_f32_16x16x32_bf16 a[16:19], a[164:167], v[32:35], a[16:19]\n"
    "v_exp_f32 v3, v3\n"
    "v_mfma_f32_16x16x32_bf16 a[32:35], a[168:171], v[32:35], a[32:35]\n"
    "v_exp_f32 v4, v4\n"
    "v_mfma_f32_16x16x32_bf16 a[36:39], a[168:171], v[36:39], a[36:39]\n"
    "ds_read_b64_tr_b16 a[188:189], v213 offset:1536\n"
    "ds_read_b64_tr_b16 a[190:191], v213 offset:5632\n"
    "v_mfma_f32_16x16x32_bf16 a[40:43], a[168:171], v[40:43], a[40:43]\n"
    "v_exp_f32 v5, v5\n"
    "v_mfma_f32_16x16x32_bf16 a[44:47], a[168:171], v[44:47], a[44:47]\n"
    "v_exp_f32 v6, v6\n"
    "v_mfma_f32_16x16x32_bf16 a[60:63], a[172:175], v[44:47], a[60:63]\n"
    "buffer_load_dwordx4 v209, s[68:71], s72 offen offset:3072 nt lds\n"
    "v_mfma_f32_16x16x32_bf16 a[56:59], a[172:175], v[40:43], a[56:59]\n"
    "s_add_u32 s72, s72, 16384\n"
    "v_exp_f32 v7, v7\n"
    "v_mfma_f32_16x16x32_bf16 a[52:55], a[172:175], v[36:39], a[52:55]\n"
    "v_mfma_f32_16x16x32_bf16 a[48:51], a[172:175], v[32:35], a[48:51]\n"
    "buffer_load_dwordx4 v[216:219], %10, s[84:87], s81 offen\n"
    "v_mfma_f32_16x16x32_bf16 v[108:111], v[124:127], v[32:35], v[108:111]\n"
    "v_mfma_f32_16x16x32_bf16 v[112:115], v[124:127], v[36:39], v[112:115]\n"
    "v_cvt_pk_bf16_f32 v48, v0, v1\n"
    "v_cvt_pk_bf16_f32 v49, v2, v3\n"
    "v_mfma_f32_16x16x32_bf16 v[116:119], v[124:127], v[40:43], v[116:119]\n"
    "s_waitcnt lgkmcnt(8)\n"
    "v_cvt_pk_bf16_f32 v52, v4, v5\n"
    "v_cvt_pk_bf16_f32 v53, v6, v7\n"
    "v_mfma_f32_16x16x32_bf16 v[16:19], a[144:147], v[128:131], v[64:67]\n"
    "s_waitcnt vmcnt(10)\n"
    "v_exp_f32 v8, v8\n"
    "v_mfma_f32_16x16x32_bf16 v[16:19], a[148:151], v[132:135], v[16:19]\n"
    "s_barrier\n"
    "v_exp_f32 v9, v9\n"
    "v_mfma_f32_16x16x32_bf16 v[16:19], a[152:155], v[136:139], v[16:19]\n"
    "s_sub_u32 s78, s78, 1\n"
    "ds_read_b128 a[128:131], v210 offset:0\n"
    "ds_read_b128 a[132:135], v211 offset:0\n"
    "v_mfma_f32_16x16x32_bf16 v[16:19], a[156:159], v[140:143], v[16:19]\n"
    "s_cmp_gt_i32 s78, 0\n"
    "v_exp_f32 v10, v10\n"
    "v_mfma_f32_16x16x32_bf16 v[20:23], a[156:159], v[156:159], v[68:71]\n"
    "s_cselect_b32 s66, s66, 0\n"
    "v_exp_f32 v11, v11\n"
    "v_mfma_f32_16x16x32_bf16 v[20:23], a[152:155], v[152:155], v[20:23]\n"
    "s_cselect_b32 s70, s70, 0\n"
    "ds_read_b128 a[136:139], v210 offset:1024\n"
    "ds_read_b128 a[140:143], v211 offset:1024\n"
    "v_mfma_f32_16x16x32_bf16 v[20:23], a[148:151], v[148:151], v[20:23]\n"
    "v_exp_f32 v12, v12\n"
    "v_mfma_f32_16x16x32_bf16 v[20:23], a[144:147], v[144:147], v[20:23]\n"
    "v_exp_f32 v13, v13\n"
    "v_mfma_f32_16x16x32_bf16 v[24:27], a[144:147], v[160:163], v[72:75]\n"
    "v_exp_f32 v14, v14\n"
    "v_mfma_f32_16x16x32_bf16 v[24:27], a[148:151], v[164:167], v[24:27]\n"
    "v_exp_f32 v15, v15\n"
    "v_mfma_f32_16x16x32_bf16 v[24:27], a[152:155], v[168:171], v[24:27]\n"
    "s_add_u32 m0, s73, s79\n"
    "v_cvt_pk_bf16_f32 v56, v8, v9\n"
    "v_cvt_pk_bf16_f32 v57, v10, v11\n"
    "v_mfma_f32_16x16x32_bf16 v[24:27], a[156:159], v[172:175], v[24:27]\n"
    "buffer_load_dwordx4 v204, s[64:67], s72 offen nt lds\n"
    "v_mfma_f32_16x16x32_bf16 v[28:31], a[156:159], v[188:191], v[76:79]\n"
    "v_cvt_pk_bf16_f32 v60, v12, v13\n"
    "v_cvt_pk_bf16_f32 v61, v14, v15\n"
    "v_mfma_f32_16x16x32_bf16 v[28:31], a[152:155], v[184:187], v[28:31]\n"
    "v_add_f32 v83, v83, v12\n"
    "v_add_f32 v87, v87, v13\n"
    "v_mfma_f32_16x16x32_bf16 v[28:31], a[148:151], v[180:183], v[28:31]\n"
    "v_add_f32 v83, v83, v14\n"
    "v_add_f32 v87, v87, v15\n"
    "v_mfma_f32_16x16x32_bf16 v[28:31], a[144:147], v[176:179], v[28:31]\n"
    "s_waitcnt lgkmcnt(4)\n"
    "v_mfma_f32_16x16x32_bf16 a[64:67], a[176:179], v[32:35], a[64:67]\n"
    "s_add_u32 s81, s81, 1024\n"
    "ds_read_b64_tr_b16 a[160:161], v212 offset:8192\n"
    "ds_read_b64_tr_b16 a[162:163], v212 offset:12288\n"
    "v_mfma_f32_16x16x32_bf16 a[68:71], a[176:179], v[36:39], a[68:71]\n"
    "s_cmp_eq_u32 s81, s86\n"
    "v_exp_f32 v16, v16\n"
    "v_mfma_f32_16x16x32_bf16 a[72:75], a[176:179], v[40:43], a[72:75]\n"
    "s_cselect_b32 s84, s88, s84\n"
    "v_exp_f32 v17, v17\n"
    "v_mfma_f32_16x16x32_bf16 a[76:79], a[176:179], v[44:47], a[76:79]\n"
    "s_cselect_b32 s85, s89, s85\n"
    "ds_read_b64_tr_b16 a[164:165], v213 offset:8192\n"
    "ds_read_b64_tr_b16 a[166:167], v213 offset:12288\n"
    "v_mfma_f32_16x16x32_bf16 a[92:95], a[180:183], v[44:47], a[92:95]\n"
    "s_cselect_b32 s86, s90, s86\n"
    "v_exp_f32 v18, v18\n"
    "v_mfma_f32_16x16x32_bf16 a[88:91], a[180:183], v[40:43], a[88:91]\n"
    "s_cselect_b32 s81, s91, s81\n"
    "v_exp_f32 v19, v19\n"
    "v_mfma_f32_16x16x32_bf16 a[84:87], a[180:183], v[36:39], a[84:87]\n"
    "ds_read_b64_tr_b16 a[168:169], v212 offset:8704\n"
    "ds_read_b64_tr_b16 a[170:171], v212 offset:12800\n"
    "v_mfma_f32_16x16x32_bf16 a[80:83], a[180:183], v[32:35], a[80:83]\n"
    "v_exp_f32 v20, v20\n"
    "v_mfma_f32_16x16x32_bf16 a[96:99], a[184:187], v[32:35], a[96:99]\n"
    "v_exp_f32 v21, v21\n"
    "v_mfma_f32_16x16x32_bf16 a[100:103], a[184:187], v[36:39], a[100:103]\n"
    "ds_read_b64_tr_b16 a[172:173], v213 offset:8704\n"
    "ds_read_b64_tr_b16 a[174:175], v213 offset:12800\n"
    "v_mfma_f32_16x16x32_bf16 a[104:107], a[184:187], v[40:43], a[104:107]\n"
    "v_exp_f32 v22, v22\n"
    "v_mfma_f32_16x16x32_bf16 a[108:111], a[184:187], v[44:47], a[108:111]\n"
    "v_exp_f32 v23, v23\n"
    "v_mfma_f32_16x16x32_bf16 a[124:127], a[188:191], v[44:47], a[124:127]\n"
    "buffer_load_dwordx4 v205, s[64:67], s72 offen offset:1024 nt lds\n"
    "v_mfma_f32_16x16x32_bf16 a[120:123], a[188:191], v[40:43], a[120:123]\n"
    "v_mfma_f32_16x16x32_bf16 a[116:119], a[188:191], v[36:39], a[116:119]\n"
    "v_cvt_pk_bf16_f32 v50, v16, v17\n"
    "v_cvt_pk_bf16_f32 v51, v18, v19\n"
    "v_mfma_f32_16x16x32_bf16 a[112:115], a[188:191], v[32:35], a[112:115]\n"
    "s_waitcnt lgkmcnt(8)\n"
    "v_cvt_pk_bf16_f32 v54, v20, v21\n"
    "v_cvt_pk_bf16_f32 v55, v22, v23\n"
    "s_sub_u32 s77, s77, 1\n"
    "s_cmp_lg_u32 s77, 0\n"
    "s_cbranch_scc1 L_attn_loop\n"
    "s_waitcnt lgkmcnt(0)\n"
    "ds_read_b64_tr_b16 a[176:177], v212 offset:9216\n"
    "ds_read_b64_tr_b16 a[178:179], v212 offset:13312\n"
    "ds_read_b64_tr_b16 a[180:181], v213 offset:9216\n"
    "ds_read_b64_tr_b16 a[182:183], v213 offset:13312\n"
    "ds_read_b64_tr_b16 a[184:185], v212 offset:9728\n"
    "ds_read_b64_tr_b16 a[186:187], v212 offset:13824\n"
    "ds_read_b64_tr_b16 a[188:189], v213 offset:9728\n"
    "ds_read_b64_tr_b16 a[190:191], v213 offset:13824\n"
    "v_exp_f32 v24, v24\n"
    "v_exp_f32 v25, v25\n"
    "v_exp_f32 v26, v26\n"
    "v_exp_f32 v27, v27\n"
    "v_exp_f32 v28, v28\n"
    "v_exp_f32 v29, v29\n"
    "v_exp_f32 v30, v30\n"
    "v_exp_f32 v31, v31\n"
    "v_add_f32 v83, v83, v28\n"
    "v_add_f32 v87, v87, v29\n"
    "v_add_f32 v83, v83, v30\n"
    "v_add_f32 v87, v87, v31\n"
    "v_cvt_pk_bf16_f32 v58, v24, v25\n"
    "v_cvt_pk_bf16_f32 v59, v26, v27\n"
    "v_cvt_pk_bf16_f32 v62, v28, v29\n"
    "v_cvt_pk_bf16_f32 v63, v30, v31\n"
    "s_nop 1\n"
    "v_mfma_f32_16x16x32_bf16 a[0:3], a[160:163], v[48:51], a[0:3]\n"
    "v_mfma_f32_16x16x32_bf16 a[4:7], a[160:163], v[52:55], a[4:7]\n"
    "v_mfma_f32_16x16x32_bf16 a[8:11], a[160:163], v[56:59], a[8:11]\n"
    "v_mfma_f32_16x16x32_bf16 a[12:15], a[160:163], v[60:63], a[12:15]\n"
    "v_mfma_f32_16x16x32_bf16 a[28:31], a[164:167], v[60:63], a[28:31]\n"
    "v_mfma_f32_16x16x32_bf16 a[24:27], a[164:167], v[56:59], a[24:27]\n"
    "v_mfma_f32_16x16x32_bf16 a[20:23], a[164:167], v[52:55], a[20:23]\n"
    "v_mfma_f32_16x16x32_bf16 a[16:19], a[164:167], v[48:51], a[16:19]\n"
    "v_mfma_f32_16x16x32_bf16 a[32:35], a[168:171], v[48:51], a[32:35]\n"
    "v_mfma_f32_16x16x32_bf16 a[36:39], a[168:171], v[52:55], a[36:39]\n"
    "v_mfma_f32_16x16x32_bf16 a[40:43], a[168:171], v[56:59], a[40:43]\n"
    "v_mfma_f32_16x16x32_bf16 a[44:47], a[168:171], v[60:63], a[44:47]\n"
    "v_mfma_f32_16x16x32_bf16 a[60:63], a[172:175], v[60:63], a[60:63]\n"
    "v_mfma_f32_16x16x32_bf16 a[56:59], a[172:175], v[56:59], a[56:59]\n"
    "v_mfma_f32_16x16x32_bf16 a[52:55], a[172:175], v[52:55], a[52:55]\n"
    "v_mfma_f32_16x16x32_bf16 a[48:51], a[172:175], v[48:51], a[48:51]\n"
    "v_mfma_f32_16x16x32_bf16 v[108:111], v[124:127], v[48:51], v[108:111]\n"
    "v_mfma_f32_16x16x32_bf16 v[112:115], v[124:127], v[52:55], v[112:115]\n"
    "v_mfma_f32_16x16x32_bf16 v[116:119], v[124:127], v[56:59], v[116:119]\n"
    "s_waitcnt lgkmcnt(0)\n"
    "v_mfma_f32_16x16x32_bf16 a[64:67], a[176:179], v[48:51], a[64:67]\n"
    "v_mfma_f32_16x16x32_bf16 a[68:71], a[176:179], v[52:55], a[68:71]\n"
    "v_mfma_f32_16x16x32_bf16 a[72:75], a[176:179], v[56:59], a[72:75]\n"
    "v_mfma_f32_16x16x32_bf16 a[76:79], a[176:179], v[60:63], a[76:79]\n"
    "v_mfma_f32_16x16x32_bf16 a[92:95], a[180:183], v[60:63], a[92:95]\n"
    "v_mfma_f32_16x16x32_bf16 a[88:91], a[180:183], v[56:59], a[88:91]\n"
    "v_mfma_f32_16x16x32_bf16 a[84:87], a[180:183], v[52:55], a[84:87]\n"
    "v_mfma_f32_16x16x32_bf16 a[80:83], a[180:183], v[48:51], a[80:83]\n"
    "v_mfma_f32_16x16x32_bf16 a[96:99], a[184:187], v[48:51], a[96:99]\n"
    "v_mfma_f32_16x16x32_bf16 a[100:103], a[184:187], v[52:55], a[100:103]\n"
    "v_mfma_f32_16x16x32_bf16 a[104:107], a[184:187], v[56:59], a[104:107]\n"
    "v_mfma_f32_16x16x32_bf16 a[108:111], a[184:187], v[60:63], a[108:111]\n"
    "v_mfma_f32_16x16x32_bf16 a[124:127], a[188:191], v[60:63], a[124:127]\n"
    "v_mfma_f32_16x16x32_bf16 a[120:123], a[188:191], v[56:59], a[120:123]\n"
    "v_mfma_f32_16x16x32_bf16 a[116:119], a[188:191], v[52:55], a[116:119]\n"
    "v_mfma_f32_16x16x32_bf16 a[112:115], a[188:191], v[48:51], a[112:115]\n"
    "s_waitcnt vmcnt(0)\n"
    "s_nop 7\n"
    "s_nop 7\n"
    "s_mov_b64 s[92:93], %28\n"
    "s_mov_b32 s94, 2048\n"
    "s_mov_b32 s95, 0x20000\n"
    "s_mov_b32 s80, 0\n"
    "buffer_load_dwordx4 v[100:103], %29, s[92:95], s80 offen\n"
    "s_mov_b64 s[92:93], %30\n"
    "s_mov_b32 s94, 4\n"
    "buffer_load_dword v104, off, s[92:95], s80 sc1\n"
    "v_add_f32 v83, v83, v87\n"
    "v_mov_b32 v97, v83\n"
    "s_nop 1\n"
    "v_permlane16_swap_b32 v83, v97\n"
    "s_nop 1\n"
    "v_add_f32 v83, v83, v97\n"
    "v_mov_b32 v97, v83\n"
    "s_nop 1\n"
    "v_permlane32_swap_b32 v83, v97\n"
    "s_nop 1\n"
    "v_add_f32 v83, v83, v97\n"
    "v_mov_b32 v80, v108\n"
    "v_mov_b32 v81, v112\n"
    "v_mov_b32 v82, v116\n"
    "v_rcp_f32 v88, v80\n"
    "v_rcp_f32 v90, v81\n"
    "v_rcp_f32 v92, v82\n"
    "v_rcp_f32 v98, v83\n"
    "s_mov_b64 s[92:93], %26\n"
    "s_mov_b32 s94, 16777216\n"
    "s_mov_b32 s95, 0x20000\n"
    "v_mov_b32 v84, 0\n"
    "s_mov_b32 s80, 0\n"
    "v_accvgpr_read_b32 v0, a0\n"
    "v_accvgpr_read_b32 v1, a1\n"
    "v_accvgpr_read_b32 v2, a2\n"
    "v_accvgpr_read_b32 v3, a3\n"
    "v_accvgpr_read_b32 v4, a16\n"
    "v_accvgpr_read_b32 v5, a17\n"
    "v_accvgpr_read_b32 v6, a18\n"
    "v_accvgpr_read_b32 v7, a19\n"
    "v_pk_mul_f32 v[0:1], v[88:89], v[0:1] op_sel_hi:[0,1]\n"
    "v_pk_mul_f32 v[2:3], v[88:89], v[2:3] op_sel_hi:[0,1]\n"
    "v_pk_mul_f32 v[4:5], v[88:89], v[4:5] op_sel_hi:[0,1]\n"
    "v_pk_mul_f32 v[6:7], v[88:89], v[6:7] op_sel_hi:[0,1]\n"
    "v_cvt_pk_bf16_f32 v8, v0, v1\n"
    "v_cvt_pk_bf16_f32 v9, v2, v3\n"
    "v_cvt_pk_bf16_f32 v10, v4, v5\n"
    "v_cvt_pk_bf16_f32 v11, v6, v7\n"
    "buffer_store_dwordx4 v[8:11], %27, s[92:95], s80 offen sc1\n"
    "v_dot2c_f32_bf16 v84, v8, v8\n"
    "v_dot2c_f32_bf16 v84, v9, v9\n"
    "v_dot2c_f32_bf16 v84, v10, v10\n"
    "v_dot2c_f32_bf16 v84, v11, v11\n"
    "s_mov_b32 s80, 4096\n"
    "v_accvgpr_read_b32 v16, a32\n"
    "v_accvgpr_read_b32 v17, a33\n"
    "v_accvgpr_read_b32 v18, a34\n"
    "v_accvgpr_read_b32 v19, a35\n"
    "v_accvgpr_read_b32 v20, a48\n"
    "v_accvgpr_read_b32 v21, a49\n"
    "v_accvgpr_read_b32 v22, a50\n"
    "v_accvgpr_read_b32 v23, a51\n"
    "v_pk_mul_f32 v[16:17], v[88:89], v[16:17] op_sel_hi:[0,1]\n"
    "v_pk_mul_f32 v[18:19], v[88:89], v[18:19] op_sel_hi:[0,1]\n"
    "v_pk_mul_f32 v[20:21], v[88:89], v[20:21] op_sel_hi:[0,1]\n"
    "v_pk_mul_f32 v[22:23], v[88:89], v[22:23] op_sel_hi:[0,1]\n"
    "v_cvt_pk_bf16_f32 v24, v16, v17\n"
    "v_cvt_pk_bf16_f32 v25, v18, v19\n"
    "v_cvt_pk_bf16_f32 v26, v20, v21\n"
    "v_cvt_pk_bf16_f32 v27, v22, v23\n"
    "buffer_store_dwordx4 v[24:27], %27, s[92:95], s80 offen sc1\n"
    "v_dot2c_f32_bf16 v84, v24, v24\n"
    "v_dot2c_f32_bf16 v84, v25, v25\n"
    "v_dot2c_f32_bf16 v84, v26, v26\n"
    "v_dot2c_f32_bf16 v84, v27, v27\n"
    "s_mov_b32 s80, 8192\n"
    "v_accvgpr_read_b32 v0, a64\n"
    "v_accvgpr_read_b32 v1, a65\n"
    "v_accvgpr_read_b32 v2, a66\n"
    "v_accvgpr_read_b32 v3, a67\n"
    "v_accvgpr_read_b32 v4, a80\n"
    "v_accvgpr_read_b32 v5, a81\n"
    "v_accvgpr_read_b32 v6, a82\n"
    "v_accvgpr_read_b32 v7, a83\n"
    "v_pk_mul_f32 v[0:1], v[88:89], v[0:1] op_sel_hi:[0,1]\n"
    "v_pk_mul_f32 v[2:3], v[88:89], v[2:3] op_sel_hi:[0,1]\n"
    "v_pk_mul_f32 v[4:5], v[88:89], v[4:5] op_sel_hi:[0,1]\n"
    "v_pk_mul_f32 v[6:7], v[88:89], v[6:7] op_sel_hi:[0,1]\n"
    "v_cvt_pk_bf16_f32 v8, v0, v1\n"
    "v_cvt_pk_bf16_f32 v9, v2, v3\n"
    "v_cvt_pk_bf16_f32 v10, v4, v5\n"
    "v_cvt_pk_bf16_f32 v11, v6, v7\n"
    "buffer_store_dwordx4 v[8:11], %27, s[92:95], s80 offen sc1\n"
    "v_dot2c_f32_bf16 v84, v8, v8\n"
    "v_dot2c_f32_bf16 v84, v9, v9\n"
    "v_dot2c_f32_bf16 v84, v10, v10\n"
    "v_dot2c_f32_bf16 v84, v11, v11\n"
    "s_mov_b32 s80, 12288\n"
    "v_accvgpr_read_b32 v16, a96\n"
    "v_accvgpr_read_b32 v17, a97\n"
    "v_accvgpr_read_b32 v18, a98\n"
    "v_accvgpr_read_b32 v19, a99\n"
    "v_accvgpr_read_b32 v20, a112\n"
    "v_accvgpr_read_b32 v21, a113\n"
    "v_accvgpr_read_b32 v22, a114\n"
    "v_accvgpr_read_b32 v23, a115\n"
    "v_pk_mul_f32 v[16:17], v[88:89], v[16:17] op_sel_hi:[0,1]\n"
    "v_pk_mul_f32 v[18:19], v[88:89], v[18:19] op_sel_hi:[0,1]\n"
    "v_pk_mul_f32 v[20:21], v[88:89], v[20:21] op_sel_hi:[0,1]\n"
    "v_pk_mul_f32 v[22:23], v[88:89], v[22:23] op_sel_hi:[0,1]\n"
    "v_cvt_pk_bf16_f32 v24, v16, v17\n"
    "v_cvt_pk_bf16_f32 v25, v18, v19\n"
    "v_cvt_pk_bf16_f32 v26, v20, v21\n"
    "v_cvt_pk_bf16_f32 v27, v22, v23\n"
    "buffer_store_dwordx4 v[24:27], %27, s[92:95], s80 offen sc1\n"
    "v_dot2c_f32_bf16 v84, v24, v24\n"
    "v_dot2c_f32_bf16 v84, v25, v25\n"
    "v_dot2c_f32_bf16 v84, v26, v26\n"
    "v_dot2c_f32_bf16 v84, v27, v27\n"
    "s_mov_b32 s80, 0\n"
    "v_accvgpr_read_b32 v0, a4\n"
    "v_accvgpr_read_b32 v1, a5\n"
    "v_accvgpr_read_b32 v2, a6\n"
    "v_accvgpr_read_b32 v3, a7\n"
    "v_accvgpr_read_b32 v4, a20\n"
    "v_accvgpr_read_b32 v5, a21\n"
    "v_accvgpr_read_b32 v6, a22\n"
    "v_accvgpr_read_b32 v7, a23\n"
    "v_pk_mul_f32 v[0:1], v[90:91], v[0:1] op_sel_hi:[0,1]\n"
    "v_pk_mul_f32 v[2:3], v[90:91], v[2:3] op_sel_hi:[0,1]\n"
    "v_pk_mul_f32 v[4:5], v[90:91], v[4:5] op_sel_hi:[0,1]\n"
    "v_pk_mul_f32 v[6:7], v[90:91], v[6:7] op_sel_hi:[0,1]\n"
    "v_cvt_pk_bf16_f32 v8, v0, v1\n"
    "v_cvt_pk_bf16_f32 v9, v2, v3\n"
    "v_cvt_pk_bf16_f32 v10, v4, v5\n"
    "v_cvt_pk_bf16_f32 v11, v6, v7\n"
    "buffer_store_dwordx4 v[8:11], %27, s[92:95], s80 offen offset:1024 sc1\n"
    "v_dot2c_f32_bf16 v84, v8, v8\n"
    "v_dot2c_f32_bf16 v84, v9, v9\n"
    "v_dot2c_f32_bf16 v84, v10, v10\n"
    "v_dot2c_f32_bf16 v84, v11, v11\n"
    "s_mov_b32 s80, 4096\n"
    "v_accvgpr_read_b32 v16, a36\n"
    "v_accvgpr_read_b32 v17, a37\n"
    "v_accvgpr_read_b32 v18, a38\n"
    "v_accvgpr_read_b32 v19, a39\n"
    "v_accvgpr_read_b32 v20, a52\n"
    "v_accvgpr_read_b32 v21, a53\n"
    "v_accvgpr_read_b32 v22, a54\n"
    "v_accvgpr_read_b32 v23, a55\n"
    "v_pk_mul_f32 v[16:17], v[90:91], v[16:17] op_sel_hi:[0,1]\n"
    "v_pk_mul_f32 v[18:19], v[90:91], v[18:19] op_sel_hi:[0,1]\n"
    "v_pk_mul_f32 v[20:21], v[90:91], v[20:21] op_sel_hi:[0,1]\n"
    "v_pk_mul_f32 v[22:23], v[90:91], v[22:23] op_sel_hi:[0,1]\n"
    "v_cvt_pk_bf16_f32 v24, v16, v17\n"
    "v_cvt_pk_bf16_f32 v25, v18, v19\n"
    "v_cvt_pk_bf16_f32 v26, v20, v21\n"
    "v_cvt_pk_bf16_f32 v27, v22, v23\n"
    "buffer_store_dwordx4 v[24:27], %27, s[92:95], s80 offen offset:1024 sc1\n"
    "v_dot2c_f32_bf16 v84, v24, v24\n"
    "v_dot2c_f32_bf16 v84, v25, v25\n"
    "v_dot2c_f32_bf16 v84, v26, v26\n"
    "v_dot2c_f32_bf16 v84, v27, v27\n"
    "s_mov_b32 s80, 8192\n"
    "v_accvgpr_read_b32 v0, a68\n"
    "v_accvgpr_read_b32 v1, a69\n"
    "v_accvgpr_read_b32 v2, a70\n"
    "v_accvgpr_read_b32 v3, a71\n"
    "v_accvgpr_read_b32 v4, a84\n"
    "v_accvgpr_read_b32 v5, a85\n"
    "v_accvgpr_read_b32 v6, a86\n"
    "v_accvgpr_read_b32 v7, a87\n"
    "v_pk_mul_f32 v[0:1], v[90:91], v[0:1] op_sel_hi:[0,1]\n"
    "v_pk_mul_f32 v[2:3], v[90:91], v[2:3] op_sel_hi:[0,1]\n"
    "v_pk_mul_f32 v[4:5], v[90:91], v[4:5] op_sel_hi:[0,1]\n"
    "v_pk_mul_f32 v[6:7], v[90:91], v[6:7] op_sel_hi:[0,1]\n"
    "v_cvt_pk_bf16_f32 v8, v0, v1\n"
    "v_cvt_pk_bf16_f32 v9, v2, v3\n"
    "v_cvt_pk_bf16_f32 v10, v4, v5\n"
    "v_cvt_pk_bf16_f32 v11, v6, v7\n"
    "buffer_store_dwordx4 v[8:11], %27, s[92:95], s80 offen offset:1024 sc1\n"
    "v_dot2c_f32_bf16 v84, v8, v8\n"
    "v_dot2c_f32_bf16 v84, v9, v9\n"
    "v_dot2c_f32_bf16 v84, v10, v10\n"
    "v_dot2c_f32_bf16 v84, v11, v11\n"
    "s_mov_b32 s80, 12288\n"
    "v_accvgpr_read_b32 v16, a100\n"
    "v_accvgpr_read_b32 v17, a101\n"
    "v_accvgpr_read_b32 v18, a102\n"
    "v_accvgpr_read_b32 v19, a103\n"
    "v_accvgpr_read_b32 v20, a116\n"
    "v_accvgpr_read_b32 v21, a117\n"
    "v_accvgpr_read_b32 v22, a118\n"
    "v_accvgpr_read_b32 v23, a119\n"
    "v_pk_mul_f32 v[16:17], v[90:91], v[16:17] op_sel_hi:[0,1]\n"
    "v_pk_mul_f32 v[18:19], v[90:91], v[18:19] op_sel_hi:[0,1]\n"
    "v_pk_mul_f32 v[20:21], v[90:91], v[20:21] op_sel_hi:[0,1]\n"
    "v_pk_mul_f32 v[22:23], v[90:91], v[22:23] op_sel_hi:[0,1]\n"
    "v_cvt_pk_bf16_f32 v24, v16, v17\n"
    "v_cvt_pk_bf16_f32 v25, v18, v19\n"
    "v_cvt_pk_bf16_f32 v26, v20, v21\n"
    "v_cvt_pk_bf16_f32 v27, v22, v23\n"
    "buffer_store_dwordx4 v[24:27], %27, s[92:95], s80 offen offset:1024 sc1\n"
    "v_dot2c_f32_bf16 v84, v24, v24\n"
    "v_dot2c_f32_bf16 v84, v25, v25\n"
    "v_dot2c_f32_bf16 v84, v26, v26\n"
    "v_dot2c_f32_bf16 v84, v27, v27\n"
    "s_mov_b32 s80, 0\n"
    "v_accvgpr_read_b32 v0, a8\n"
    "v_accvgpr_read_b32 v1, a9\n"
    "v_accvgpr_read_b32 v2, a10\n"
    "v_accvgpr_read_b32 v3, a11\n"
    "v_accvgpr_read_b32 v4, a24\n"
    "v_accvgpr_read_b32 v5, a25\n"
    "v_accvgpr_read_b32 v6, a26\n"
    "v_accvgpr_read_b32 v7, a27\n"
    "v_pk_mul_f32 v[0:1], v[92:93], v[0:1] op_sel_hi:[0,1]\n"
    "v_pk_mul_f32 v[2:3], v[92:93], v[2:3] op_sel_hi:[0,1]\n"
    "v_pk_mul_f32 v[4:5], v[92:93], v[4:5] op_sel_hi:[0,1]\n"
    "v_pk_mul_f32 v[6:7], v[92:93], v[6:7] op_sel_hi:[0,1]\n"
    "v_cvt_pk_bf16_f32 v8, v0, v1\n"
    "v_cvt_pk_bf16_f32 v9, v2, v3\n"
    "v_cvt_pk_bf16_f32 v10, v4, v5\n"
    "v_cvt_pk_bf16_f32 v11, v6, v7\n"
    "buffer_store_dwordx4 v[8:11], %27, s[92:95], s80 offen offset:2048 sc1\n"
    "v_dot2c_f32_bf16 v84, v8, v8\n"
    "v_dot2c_f32_bf16 v84, v9, v9\n"
    "v_dot2c_f32_bf16 v84, v10, v10\n"
    "v_dot2c_f32_bf16 v84, v11, v11\n"
    "s_mov_b32 s80, 4096\n"
    "v_accvgpr_read_b32 v16, a40\n"
    "v_accvgpr_read_b32 v17, a41\n"
    "v_accvgpr_read_b32 v18, a42\n"
    "v_accvgpr_read_b32 v19, a43\n"
    "v_accvgpr_read_b32 v20, a56\n"
    "v_accvgpr_read_b32 v21, a57\n"
    "v_accvgpr_read_b32 v22, a58\n"
    "v_accvgpr_read_b32 v23, a59\n"
    "v_pk_mul_f32 v[16:17], v[92:93], v[16:17] op_sel_hi:[0,1]\n"
    "v_pk_mul_f32 v[18:19], v[92:93], v[18:19] op_sel_hi:[0,1]\n"
    "v_pk_mul_f32 v[20:21], v[92:93], v[20:21] op_sel_hi:[0,1]\n"
    "v_pk_mul_f32 v[22:23], v[92:93], v[22:23] op_sel_hi:[0,1]\n"
    "v_cvt_pk_bf16_f32 v24, v16, v17\n"
    "v_cvt_pk_bf16_f32 v25, v18, v19\n"
    "v_cvt_pk_bf16_f32 v26, v20, v21\n"
    "v_cvt_pk_bf16_f32 v27, v22, v23\n"
    "buffer_store_dwordx4 v[24:27], %27, s[92:95], s80 offen offset:2048 sc1\n"
    "v_dot2c_f32_bf16 v84, v24, v24\n"
    "v_dot2c_f32_bf16 v84, v25, v25\n"
    "v_dot2c_f32_bf16 v84, v26, v26\n"
    "v_dot2c_f32_bf16 v84, v27, v27\n"
    "s_mov_b32 s80, 8192\n"
    "v_accvgpr_read_b32 v0, a72\n"
    "v_accvgpr_read_b32 v1, a73\n"
    "v_accvgpr_read_b32 v2, a74\n"
    "v_accvgpr_read_b32 v3, a75\n"
    "v_accvgpr_read_b32 v4, a88\n"
    "v_accvgpr_read_b32 v5, a89\n"
    "v_accvgpr_read_b32 v6, a90\n"
    "v_accvgpr_read_b32 v7, a91\n"
    "v_pk_mul_f32 v[0:1], v[92:93], v[0:1] op_sel_hi:[0,1]\n"
    "v_pk_mul_f32 v[2:3], v[92:93], v[2:3] op_sel_hi:[0,1]\n"
    "v_pk_mul_f32 v[4:5], v[92:93], v[4:5] op_sel_hi:[0,1]\n"
    "v_pk_mul_f32 v[6:7], v[92:93], v[6:7] op_sel_hi:[0,1]\n"
    "v_cvt_pk_bf16_f32 v8, v0, v1\n"
    "v_cvt_pk_bf16_f32 v9, v2, v3\n"
    "v_cvt_pk_bf16_f32 v10, v4, v5\n"
    "v_cvt_pk_bf16_f32 v11, v6, v7\n"
    "buffer_store_dwordx4 v[8:11], %27, s[92:95], s80 offen offset:2048 sc1\n"
    "v_dot2c_f32_bf16 v84, v8, v8\n"
    "v_dot2c_f32_bf16 v84, v9, v9\n"
    "v_dot2c_f32_bf16 v84, v10, v10\n"
    "v_dot2c_f32_bf16 v84, v11, v11\n"
    "s_mov_b32 s80, 12288\n"
    "v_accvgpr_read_b32 v16, a104\n"
    "v_accvgpr_read_b32 v17, a105\n"
    "v_accvgpr_read_b32 v18, a106\n"
    "v_accvgpr_read_b32 v19, a107\n"
    "v_accvgpr_read_b32 v20, a120\n"
    "v_accvgpr_read_b32 v21, a121\n"
    "v_accvgpr_read_b32 v22, a122\n"
    "v_accvgpr_read_b32 v23, a123\n"
    "v_pk_mul_f32 v[16:17], v[92:93], v[16:17] op_sel_hi:[0,1]\n"
    "v_pk_mul_f32 v[18:19], v[92:93], v[18:19] op_sel_hi:[0,1]\n"
    "v_pk_mul_f32 v[20:21], v[92:93], v[20:21] op_sel_hi:[0,1]\n"
    "v_pk_mul_f32 v[22:23], v[92:93], v[22:23] op_sel_hi:[0,1]\n"
    "v_cvt_pk_bf16_f32 v24, v16, v17\n"
    "v_cvt_pk_bf16_f32 v25, v18, v19\n"
    "v_cvt_pk_bf16_f32 v26, v20, v21\n"
    "v_cvt_pk_bf16_f32 v27, v22, v23\n"
    "buffer_store_dwordx4 v[24:27], %27, s[92:95], s80 offen offset:2048 sc1\n"
    "v_dot2c_f32_bf16 v84, v24, v24\n"
    "v_dot2c_f32_bf16 v84, v25, v25\n"
    "v_dot2c_f32_bf16 v84, v26, v26\n"
    "v_dot2c_f32_bf16 v84, v27, v27\n"
    "s_mov_b32 s80, 0\n"
    "v_accvgpr_read_b32 v0, a12\n"
    "v_accvgpr_read_b32 v1, a13\n"
    "v_accvgpr_read_b32 v2, a14\n"
    "v_accvgpr_read_b32 v3, a15\n"
    "v_accvgpr_read_b32 v4, a28\n"
    "v_accvgpr_read_b32 v5, a29\n"
    "v_accvgpr_read_b32 v6, a30\n"
    "v_accvgpr_read_b32 v7, a31\n"
    "v_pk_mul_f32 v[0:1], v[98:99], v[0:1] op_sel_hi:[0,1]\n"
    "v_pk_mul_f32 v[2:3], v[98:99], v[2:3] op_sel_hi:[0,1]\n"
    "v_pk_mul_f32 v[4:5], v[98:99], v[4:5] op_sel_hi:[0,1]\n"
    "v_pk_mul_f32 v[6:7], v[98:99], v[6:7] op_sel_hi:[0,1]\n"
    "v_cvt_pk_bf16_f32 v8, v0, v1\n"
    "v_cvt_pk_bf16_f32 v9, v2, v3\n"
    "v_cvt_pk_bf16_f32 v10, v4, v5\n"
    "v_cvt_pk_bf16_f32 v11, v6, v7\n"
    "buffer_store_dwordx4 v[8:11], %27, s[92:95], s80 offen offset:3072 sc1\n"
    "v_dot2c_f32_bf16 v84, v8, v8\n"
    "v_dot2c_f32_bf16 v84, v9, v9\n"
    "v_dot2c_f32_bf16 v84, v10, v10\n"
    "v_dot2c_f32_bf16 v84, v11, v11\n"
    "s_mov_b32 s80, 4096\n"
    "v_accvgpr_read_b32 v16, a44\n"
    "v_accvgpr_read_b32 v17, a45\n"
    "v_accvgpr_read_b32 v18, a46\n"
    "v_accvgpr_read_b32 v19, a47\n"
    "v_accvgpr_read_b32 v20, a60\n"
    "v_accvgpr_read_b32 v21, a61\n"
    "v_accvgpr_read_b32 v22, a62\n"
    "v_accvgpr_read_b32 v23, a63\n"
    "v_pk_mul_f32 v[16:17], v[98:99], v[16:17] op_sel_hi:[0,1]\n"
    "v_pk_mul_f32 v[18:19], v[98:99], v[18:19] op_sel_hi:[0,1]\n"
    "v_pk_mul_f32 v[20:21], v[98:99], v[20:21] op_sel_hi:[0,1]\n"
    "v_pk_mul_f32 v[22:23], v[98:99], v[22:23] op_sel_hi:[0,1]\n"
    "v_cvt_pk_bf16_f32 v24, v16, v17\n"
    "v_cvt_pk_bf16_f32 v25, v18, v19\n"
    "v_cvt_pk_bf16_f32 v26, v20, v21\n"
    "v_cvt_pk_bf16_f32 v27, v22, v23\n"
    "buffer_store_dwordx4 v[24:27], %27, s[92:95], s80 offen offset:3072 sc1\n"
    "v_dot2c_f32_bf16 v84, v24, v24\n"
    "v_dot2c_f32_bf16 v84, v25, v25\n"
    "v_dot2c_f32_bf16 v84, v26, v26\n"
    "v_dot2c_f32_bf16 v84, v27, v27\n"
    "s_mov_b32 s80, 8192\n"
    "v_accvgpr_read_b32 v0, a76\n"
    "v_accvgpr_read_b32 v1, a77\n"
    "v_accvgpr_read_b32 v2, a78\n"
    "v_accvgpr_read_b32 v3, a79\n"
    "v_accvgpr_read_b32 v4, a92\n"
    "v_accvgpr_read_b32 v5, a93\n"
    "v_accvgpr_read_b32 v6, a94\n"
    "v_accvgpr_read_b32 v7, a95\n"
    "v_pk_mul_f32 v[0:1], v[98:99], v[0:1] op_sel_hi:[0,1]\n"
    "v_pk_mul_f32 v[2:3], v[98:99], v[2:3] op_sel_hi:[0,1]\n"
    "v_pk_mul_f32 v[4:5], v[98:99], v[4:5] op_sel_hi:[0,1]\n"
    "v_pk_mul_f32 v[6:7], v[98:99], v[6:7] op_sel_hi:[0,1]\n"
    "v_cvt_pk_bf16_f32 v8, v0, v1\n"
    "v_cvt_pk_bf16_f32 v9, v2, v3\n"
    "v_cvt_pk_bf16_f32 v10, v4, v5\n"
    "v_cvt_pk_bf16_f32 v11, v6, v7\n"
    "buffer_store_dwordx4 v[8:11], %27, s[92:95], s80 offen offset:3072 sc1\n"
    "v_dot2c_f32_bf16 v84, v8, v8\n"
    "v_dot2c_f32_bf16 v84, v9, v9\n"
    "v_dot2c_f32_bf16 v84, v10, v10\n"
    "v_dot2c_f32_bf16 v84, v11, v11\n"
    "s_mov_b32 s80, 12288\n"
    "v_accvgpr_read_b32 v16, a108\n"
    "v_accvgpr_read_b32 v17, a109\n"
    "v_accvgpr_read_b32 v18, a110\n"
    "v_accvgpr_read_b32 v19, a111\n"
    "v_accvgpr_read_b32 v20, a124\n"
    "v_accvgpr_read_b32 v21, a125\n"
    "v_accvgpr_read_b32 v22, a126\n"
    "v_accvgpr_read_b32 v23, a127\n"
    "v_pk_mul_f32 v[16:17], v[98:99], v[16:17] op_sel_hi:[0,1]\n"
    "v_pk_mul_f32 v[18:19], v[98:99], v[18:19] op_sel_hi:[0,1]\n"
    "v_pk_mul_f32 v[20:21], v[98:99], v[20:21] op_sel_hi:[0,1]\n"
    "v_pk_mul_f32 v[22:23], v[98:99], v[22:23] op_sel_hi:[0,1]\n"
    "v_cvt_pk_bf16_f32 v24, v16, v17\n"
    "v_cvt_pk_bf16_f32 v25, v18, v19\n"
    "v_cvt_pk_bf16_f32 v26, v20, v21\n"
    "v_cvt_pk_bf16_f32 v27, v22, v23\n"
    "buffer_store_dwordx4 v[24:27], %27, s[92:95], s80 offen offset:3072 sc1\n"
    "v_dot2c_f32_bf16 v84, v24, v24\n"
    "v_dot2c_f32_bf16 v84, v25, v25\n"
    "v_dot2c_f32_bf16 v84, v26, v26\n"
    "v_dot2c_f32_bf16 v84, v27, v27\n"
    "s_waitcnt vmcnt(16)\n"
    "s_nop 4\n"
    "s_branch L_attn_end\n"
    "L_attn_slow:\n"
    "s_nop 7\n"
    "v_cvt_pk_bf16_f32 v128, v0, v1\n"
    "v_cvt_pk_bf16_f32 v129, v2, v3\n"
    "v_cvt_pk_bf16_f32 v130, v16, v17\n"
    "v_cvt_pk_bf16_f32 v131, v18, v19\n"
    "v_cvt_pk_bf16_f32 v132, v32, v33\n"
    "v_cvt_pk_bf16_f32 v133, v34, v35\n"
    "v_cvt_pk_bf16_f32 v134, v48, v49\n"
    "v_cvt_pk_bf16_f32 v135, v50, v51\n"
    "v_cvt_pk_bf16_f32 v136, v64, v65\n"
    "v_cvt_pk_bf16_f32 v137, v66, v67\n"
    "v_cvt_pk_bf16_f32 v138, v80, v81\n"
    "v_cvt_pk_bf16_f32 v139, v82, v83\n"
    "v_cvt_pk_bf16_f32 v140, v96, v97\n"
    "v_cvt_pk_bf16_f32 v141, v98, v99\n"
    "v_cvt_pk_bf16_f32 v142, v112, v113\n"
    "v_cvt_pk_bf16_f32 v143, v114, v115\n"
    "v_cvt_pk_bf16_f32 v144, v4, v5\n"
    "v_cvt_pk_bf16_f32 v145, v6, v7\n"
    "v_cvt_pk_bf16_f32 v146, v20, v21\n"
    "v_cvt_pk_bf16_f32 v147, v22, v23\n"
    "v_cvt_pk_bf16_f32 v148, v36, v37\n"
    "v_cvt_pk_bf16_f32 v149, v38, v39\n"
    "v_cvt_pk_bf16_f32 v150, v52, v53\n"
    "v_cvt_pk_bf16_f32 v151, v54, v55\n"
    "v_cvt_pk_bf16_f32 v152, v68, v69\n"
    "v_cvt_pk_bf16_f32 v153, v70, v71\n"
    "v_cvt_pk_bf16_f32 v154, v84, v85\n"
    "v_cvt_pk_bf16_f32 v155, v86, v87\n"
    "v_cvt_pk_bf16_f32 v156, v100, v101\n"
    "v_cvt_pk_bf16_f32 v157, v102, v103\n"
    "v_cvt_pk_bf16_f32 v158, v116, v117\n"
    "v_cvt_pk_bf16_f32 v159, v118, v119\n"
    "v_cvt_pk_bf16_f32 v160, v8, v9\n"
    "v_cvt_pk_bf16_f32 v161, v10, v11\n"
    "v_cvt_pk_bf16_f32 v162, v24, v25\n"
    "v_cvt_pk_bf16_f32 v163, v26, v27\n"
    "v_cvt_pk_bf16_f32 v164, v40, v41\n"
    "v_cvt_pk_bf16_f32 v165, v42, v43\n"
    "v_cvt_pk_bf16_f32 v166, v56, v57\n"
    "v_cvt_pk_bf16_f32 v167, v58, v59\n"
    "v_cvt_pk_bf16_f32 v168, v72, v73\n"
    "v_cvt_pk_bf16_f32 v169, v74, v75\n"
    "v_cvt_pk_bf16_f32 v170, v88, v89\n"
    "v_cvt_pk_bf16_f32 v171, v90, v91\n"
    "v_cvt_pk_bf16_f32 v172, v104, v105\n"
    "v_cvt_pk_bf16_f32 v173, v106, v107\n"
    "v_cvt_pk_bf16_f32 v174, v120, v121\n"
    "v_cvt_pk_bf16_f32 v175, v122, v123\n"
    "v_cvt_pk_bf16_f32 v176, v12, v13\n"
    "v_cvt_pk_bf16_f32 v177, v14, v15\n"
    "v_cvt_pk_bf16_f32 v178, v28, v29\n"
    "v_cvt_pk_bf16_f32 v179, v30, v31\n"
    "v_cvt_pk_bf16_f32 v180, v44, v45\n"
    "v_cvt_pk_bf16_f32 v181, v46, v47\n"
    "v_cvt_pk_bf16_f32 v182, v60, v61\n"
    "v_cvt_pk_bf16_f32 v183, v62, v63\n"
    "v_cvt_pk_bf16_f32 v184, v76, v77\n"
    "v_cvt_pk_bf16_f32 v185, v78, v79\n"
    "v_cvt_pk_bf16_f32 v186, v92, v93\n"
    "v_cvt_pk_bf16_f32 v187, v94, v95\n"
    "v_cvt_pk_bf16_f32 v188, v108, v109\n"
    "v_cvt_pk_bf16_f32 v189, v110, v111\n"
    "v_cvt_pk_bf16_f32 v190, v124, v125\n"
    "v_cvt_pk_bf16_f32 v191, v126, v127\n"
    "s_nop 1\n"
    "s_waitcnt vmcnt(0)\n"
    "s_barrier\n"
    "v_mov_b32 v90, 0xff800000\n"
    "v_mov_b32 v91, 0xff800000\n"
    "v_mov_b32 v92, 0xff800000\n"
    "v_mov_b32 v93, 0xff800000\n"
    "s_mov_b32 s80, s79\n"
    "s_mov_b32 s77, 32\n"
    "L_attn_max:\n"
    "s_add_u32 m0, s79, 81920\n"
    "s_nop 0\n"
    "buffer_load_dwordx4 v204, s[64:67], s80 offen nt lds\n"
    "buffer_load_dwordx4 v205, s[64:67], s80 offen offset:1024 nt lds\n"
    "buffer_load_dwordx4 v206, s[64:67], s80 offen offset:2048 nt lds\n"
    "buffer_load_dwordx4 v207, s[64:67], s80 offen offset:3072 nt lds\n"
    "s_add_u32 s80, s80, 16384\n"
    "s_waitcnt vmcnt(0)\n"
    "s_barrier\n"
    "ds_read_b128 a[128:131], v210 offset:32768\n"
    "ds_read_b128 a[132:135], v211 offset:32768\n"
    "ds_read_b128 a[136:139], v210 offset:33792\n"
    "ds_read_b128 a[140:143], v211 offset:33792\n"
    "s_waitcnt lgkmcnt(0)\n"
    "v_mfma_f32_16x16x32_bf16 v[0:3], a[128:131], v[128:131], 0\n"
    "v_mfma_f32_16x16x32_bf16 v[0:3], a[132:135], v[132:135], v[0:3]\n"
    "v_mfma_f32_16x16x32_bf16 v[0:3], a[136:139], v[136:139], v[0:3]\n"
    "v_mfma_f32_16x16x32_bf16 v[0:3], a[140:143], v[140:143], v[0:3]\n"
    "v_mfma_f32_16x16x32_bf16 v[4:7], a[128:131], v[144:147], 0\n"
    "v_mfma_f32_16x16x32_bf16 v[4:7], a[132:135], v[148:151], v[4:7]\n"
    "v_mfma_f32_16x16x32_bf16 v[4:7], a[136:139], v[152:155], v[4:7]\n"
    "v_mfma_f32_16x16x32_bf16 v[4:7], a[140:143], v[156:159], v[4:7]\n"
    "v_mfma_f32_16x16x32_bf16 v[8:11], a[128:131], v[160:163], 0\n"
    "v_mfma_f32_16x16x32_bf16 v[8:11], a[132:135], v[164:167], v[8:11]\n"
    "v_mfma_f32_16x16x32_bf16 v[8:11], a[136:139], v[168:171], v[8:11]\n"
    "v_mfma_f32_16x16x32_bf16 v[8:11], a[140:143], v[172:175], v[8:11]\n"
    "v_mfma_f32_16x16x32_bf16 v[12:15], a[128:131], v[176:179], 0\n"
    "v_mfma_f32_16x16x32_bf16 v[12:15], a[132:135], v[180:183], v[12:15]\n"
    "v_mfma_f32_16x16x32_bf16 v[12:15], a[136:139], v[184:187], v[12:15]\n"
    "v_mfma_f32_16x16x32_bf16 v[12:15], a[140:143], v[188:191], v[12:15]\n"
    "s_nop 7\n"
    "s_nop 1\n"
    "v_max3_f32 v90, v90, v0, v1\n"
    "v_max3_f32 v91, v91, v4, v5\n"
    "v_max3_f32 v92, v92, v8, v9\n"
    "v_max3_f32 v93, v93, v12, v13\n"
    "v_max3_f32 v90, v90, v2, v3\n"
    "v_max3_f32 v91, v91, v6, v7\n"
    "v_max3_f32 v92, v92, v10, v11\n"
    "v_max3_f32 v93, v93, v14, v15\n"
    "s_nop 3\n"
    "ds_read_b128 a[128:131], v210 offset:36864\n"
    "ds_read_b128 a[132:135], v211 offset:36864\n"
    "ds_read_b128 a[136:139], v210 offset:37888\n"
    "ds_read_b128 a[140:143], v211 offset:37888\n"
    "s_waitcnt lgkmcnt(0)\n"
    "v_mfma_f32_16x16x32_bf16 v[0:3], a[128:131], v[128:131], 0\n"
    "v_mfma_f32_16x16x32_bf16 v[0:3], a[132:135], v[132:135], v[0:3]\n"
    "v_mfma_f32_16x16x32_bf16 v[0:3], a[136:139], v[136:139], v[0:3]\n"
    "v_mfma_f32_16x16x32_bf16 v[0:3], a[140:143], v[140:143], v[0:3]\n"
    "v_mfma_f32_16x16x32_bf16 v[4:7], a[128:131], v[144:147], 0\n"
    "v_mfma_f32_16x16x32_bf16 v[4:7], a[132:135], v[148:151], v[4:7]\n"
    "v_mfma_f32_16x16x32_bf16 v[4:7], a[136:139], v[152:155], v[4:7]\n"
    "v_mfma_f32_16x16x32_bf16 v[4:7], a[140:143], v[156:159], v[4:7]\n"
    "v_mfma_f32_16x16x32_bf16 v[8:11], a[128:131], v[160:163], 0\n"
    "v_mfma_f32_16x16x32_bf16 v[8:11], a[132:135], v[164:167], v[8:11]\n"
    "v_mfma_f32_16x16x32_bf16 v[8:11], a[136:139], v[168:171], v[8:11]\n"
    "v_mfma_f32_16x16x32_bf16 v[8:11], a[140:143], v[172:175], v[8:11]\n"
    "v_mfma_f32_16x16x32_bf16 v[12:15], a[128:131], v[176:179], 0\n"
    "v_mfma_f32_16x16x32_bf16 v[12:15], a[132:135], v[180:183], v[12:15]\n"
    "v_mfma_f32_16x16x32_bf16 v[12:15], a[136:139], v[184:187], v[12:15]\n"
    "v_mfma_f32_16x16x32_bf16 v[12:15], a[140:143], v[188:191], v[12:15]\n"
    "s_nop 7\n"
    "s_nop 1\n"
    "v_max3_f32 v90, v90, v0, v1\n"
    "v_max3_f32 v91, v91, v4, v5\n"
    "v_max3_f32 v92, v92, v8, v9\n"
    "v_max3_f32 v93, v93, v12, v13\n"
    "v_max3_f32 v90, v90, v2, v3\n"
    "v_max3_f32 v91, v91, v6, v7\n"
    "v_max3_f32 v92, v92, v10, v11\n"
    "v_max3_f32 v93, v93, v14, v15\n"
    "s_nop 3\n"
    "ds_read_b128 a[128:131], v210 offset:40960\n"
    "ds_read_b128 a[132:135], v211 offset:40960\n"
    "ds_read_b128 a[136:139], v210 offset:41984\n"
    "ds_read_b128 a[140:143], v211 offset:41984\n"
    "s_waitcnt lgkmcnt(0)\n"
    "v_mfma_f32_16x16x32_bf16 v[0:3], a[128:131], v[128:131], 0\n"
    "v_mfma_f32_16x16x32_bf16 v[0:3], a[132:135], v[132:135], v[0:3]\n"
    "v_mfma_f32_16x16x32_bf16 v[0:3], a[136:139], v[136:139], v[0:3]\n"
    "v_mfma_f32_16x16x32_bf16 v[0:3], a[140:143], v[140:143], v[0:3]\n"
    "v_mfma_f32_16x16x32_bf16 v[4:7], a[128:131], v[144:147], 0\n"
    "v_mfma_f32_16x16x32_bf16 v[4:7], a[132:135], v[148:151], v[4:7]\n"
    "v_mfma_f32_16x16x32_bf16 v[4:7], a[136:139], v[152:155], v[4:7]\n"
    "v_mfma_f32_16x16x32_bf16 v[4:7], a[140:143], v[156:159], v[4:7]\n"
    "v_mfma_f32_16x16x32_bf16 v[8:11], a[128:131], v[160:163], 0\n"
    "v_mfma_f32_16x16x32_bf16 v[8:11], a[132:135], v[164:167], v[8:11]\n"
    "v_mfma_f32_16x16x32_bf16 v[8:11], a[136:139], v[168:171], v[8:11]\n"
    "v_mfma_f32_16x16x32_bf16 v[8:11], a[140:143], v[172:175], v[8:11]\n"
    "v_mfma_f32_16x16x32_bf16 v[12:15], a[128:131], v[176:179], 0\n"
    "v_mfma_f32_16x16x32_bf16 v[12:15], a[132:135], v[180:183], v[12:15]\n"
    "v_mfma_f32_16x16x32_bf16 v[12:15], a[136:139], v[184:187], v[12:15]\n"
    "v_mfma_f32_16x16x32_bf16 v[12:15], a[140:143], v[188:191], v[12:15]\n"
    "s_nop 7\n"
    "s_nop 1\n"
    "v_max3_f32 v90, v90, v0, v1\n"
    "v_max3_f32 v91, v91, v4, v5\n"
    "v_max3_f32 v92, v92, v8, v9\n"
    "v_max3_f32 v93, v93, v12, v13\n"
    "v_max3_f32 v90, v90, v2, v3\n"
    "v_max3_f32 v91, v91, v6, v7\n"
    "v_max3_f32 v92, v92, v10, v11\n"
    "v_max3_f32 v93, v93, v14, v15\n"
    "s_nop 3\n"
    "ds_read_b128 a[128:131], v210 offset:45056\n"
    "ds_read_b128 a[132:135], v211 offset:45056\n"
    "ds_read_b128 a[136:139], v210 offset:46080\n"
    "ds_read_b128 a[140:143], v211 offset:46080\n"
    "s_waitcnt lgkmcnt(0)\n"
    "v_mfma_f32_16x16x32_bf16 v[0:3], a[128:131], v[128:131], 0\n"
    "v_mfma_f32_16x16x32_bf16 v[0:3], a[132:135], v[132:135], v[0:3]\n"
    "v_mfma_f32_16x16x32_bf16 v[0:3], a[136:139], v[136:139], v[0:3]\n"
    "v_mfma_f32_16x16x32_bf16 v[0:3], a[140:143], v[140:143], v[0:3]\n"
    "v_mfma_f32_16x16x32_bf16 v[4:7], a[128:131], v[144:147], 0\n"
    "v_mfma_f32_16x16x32_bf16 v[4:7], a[132:135], v[148:151], v[4:7]\n"
    "v_mfma_f32_16x16x32_bf16 v[4:7], a[136:139], v[152:155], v[4:7]\n"
    "v_mfma_f32_16x16x32_bf16 v[4:7], a[140:143], v[156:159], v[4:7]\n"
    "v_mfma_f32_16x16x32_bf16 v[8:11], a[128:131], v[160:163], 0\n"
    "v_mfma_f32_16x16x32_bf16 v[8:11], a[132:135], v[164:167], v[8:11]\n"
    "v_mfma_f32_16x16x32_bf16 v[8:11], a[136:139], v[168:171], v[8:11]\n"
    "v_mfma_f32_16x16x32_bf16 v[8:11], a[140:143], v[172:175], v[8:11]\n"
    "v_mfma_f32_16x16x32_bf16 v[12:15], a[128:131], v[176:179], 0\n"
    "v_mfma_f32_16x16x32_bf16 v[12:15], a[132:135], v[180:183], v[12:15]\n"
    "v_mfma_f32_16x16x32_bf16 v[12:15], a[136:139], v[184:187], v[12:15]\n"
    "v_mfma_f32_16x16x32_bf16 v[12:15], a[140:143], v[188:191], v[12:15]\n"
    "s_nop 7\n"
    "s_nop 1\n"
    "v_max3_f32 v90, v90, v0, v1\n"
    "v_max3_f32 v91, v91, v4, v5\n"
    "v_max3_f32 v92, v92, v8, v9\n"
    "v_max3_f32 v93, v93, v12, v13\n"
    "v_max3_f32 v90, v90, v2, v3\n"
    "v_max3_f32 v91, v91, v6, v7\n"
    "v_max3_f32 v92, v92, v10, v11\n"
    "v_max3_f32 v93, v93, v14, v15\n"
    "s_nop 3\n"
    "s_barrier\n"
    "s_sub_u32 s77, s77, 1\n"
    "s_cmp_lg_u32 s77, 0\n"
    "s_cbranch_scc1 L_attn_max\n"
    "s_add_u32 m0, s73, s79\n"
    "s_nop 0\n"
    "buffer_load_dwordx4 v204, s[64:67], s72 offen nt lds\n"
    "buffer_load_dwordx4 v205, s[64:67], s72 offen offset:1024 nt lds\n"
    "buffer_load_dwordx4 v206, s[64:67], s72 offen offset:2048 nt lds\n"
    "buffer_load_dwordx4 v207, s[64:67], s72 offen offset:3072 nt lds\n"
    "s_add_u32 m0, s74, s79\n"
    "s_nop 0\n"
    "buffer_load_dwordx4 v208, s[68:71], s72 offen nt lds\n"
    "buffer_load_dwordx4 v209, s[68:71], s72 offen offset:1024 nt lds\n"
    "buffer_load_dwordx4 v208, s[68:71], s72 offen offset:2048 nt lds\n"
    "buffer_load_dwordx4 v209, s[68:71], s72 offen offset:3072 nt lds\n"
    "s_add_u32 s72, s72, 16384\n"
    "s_add_u32 s73, s73, 16384\n"
    "s_cmp_ge_u32 s73, 98304\n"
    "s_cselect_b32 s73, 49152, s73\n"
    "s_add_u32 m0, s73, s79\n"
    "s_nop 0\n"
    "buffer_load_dwordx4 v204, s[64:67], s72 offen nt lds\n"
    "buffer_load_dwordx4 v205, s[64:67], s72 offen offset:1024 nt lds\n"
    "s_mov_b32 s78, 30\n"
    "s_mov_b32 s77, 16\n"
    "s_mov_b32 s76, 147456\n"
    "s_waitcnt vmcnt(10)\n"
    "s_mov_b64 s[92:93], %22\n"
    "s_mov_b32 s94, %23\n"
    "s_mov_b32 s95, 0x20000\n"
    "s_mov_b32 s82, 0\n"
    "s_lshl_b32 s96, s79, 1\n"
    "s_lshr_b32 s97, s79, 2\n"
    "s_add_u32 s97, s97, 32768\n"
    "ds_read_b128 a[128:131], v210 offset:0\n"
    "ds_read_b128 a[132:135], v211 offset:0\n"
    "ds_read_b128 a[136:139], v210 offset:1024\n"
    "ds_read_b128 a[140:143], v211 offset:1024\n"
    "v_mov_b32 v16, 0xff800000\n"
    "v_mov_b32 v32, 0\n"
    "v_mov_b32 v48, 0\n"
    "v_mov_b32 v17, 0xff800000\n"
    "v_mov_b32 v33, 0\n"
    "v_mov_b32 v49, 0\n"
    "v_mov_b32 v18, 0xff800000\n"
    "v_mov_b32 v34, 0\n"
    "v_mov_b32 v50, 0\n"
    "v_mov_b32 v19, 0xff800000\n"
    "v_mov_b32 v35, 0\n"
    "v_mov_b32 v51, 0\n"
    "v_mov_b32 v80, 0\n"
    "v_mov_b32 v84, 0\n"
    "v_mov_b32 v20, 0xff800000\n"
    "v_mov_b32 v36, 0\n"
    "v_mov_b32 v52, 0\n"
    "v_mov_b32 v21, 0xff800000\n"
    "v_mov_b32 v37, 0\n"
    "v_mov_b32 v53, 0\n"
    "v_mov_b32 v22, 0xff800000\n"
    "v_mov_b32 v38, 0\n"
    "v_mov_b32 v54, 0\n"
    "v_mov_b32 v23, 0xff800000\n"
    "v_mov_b32 v39, 0\n"
    "v_mov_b32 v55, 0\n"
    "v_mov_b32 v81, 0\n"
    "v_mov_b32 v85, 0\n"
    "v_mov_b32 v24, 0xff800000\n"
    "v_mov_b32 v40, 0\n"
    "v_mov_b32 v56, 0\n"
    "v_mov_b32 v25, 0xff800000\n"
    "v_mov_b32 v41, 0\n"
    "v_mov_b32 v57, 0\n"
    "v_mov_b32 v26, 0xff800000\n"
    "v_mov_b32 v42, 0\n"
    "v_mov_b32 v58, 0\n"
    "v_mov_b32 v27, 0xff800000\n"
    "v_mov_b32 v43, 0\n"
    "v_mov_b32 v59, 0\n"
    "v_mov_b32 v82, 0\n"
    "v_mov_b32 v86, 0\n"
    "v_mov_b32 v28, 0xff800000\n"
    "v_mov_b32 v44, 0\n"
    "v_mov_b32 v60, 0\n"
    "v_mov_b32 v29, 0xff800000\n"
    "v_mov_b32 v45, 0\n"
    "v_mov_b32 v61, 0\n"
    "v_mov_b32 v30, 0xff800000\n"
    "v_mov_b32 v46, 0\n"
    "v_mov_b32 v62, 0\n"
    "v_mov_b32 v31, 0xff800000\n"
    "v_mov_b32 v47, 0\n"
    "v_mov_b32 v63, 0\n"
    "v_mov_b32 v83, 0\n"
    "v_mov_b32 v87, 0\n"
    "v_accvgpr_write_b32 a160, 0\n"
    "v_accvgpr_write_b32 a161, 0\n"
    "v_accvgpr_write_b32 a162, 0\n"
    "v_accvgpr_write_b32 a163, 0\n"
    "v_accvgpr_write_b32 a164, 0\n"
    "v_accvgpr_write_b32 a165, 0\n"
    "v_accvgpr_write_b32 a166, 0\n"
    "v_accvgpr_write_b32 a167, 0\n"
    "v_accvgpr_write_b32 a168, 0\n"
    "v_accvgpr_write_b32 a169, 0\n"
    "v_accvgpr_write_b32 a170, 0\n"
    "v_accvgpr_write_b32 a171, 0\n"
    "v_accvgpr_write_b32 a172, 0\n"
    "v_accvgpr_write_b32 a173, 0\n"
    "v_accvgpr_write_b32 a174, 0\n"
    "v_accvgpr_write_b32 a175, 0\n"
    "v_accvgpr_write_b32 a176, 0\n"
    "v_accvgpr_write_b32 a177, 0\n"
    "v_accvgpr_write_b32 a178, 0\n"
    "v_accvgpr_write_b32 a179, 0\n"
    "v_accvgpr_write_b32 a180, 0\n"
    "v_accvgpr_write_b32 a181, 0\n"
    "v_accvgpr_write_b32 a182, 0\n"
    "v_accvgpr_write_b32 a183, 0\n"
    "v_accvgpr_write_b32 a184, 0\n"
    "v_accvgpr_write_b32 a185, 0\n"
    "v_accvgpr_write_b32 a186, 0\n"
    "v_accvgpr_write_b32 a187, 0\n"
    "v_accvgpr_write_b32 a188, 0\n"
    "v_accvgpr_write_b32 a189, 0\n"
    "v_accvgpr_write_b32 a190, 0\n"
    "v_accvgpr_write_b32 a191, 0\n"
    "v_mov_b32 v108, 0\n"
    "v_mov_b32 v109, 0\n"
    "v_mov_b32 v110, 0\n"
    "v_mov_b32 v111, 0\n"
    "v_mov_b32 v112, 0\n"
    "v_mov_b32 v113, 0\n"
    "v_mov_b32 v114, 0\n"
    "v_mov_b32 v115, 0\n"
    "v_mov_b32 v116, 0\n"
    "v_mov_b32 v117, 0\n"
    "v_mov_b32 v118, 0\n"
    "v_mov_b32 v119, 0\n"
    "v_mov_b32 v124, 0x3f803f80\n"
    "v_mov_b32 v125, 0x3f803f80\n"
    "v_mov_b32 v126, 0x3f803f80\n"
    "v_mov_b32 v127, 0x3f803f80\n"
    "v_mov_b32 v94, v90\n"
    "v_mov_b32 v95, v91\n"
    "v_mov_b32 v96, v92\n"
    "v_mov_b32 v97, v93\n"
    "s_nop 0\n"
    "v_permlane32_swap_b32 v90, v94\n"
    "v_permlane32_swap_b32 v91, v95\n"
    "v_permlane32_swap_b32 v92, v96\n"
    "v_permlane32_swap_b32 v93, v97\n"
    "s_nop 0\n"
    "v_max_f32 v90, v90, v94\n"
    "v_max_f32 v91, v91, v95\n"
    "v_max_f32 v92, v92, v96\n"
    "v_max_f32 v93, v93, v97\n"
    "v_mov_b32 v94, v90\n"
    "v_mov_b32 v95, v91\n"
    "v_mov_b32 v96, v92\n"
    "v_mov_b32 v97, v93\n"
    "s_nop 0\n"
    "v_permlane16_swap_b32 v90, v94\n"
    "v_permlane16_swap_b32 v91, v95\n"
    "v_permlane16_swap_b32 v92, v96\n"
    "v_permlane16_swap_b32 v93, v97\n"
    "s_nop 0\n"
    "v_max_f32 v90, v90, v94\n"
    "v_max_f32 v91, v91, v95\n"
    "v_max_f32 v92, v92, v96\n"
    "v_max_f32 v93, v93, v97\n"
    "v_sub_f32 v64, 0, v90\n"
    "v_sub_f32 v65, 0, v90\n"
    "v_sub_f32 v66, 0, v90\n"
    "v_sub_f32 v67, 0, v90\n"
    "v_sub_f32 v68, 0, v91\n"
    "v_sub_f32 v69, 0, v91\n"
    "v_sub_f32 v70, 0, v91\n"
    "v_sub_f32 v71, 0, v91\n"
    "v_sub_f32 v72, 0, v92\n"
    "v_sub_f32 v73, 0, v92\n"
    "v_sub_f32 v74, 0, v92\n"
    "v_sub_f32 v75, 0, v92\n"
    "v_sub_f32 v76, 0, v93\n"
    "v_sub_f32 v77, 0, v93\n"
    "v_sub_f32 v78, 0, v93\n"
    "v_sub_f32 v79, 0, v93\n"
    "s_waitcnt lgkmcnt(0)\n"
    "s_nop 1\n"
    "s_branch L_attn_entry\n"
    "L_attn_end:\n"
      : "=&{v[80:87]}"(ls), "=&{v[100:103]}"(c8), "=&{v104}"(cO0)
      : "s"(ws+WS_H), "s"(Wq), "s"(pK), "s"(pV), "s"(offX), "s"(offW), "s"(wdst),
        "v"(vXl), "v"(vWr0), "v"(vWr1), "v"(vWd[0]), "v"(vKr0), "v"(vKr1), "v"(vKp0), "v"(vKp1), "v"(vVp), "s"(pPF), "s"(szPF), "s"(cofs),
        "s"(pMw), "s"(szMw), "v"(vMw2), "s"(mode), "s"(ws+WS_O), "v"(vOb), "s"(cv), "v"(vC8), "s"(CNT(rt,C_O+sp)), "s"(pHF), "s"(tagLo), "s"(tagHi), "v"(vWq0)
      : "memory", "vcc", "scc", "m0", "s36", "s37", "s38", "s39", "s40", "s41", "s42", "s43", "s44", "s45", "s46", "s47", "s48", "s49", "s50", "s51", "s52", "s53", "s54", "s55", "s56", "s57", "s58", "s59", "s60", "s61", "s62", "s63", "s64", "s65", "s66", "s67", "s68", "s69", "s70", "s71", "s72", "s73", "s74", "s75", "s76", "s77", "s78", "s79", "s80", "s81", "s82", "s83", "s84", "s85", "s86", "s87", "s88", "s89", "s90", "s91", "s92", "s93", "s94", "s95", "s96", "s97", "s98", "s99", "v0", "v1", "v2", "v3", "v4", "v5", "v6", "v7", "v8", "v9", "v10", "v11", "v12", "v13", "v14", "v15", "v16", "v17", "v18", "v19", "v20", "v21", "v22", "v23", "v24", "v25", "v26", "v27", "v28", "v29", "v30", "v31", "v32", "v33", "v34", "v35", "v36", "v37", "v38", "v39", "v40", "v41", "v42", "v43", "v44", "v45", "v46", "v47", "v48", "v49", "v50", "v51", "v52", "v53", "v54", "v55", "v56", "v57", "v58", "v59", "v60", "v61", "v62", "v63", "v64", "v65", "v66", "v67", "v68", "v69", "v70", "v71", "v72", "v73", "v74", "v75", "v76", "v77", "v78", "v79", "v88", "v89", "v90", "v91", "v92", "v93", "v94", "v95", "v96", "v97", "v98", "v99", "v105", "v106", "v107", "v108", "v109", "v110", "v111", "v112", "v113", "v114", "v115", "v116", "v117", "v118", "v119", "v120", "v121", "v122", "v123", "v124", "v125", "v126", "v127", "v128", "v129", "v130", "v131", "v132", "v133", "v134", "v135", "v136", "v137", "v138", "v139", "v140", "v141", "v142", "v143", "v144", "v145", "v146", "v147", "v148", "v149", "v150", "v151", "v152", "v153", "v154", "v155", "v156", "v157", "v158", "v159", "v160", "v161", "v162", "v163", "v164", "v165", "v166", "v167", "v168", "v169", "v170", "v171", "v172", "v173", "v174", "v175", "v176", "v177", "v178", "v179", "v180", "v181", "v182", "v183", "v184", "v185", "v186", "v187", "v188", "v189", "v190", "v191", "v192", "v193", "v194", "v195", "v196", "v197", "v198", "v199", "v200", "v201", "v202", "v203", "v204", "v205", "v206", "v207", "v208", "v209", "v210", "v211", "v212", "v213", "v214", "v215", "v216", "v217", "v218", "v219", "a0", "a1", "a2", "a3", "a4", "a5", "a6", "a7", "a8", "a9", "a10", "a11", "a12", "a13", "a14", "a15", "a16", "a17", "a18", "a19", "a20", "a21", "a22", "a23", "a24", "a25", "a26", "a27", "a28", "a29", "a30", "a31", "a32", "a33", "a34", "a35", "a36", "a37", "a38", "a39", "a40", "a41", "a42", "a43", "a44", "a45", "a46", "a47", "a48", "a49", "a50", "a51", "a52", "a53", "a54", "a55", "a56", "a57", "a58", "a59", "a60", "a61", "a62", "a63", "a64", "a65", "a66", "a67", "a68", "a69", "a70", "a71", "a72", "a73", "a74", "a75", "a76", "a77", "a78", "a79", "a80", "a81", "a82", "a83", "a84", "a85", "a86", "a87", "a88", "a89", "a90", "a91", "a92", "a93", "a94", "a95", "a96", "a97", "a98", "a99", "a100", "a101", "a102", "a103", "a104", "a105", "a106", "a107", "a108", "a109", "a110", "a111", "a112", "a113", "a114", "a115", "a116", "a117", "a118", "a119", "a120", "a121", "a122", "a123", "a124", "a125", "a126", "a127", "a128", "a129", "a130", "a131", "a132", "a133", "a134", "a135", "a136", "a137", "a138", "a139", "a140", "a141", "a142", "a143", "a144", "a145", "a146", "a147", "a148", "a149", "a150", "a151", "a152", "a153", "a154", "a155", "a156", "a157", "a158", "a159", "a160", "a161", "a162", "a163", "a164", "a165", "a166", "a167", "a168", "a169", "a170", "a171", "a172", "a173", "a174", "a175", "a176", "a177", "a178", "a179", "a180", "a181", "a182", "a183", "a184", "a185", "a186", "a187", "a188", "a189", "a190", "a191", "a192", "a193", "a194", "a195", "a196", "a197", "a198", "a199", "a200", "a201", "a202", "a203", "a204", "a205", "a206", "a207", "a208", "a209", "a210", "a211", "a212", "a213", "a214", "a215", "a216", "a217", "a218", "a219", "a220", "a221", "a222", "a223", "a224", "a225", "a226", "a227", "a228", "a229", "a230", "a231", "a232", "a233", "a234", "a235", "a236", "a237", "a238", "a239", "a240", "a241", "a242", "a243", "a244", "a245", "a246", "a247", "a248", "a249", "a250", "a251", "a252", "a253", "a254", "a255");
   { PHASE() LANE() (void)g;(void)i16;(void)q;(void)p;(void)vXl;
    u32x4 mw[8];
    #pragma unroll
    for(int i=0;i<8;i++) mw[i]=*(const u32x4*)(smem_+w*8192+i*1024+lane*16);
    const float lsc=fminf(fminf(ls[0],ls[1]),fminf(ls[2],ls[3]))*1.1529215e18f;
    const bool bad=!(ls[0]<=1.329228e36f && ls[1]<=1.329228e36f && ls[2]<=1.329228e36f && ls[3]<=1.329228e36f && ls[4]<=3.0e38f && ls[4]*lsc*lsc>=2.5849394e-26f);
    { float a[8]={0,0,0,0,0,0,0,0}; float* red=(float*)(smem_+49152+16+w*4096);
      #pragma unroll
      for(int i=0;i<8;i++){ const float c_=(i&1)?bfhi(c8[i>>1]):bflo(c8[i>>1]), sv=rb(c_*__builtin_amdgcn_rcpf(1.f+__expf(-c_)));
        #pragma unroll
        for(int e=0;e<4;e++){ a[2*e]+=sv*bflo(mw[i][e]); a[2*e+1]+=sv*bfhi(mw[i][e]); } }
      #pragma unroll
      for(int e=0;e<8;e++){ a[e]+=dppf<0x124>(a[e]); a[e]+=dppf<0x128>(a[e]); a[e]=sumg(a[e]); }
      if(lane<4){ *(f32x4*)(red+lane*8)=(f32x4){a[0],a[1],a[2],a[3]}; *(f32x4*)(red+lane*8+4)=(f32x4){a[4],a[5],a[6],a[7]}; }
    }
    unsigned* fl=(unsigned*)(smem_+49152);
    { const unsigned long long bb=__builtin_amdgcn_ballot_w64(bad); if(lane==0) fl[w*1024]=(bb!=0ull?1u:0u)|(cO0<32u?2u:0u); }

    BAR_();
    const unsigned fw=rfl((fl[0]|fl[1024])|(fl[2048]|fl[3072]));
    needO=fw&2u;
    if((fw&1u)==0u || pass!=0u) break;
    DRAIN_BAR();
   }
  }
  }
  { PHASE() LANE() (void)g;(void)i16;(void)q;(void)p;(void)vXl;
    if(tid_==0 && bid_==0) __hip_atomic_store((gu64*)(ws+WS_EP), ((unsigned long long)tagHi<<32)|tagLo, __ATOMIC_RELAXED, __HIP_MEMORY_SCOPE_AGENT);
    PRE_W128(Wo,4096u*2048u,sp*1024)
    WAITVM(10); ADD1(CNT(qblk,C_O+(hh>>3)));

    { const float* red=(const float*)(smem_+49152+16);
      if(w==1 && lane<32){
        const float v=(red[lane]+red[1024+lane])+(red[2048+lane]+red[3072+lane]);
        const unsigned hi=((__float_as_uint(v)+0x80u)&0xffffff00u)|(tagHi&0xffu);
        __hip_atomic_store((gu64*)(ws+WS_MODF)+(loc&1)*4096+(xcd*16+(loc>>1))*32+fpos32(lane), ((unsigned long long)hi<<32)|tagLo, __ATOMIC_RELAXED, __HIP_MEMORY_SCOPE_AGENT); } }
    if(w!=0) PRE_G112()
  }
  { PHASE() LANE() W128_RD() W128_DMA(2048u) X_FRAG()
    const unsigned T=rt*4+w, col0=ct*128;
    const unsigned offX=rfl((T*128+sp*32)*4096), offW=rfl((sp*1024+16*w)*2048+col0*2), wdst=rfl(w*4096);

    f32x32 a0,a1,a2,a3; u32x16 xr; const unsigned cofs=rfl((loc*4+w)*1024);
    asm volatile(
    "s_mov_b64 s[36:37], %5\n"
    "s_mov_b32 s38, %6\n"
    "s_mov_b32 s39, 0x20000\n"
    "s_mov_b64 s[40:41], %7\n"
    "s_mov_b32 s42, %8\n"
    "s_mov_b32 s43, 0x20000\n"
    "s_mov_b32 s52, %9\n"
    "s_mov_b32 s53, %10\n"
    "s_mov_b32 s56, %11\n"
    "s_mov_b32 s57, 0\n"
    "s_mov_b32 s58, 0\n"
    "v_add_u32 v128, 0, %13\n"
    "v_add_u32 v129, 0, %14\n"
    "v_mov_b32 v0, 0\n"
    "v_mov_b32 v1, 0\n"
    "v_mov_b32 v2, 0\n"
    "v_mov_b32 v3, 0\n"
    "v_mov_b32 v4, 0\n"
    "v_mov_b32 v5, 0\n"
    "v_mov_b32 v6, 0\n"
    "v_mov_b32 v7, 0\n"
    "v_mov_b32 v8, 0\n"
    "v_mov_b32 v9, 0\n"
    "v_mov_b32 v10, 0\n"
    "v_mov_b32 v11, 0\n"
    "v_mov_b32 v12, 0\n"
    "v_mov_b32 v13, 0\n"
    "v_mov_b32 v14, 0\n"
    "v_mov_b32 v15, 0\n"
    "v_mov_b32 v16, 0\n"
    "v_mov_b32 v17, 0\n"
    "v_mov_b32 v18, 0\n"
    "v_mov_b32 v19, 0\n"
    "v_mov_b32 v20, 0\n"
    "v_mov_b32 v21, 0\n"
    "v_mov_b32 v22, 0\n"
    "v_mov_b32 v23, 0\n"
    "v_mov_b32 v24, 0\n"
    "v_mov_b32 v25, 0\n"
    "v_mov_b32 v26, 0\n"
    "v_mov_b32 v27, 0\n"
    "v_mov_b32 v28, 0\n"
    "v_mov_b32 v29, 0\n"
    "v_mov_b32 v30, 0\n"
    "v_mov_b32 v31, 0\n"
    "v_mov_b32 v32, 0\n"
    "v_mov_b32 v33, 0\n"
    "v_mov_b32 v34, 0\n"
    "v_mov_b32 v35, 0\n"
    "v_mov_b32 v36, 0\n"
    "v_mov_b32 v37, 0\n"
    "v_mov_b32 v38, 0\n"
    "v_mov_b32 v39, 0\n"
    "v_mov_b32 v40, 0\n"
    "v_mov_b32 v41, 0\n"
    "v_mov_b32 v42, 0\n"
    "v_mov_b32 v43, 0\n"
    "v_mov_b32 v44, 0\n"
    "v_mov_b32 v45, 0\n"
    "v_mov_b32 v46, 0\n"
    "v_mov_b32 v47, 0\n"
    "v_mov_b32 v48, 0\n"
    "v_mov_b32 v49, 0\n"
    "v_mov_b32 v50, 0\n"
    "v_mov_b32 v51, 0\n"
    "v_mov_b32 v52, 0\n"
    "v_mov_b32 v53, 0\n"
    "v_mov_b32 v54, 0\n"
    "v_mov_b32 v55, 0\n"
    "v_mov_b32 v56, 0\n"
    "v_mov_b32 v57, 0\n"
    "v_mov_b32 v58, 0\n"
    "v_mov_b32 v59, 0\n"
    "v_mov_b32 v60, 0\n"
    "v_mov_b32 v61, 0\n"
    "v_mov_b32 v62, 0\n"
    "v_mov_b32 v63, 0\n"
    "v_mov_b32 v64, 0\n"
    "v_mov_b32 v65, 0\n"
    "v_mov_b32 v66, 0\n"
    "v_mov_b32 v67, 0\n"
    "v_mov_b32 v68, 0\n"
    "v_mov_b32 v69, 0\n"
    "v_mov_b32 v70, 0\n"
    "v_mov_b32 v71, 0\n"
    "v_mov_b32 v72, 0\n"
    "v_mov_b32 v73, 0\n"
    "v_mov_b32 v74, 0\n"
    "v_mov_b32 v75, 0\n"
    "v_mov_b32 v76, 0\n"
    "v_mov_b32 v77, 0\n"
    "v_mov_b32 v78, 0\n"
    "v_mov_b32 v79, 0\n"
    "v_mov_b32 v80, 0\n"
    "v_mov_b32 v81, 0\n"
    "v_mov_b32 v82, 0\n"
    "v_mov_b32 v83, 0\n"
    "v_mov_b32 v84, 0\n"
    "v_mov_b32 v85, 0\n"
    "v_mov_b32 v86, 0\n"
    "v_mov_b32 v87, 0\n"
    "v_mov_b32 v88, 0\n"
    "v_mov_b32 v89, 0\n"
    "v_mov_b32 v90, 0\n"
    "v_mov_b32 v91, 0\n"
    "v_mov_b32 v92, 0\n"
    "v_mov_b32 v93, 0\n"
    "v_mov_b32 v94, 0\n"
    "v_mov_b32 v95, 0\n"
    "v_mov_b32 v96, 0\n"
    "v_mov_b32 v97, 0\n"
    "v_mov_b32 v98, 0\n"
    "v_mov_b32 v99, 0\n"
    "v_mov_b32 v100, 0\n"
    "v_mov_b32 v101, 0\n"
    "v_mov_b32 v102, 0\n"
    "v_mov_b32 v103, 0\n"
    "v_mov_b32 v104, 0\n"
    "v_mov_b32 v105, 0\n"
    "v_mov_b32 v106, 0\n"
    "v_mov_b32 v107, 0\n"
    "v_mov_b32 v108, 0\n"
    "v_mov_b32 v109, 0\n"
    "v_mov_b32 v110, 0\n"
    "v_mov_b32 v111, 0\n"
    "v_mov_b32 v112, 0\n"
    "v_mov_b32 v113, 0\n"
    "v_mov_b32 v114, 0\n"
    "v_mov_b32 v115, 0\n"
    "v_mov_b32 v116, 0\n"
    "v_mov_b32 v117, 0\n"
    "v_mov_b32 v118, 0\n"
    "v_mov_b32 v119, 0\n"
    "v_mov_b32 v120, 0\n"
    "v_mov_b32 v121, 0\n"
    "v_mov_b32 v122, 0\n"
    "v_mov_b32 v123, 0\n"
    "v_mov_b32 v124, 0\n"
    "v_mov_b32 v125, 0\n"
    "v_mov_b32 v126, 0\n"
    "v_mov_b32 v127, 0\n"
    "s_cmp_lg_u32 %11, 0\n"
    "s_cbranch_scc1 L_k3_g_done\n"
    "s_cmp_eq_u32 %23, 0\n"
    "s_cbranch_scc1 L_k3_g_done\n"
    "s_mov_b32 s90, 4\n"
    "s_mov_b32 s91, 0x20000\n"
    "s_mov_b32 s92, 131072\n"
    "s_mov_b64 s[88:89], %22\n"
    "L_k3_g_poll0:\n"
    "buffer_load_dword v130, off, s[88:91], 0 sc1\n"
    "s_waitcnt vmcnt(0)\n"
    "v_readfirstlane_b32 s93, v130\n"
    "s_nop 1\n"
    "s_cmp_ge_u32 s93, 32\n"
    "s_cbranch_scc1 L_k3_g_ok0\n"
    "s_sleep 1\n"
    "s_sub_u32 s92, s92, 1\n"
    "s_cmp_gt_i32 s92, 0\n"
    "s_cbranch_scc1 L_k3_g_poll0\n"
    "L_k3_g_ok0:\n"
    "L_k3_g_done:\n"
    "s_barrier\n"
    "s_getpc_b64 s[80:81]\n"
    "L_pc1:\n"
    "s_sub_u32 s80, s80, L_pc1-L_mega_code\n"
    "s_subb_u32 s81, s81, 0\n"
    "s_mov_b32 s82, L_text_end-L_mega_code\n"
    "s_mov_b32 s83, 0x20000\n"
    "s_and_b32 s84, %21, 3072\n"
    "s_add_u32 m0, s84, 159744\n"
    "s_nop 0\n"
    "buffer_load_dwordx4 %12, s[80:83], %21 offen lds\n"
    "s_add_u32 m0, s57, s56\n"
    "s_add_u32 m0, m0, 1024\n"
    "s_add_u32 m0, m0, 1024\n"
    "s_add_u32 m0, m0, 1024\n"
    "s_add_u32 s53, s53, 131072\n"
    "s_add_u32 s57, s57, 16384\n"
    "s_cmp_ge_u32 s57, 49152\n"
    "s_cselect_b32 s57, 0, s57\n"
    "buffer_load_dwordx4 a[64:67], %12, s[36:39], s52 offen sc1\n"
    "buffer_load_dwordx4 a[68:71], %12, s[36:39], s52 offen offset:1024 sc1\n"
    "buffer_load_dwordx4 a[72:75], %12, s[36:39], s52 offen offset:2048 sc1\n"
    "buffer_load_dwordx4 a[76:79], %12, s[36:39], s52 offen offset:3072 sc1\n"
    "s_add_u32 s52, s52, 4096\n"
    "buffer_load_dwordx4 a[80:83], %12, s[36:39], s52 offen sc1\n"
    "buffer_load_dwordx4 a[84:87], %12, s[36:39], s52 offen offset:1024 sc1\n"
    "buffer_load_dwordx4 a[88:91], %12, s[36:39], s52 offen offset:2048 sc1\n"
    "buffer_load_dwordx4 a[92:95], %12, s[36:39], s52 offen offset:3072 sc1\n"
    "s_add_u32 s52, s52, 4096\n"
    "s_add_u32 m0, s57, s56\n"
    "s_add_u32 m0, m0, 1024\n"
    "s_add_u32 m0, m0, 1024\n"
    "s_add_u32 m0, m0, 1024\n"
    "s_add_u32 s53, s53, 131072\n"
    "s_add_u32 s57, s57, 16384\n"
    "s_cmp_ge_u32 s57, 49152\n"
    "s_cselect_b32 s57, 0, s57\n"
    "buffer_load_dwordx4 a[96:99], %12, s[36:39], s52 offen sc1\n"
    "buffer_load_dwordx4 a[100:103], %12, s[36:39], s52 offen offset:1024 sc1\n"
    "buffer_load_dwordx4 a[104:107], %12, s[36:39], s52 offen offset:2048 sc1\n"
    "buffer_load_dwordx4 a[108:111], %12, s[36:39], s52 offen offset:3072 sc1\n"
    "s_add_u32 s52, s52, 4096\n"
    "buffer_load_dwordx4 a[112:115], %12, s[36:39], s52 offen sc1\n"
    "buffer_load_dwordx4 a[116:119], %12, s[36:39], s52 offen offset:1024 sc1\n"
    "buffer_load_dwordx4 a[120:123], %12, s[36:39], s52 offen offset:2048 sc1\n"
    "buffer_load_dwordx4 a[124:127], %12, s[36:39], s52 offen offset:3072 sc1\n"
    "s_add_u32 s52, s52, 4096\n"
    "s_add_u32 m0, s57, s56\n"
    "s_add_u32 m0, m0, 1024\n"
    "s_mov_b32 s60, 14\n"
    "s_mov_b32 s59, 15\n"
    "s_mov_b32 s55, 5\n"
    "s_mov_b64 s[68:69], %19\n"
    "s_mov_b32 s70, 4194304\n"
    "s_mov_b32 s71, 0x20000\n"
    "s_mov_b32 s76, 0\n"
    "buffer_load_dwordx4 v[132:135], %20, s[68:71], s76 offen sc1\n"
    "s_add_u32 s76, s76, 8192\n"
    "buffer_load_dwordx4 v[136:139], %20, s[68:71], s76 offen sc1\n"
    "s_add_u32 s76, s76, 8192\n"
    "buffer_load_dwordx4 v[140:143], %20, s[68:71], s76 offen sc1\n"
    "s_add_u32 s76, s76, 8192\n"
    "buffer_load_dwordx4 v[144:147], %20, s[68:71], s76 offen sc1\n"
    "s_add_u32 s76, s76, 8192\n"
    "s_waitcnt vmcnt(12)\n"
    "s_barrier\n"
    "ds_read_b64_tr_b16 a[0:1], v128 offset:0\n"
    "ds_read_b64_tr_b16 a[2:3], v128 offset:4096\n"
    "ds_read_b64_tr_b16 a[4:5], v128 offset:8192\n"
    "ds_read_b64_tr_b16 a[6:7], v128 offset:12288\n"
    "ds_read_b64_tr_b16 a[8:9], v129 offset:0\n"
    "ds_read_b64_tr_b16 a[10:11], v129 offset:4096\n"
    "ds_read_b64_tr_b16 a[12:13], v129 offset:8192\n"
    "ds_read_b64_tr_b16 a[14:15], v129 offset:12288\n"
    "ds_read_b64_tr_b16 a[16:17], v128 offset:512\n"
    "ds_read_b64_tr_b16 a[18:19], v128 offset:4608\n"
    "ds_read_b64_tr_b16 a[20:21], v128 offset:8704\n"
    "ds_read_b64_tr_b16 a[22:23], v128 offset:12800\n"
    "ds_read_b64_tr_b16 a[24:25], v129 offset:512\n"
    "ds_read_b64_tr_b16 a[26:27], v129 offset:4608\n"
    "ds_read_b64_tr_b16 a[28:29], v129 offset:8704\n"
    "ds_read_b64_tr_b16 a[30:31], v129 offset:12800\n"
    "s_waitcnt lgkmcnt(0)\n"
    ".p2align 6\n"
    "L_k3_loop:\n"
    "v_mfma_f32_16x16x32_bf16 v[0:3], a[0:3], a[64:67], v[0:3]\n"
    "s_sub_u32 s59, s59, 1\n"
    "ds_read_b64_tr_b16 a[32:33], v128 offset:1024\n"
    "v_mfma_f32_16x16x32_bf16 v[0:3], a[4:7], a[80:83], v[0:3]\n"
    "s_cmp_gt_i32 s59, 0\n"
    "ds_read_b64_tr_b16 a[34:35], v128 offset:5120\n"
    "v_mfma_f32_16x16x32_bf16 v[4:7], a[4:7], a[84:87], v[4:7]\n"
    "s_cselect_b32 s38, s38, 0\n"
    "ds_read_b64_tr_b16 a[36:37], v128 offset:9216\n"
    "v_mfma_f32_16x16x32_bf16 v[4:7], a[0:3], a[68:71], v[4:7]\n"
    "ds_read_b64_tr_b16 a[38:39], v128 offset:13312\n"
    "v_mfma_f32_16x16x32_bf16 v[8:11], a[0:3], a[72:75], v[8:11]\n"
    "buffer_load_dwordx4 a[128:131], %12, s[36:39], s52 offen sc1\n"
    "v_mfma_f32_16x16x32_bf16 v[8:11], a[4:7], a[88:91], v[8:11]\n"
    "ds_read_b64_tr_b16 a[40:41], v129 offset:1024\n"
    "v_mfma_f32_16x16x32_bf16 v[12:15], a[4:7], a[92:95], v[12:15]\n"
    "s_add_u32 m0, m0, 1024\n"
    "ds_read_b64_tr_b16 a[42:43], v129 offset:5120\n"
    "v_mfma_f32_16x16x32_bf16 v[12:15], a[0:3], a[76:79], v[12:15]\n"
    "buffer_load_dwordx4 %17, s[40:43], s53 offen lds\n"
    "v_mfma_f32_16x16x32_bf16 v[28:31], a[8:11], a[76:79], v[28:31]\n"
    "ds_read_b64_tr_b16 a[44:45], v129 offset:9216\n"
    "v_mfma_f32_16x16x32_bf16 v[28:31], a[12:15], a[92:95], v[28:31]\n"
    "ds_read_b64_tr_b16 a[46:47], v129 offset:13312\n"
    "v_mfma_f32_16x16x32_bf16 v[24:27], a[12:15], a[88:91], v[24:27]\n"
    "buffer_load_dwordx4 a[132:135], %12, s[36:39], s52 offen offset:1024 sc1\n"
    "v_mfma_f32_16x16x32_bf16 v[24:27], a[8:11], a[72:75], v[24:27]\n"
    "ds_read_b64_tr_b16 a[48:49], v128 offset:1536\n"
    "v_mfma_f32_16x16x32_bf16 v[20:23], a[8:11], a[68:71], v[20:23]\n"
    "s_add_u32 m0, m0, 1024\n"
    "ds_read_b64_tr_b16 a[50:51], v128 offset:5632\n"
    "v_mfma_f32_16x16x32_bf16 v[20:23], a[12:15], a[84:87], v[20:23]\n"
    "buffer_load_dwordx4 %18, s[40:43], s53 offen lds\n"
    "v_mfma_f32_16x16x32_bf16 v[16:19], a[12:15], a[80:83], v[16:19]\n"
    "s_add_u32 s53, s53, 131072\n"
    "ds_read_b64_tr_b16 a[52:53], v128 offset:9728\n"
    "v_mfma_f32_16x16x32_bf16 v[16:19], a[8:11], a[64:67], v[16:19]\n"
    "s_add_u32 s57, s57, 16384\n"
    "ds_read_b64_tr_b16 a[54:55], v128 offset:13824\n"
    "v_mfma_f32_16x16x32_bf16 v[32:35], a[16:19], a[64:67], v[32:35]\n"
    "s_cmp_ge_u32 s57, 49152\n"
    "buffer_load_dwordx4 a[136:139], %12, s[36:39], s52 offen offset:2048 sc1\n"
    "v_mfma_f32_16x16x32_bf16 v[32:35], a[20:23], a[80:83], v[32:35]\n"
    "s_cselect_b32 s57, 0, s57\n"
    "ds_read_b64_tr_b16 a[56:57], v129 offset:1536\n"
    "v_mfma_f32_16x16x32_bf16 v[36:39], a[20:23], a[84:87], v[36:39]\n"
    "ds_read_b64_tr_b16 a[58:59], v129 offset:5632\n"
    "v_mfma_f32_16x16x32_bf16 v[36:39], a[16:19], a[68:71], v[36:39]\n"
    "buffer_load_dwordx4 a[140:143], %12, s[36:39], s52 offen offset:3072 sc1\n"
    "v_mfma_f32_16x16x32_bf16 v[40:43], a[16:19], a[72:75], v[40:43]\n"
    "s_add_u32 s52, s52, 4096\n"
    "ds_read_b64_tr_b16 a[60:61], v129 offset:9728\n"
    "v_mfma_f32_16x16x32_bf16 v[40:43], a[20:23], a[88:91], v[40:43]\n"
    "ds_read_b64_tr_b16 a[62:63], v129 offset:13824\n"
    "v_mfma_f32_16x16x32_bf16 v[44:47], a[20:23], a[92:95], v[44:47]\n"
    "s_add_u32 s58, s58, 16384\n"
    "v_mfma_f32_16x16x32_bf16 v[44:47], a[16:19], a[76:79], v[44:47]\n"
    "s_cmp_ge_u32 s58, 49152\n"
    "v_mfma_f32_16x16x32_bf16 v[60:63], a[24:27], a[76:79], v[60:63]\n"
    "s_cselect_b32 s58, 0, s58\n"
    "v_mfma_f32_16x16x32_bf16 v[60:63], a[28:31], a[92:95], v[60:63]\n"
    "v_add_u32 v128, s58, %13\n"
    "v_add_u32 v129, s58, %14\n"
    "v_mfma_f32_16x16x32_bf16 v[56:59], a[28:31], a[88:91], v[56:59]\n"
    "v_mfma_f32_16x16x32_bf16 v[56:59], a[24:27], a[72:75], v[56:59]\n"
    "v_mfma_f32_16x16x32_bf16 v[52:55], a[24:27], a[68:71], v[52:55]\n"
    "v_mfma_f32_16x16x32_bf16 v[52:55], a[28:31], a[84:87], v[52:55]\n"
    "v_mfma_f32_16x16x32_bf16 v[48:51], a[28:31], a[80:83], v[48:51]\n"
    "v_mfma_f32_16x16x32_bf16 v[48:51], a[24:27], a[64:67], v[48:51]\n"
    "s_waitcnt vmcnt(14) lgkmcnt(0)\n"
    "v_mfma_f32_16x16x32_bf16 v[64:67], a[32:35], a[64:67], v[64:67]\n"
    "s_barrier\n"
    "v_mfma_f32_16x16x32_bf16 v[64:67], a[36:39], a[80:83], v[64:67]\n"
    "s_sub_u32 s60, s60, 1\n"
    "ds_read_b64_tr_b16 a[0:1], v128 offset:0\n"
    "v_mfma_f32_16x16x32_bf16 v[68:71], a[36:39], a[84:87], v[68:71]\n"
    "s_cmp_gt_i32 s60, 0\n"
    "ds_read_b64_tr_b16 a[2:3], v128 offset:4096\n"
    "v_mfma_f32_16x16x32_bf16 v[68:71], a[32:35], a[68:71], v[68:71]\n"
    "s_cselect_b32 s42, s42, 0\n"
    "ds_read_b64_tr_b16 a[4:5], v128 offset:8192\n"
    "v_mfma_f32_16x16x32_bf16 v[72:75], a[32:35], a[72:75], v[72:75]\n"
    "ds_read_b64_tr_b16 a[6:7], v128 offset:12288\n"
    "v_mfma_f32_16x16x32_bf16 v[72:75], a[36:39], a[88:91], v[72:75]\n"
    "buffer_load_dwordx4 a[144:147], %12, s[36:39], s52 offen sc1\n"
    "v_mfma_f32_16x16x32_bf16 v[76:79], a[36:39], a[92:95], v[76:79]\n"
    "ds_read_b64_tr_b16 a[8:9], v129 offset:0\n"
    "v_mfma_f32_16x16x32_bf16 v[76:79], a[32:35], a[76:79], v[76:79]\n"
    "ds_read_b64_tr_b16 a[10:11], v129 offset:4096\n"
    "v_mfma_f32_16x16x32_bf16 v[92:95], a[40:43], a[76:79], v[92:95]\n"
    "s_add_u32 m0, s57, s56\n"
    "ds_read_b64_tr_b16 a[12:13], v129 offset:8192\n"
    "v_mfma_f32_16x16x32_bf16 v[92:95], a[44:47], a[92:95], v[92:95]\n"
    "buffer_load_dwordx4 %15, s[40:43], s53 offen lds\n"
    "v_mfma_f32_16x16x32_bf16 v[88:91], a[44:47], a[88:91], v[88:91]\n"
    "ds_read_b64_tr_b16 a[14:15], v129 offset:12288\n"
    "v_mfma_f32_16x16x32_bf16 v[88:91], a[40:43], a[72:75], v[88:91]\n"
    "ds_read_b64_tr_b16 a[16:17], v128 offset:512\n"
    "v_mfma_f32_16x16x32_bf16 v[84:87], a[40:43], a[68:71], v[84:87]\n"
    "ds_read_b64_tr_b16 a[18:19], v128 offset:4608\n"
    "v_mfma_f32_16x16x32_bf16 v[84:87], a[44:47], a[84:87], v[84:87]\n"
    "buffer_load_dwordx4 a[148:151], %12, s[36:39], s52 offen offset:1024 sc1\n"
    "v_mfma_f32_16x16x32_bf16 v[80:83], a[44:47], a[80:83], v[80:83]\n"
    "ds_read_b64_tr_b16 a[20:21], v128 offset:8704\n"
    "v_mfma_f32_16x16x32_bf16 v[80:83], a[40:43], a[64:67], v[80:83]\n"
    "ds_read_b64_tr_b16 a[22:23], v128 offset:12800\n"
    "v_mfma_f32_16x16x32_bf16 v[96:99], a[48:51], a[64:67], v[96:99]\n"
    "s_add_u32 m0, m0, 1024\n"
    "ds_read_b64_tr_b16 a[24:25], v129 offset:512\n"
    "v_mfma_f32_16x16x32_bf16 v[96:99], a[52:55], a[80:83], v[96:99]\n"
    "buffer_load_dwordx4 %16, s[40:43], s53 offen lds\n"
    "v_mfma_f32_16x16x32_bf16 v[100:103], a[52:55], a[84:87], v[100:103]\n"
    "ds_read_b64_tr_b16 a[26:27], v129 offset:4608\n"
    "v_mfma_f32_16x16x32_bf16 v[100:103], a[48:51], a[68:71], v[100:103]\n"
    "ds_read_b64_tr_b16 a[28:29], v129 offset:8704\n"
    "v_mfma_f32_16x16x32_bf16 v[104:107], a[48:51], a[72:75], v[104:107]\n"
    "ds_read_b64_tr_b16 a[30:31], v129 offset:12800\n"
    "v_mfma_f32_16x16x32_bf16 v[104:107], a[52:55], a[88:91], v[104:107]\n"
    "buffer_load_dwordx4 a[152:155], %12, s[36:39], s52 offen offset:2048 sc1\n"
    "v_mfma_f32_16x16x32_bf16 v[108:111], a[52:55], a[92:95], v[108:111]\n"
    "v_mfma_f32_16x16x32_bf16 v[108:111], a[48:51], a[76:79], v[108:111]\n"
    "v_mfma_f32_16x16x32_bf16 v[124:127], a[56:59], a[76:79], v[124:127]\n"
    "v_mfma_f32_16x16x32_bf16 v[124:127], a[60:63], a[92:95], v[124:127]\n"
    "buffer_load_dwordx4 a[156:159], %12, s[36:39], s52 offen offset:3072 sc1\n"
    "v_mfma_f32_16x16x32_bf16 v[120:123], a[60:63], a[88:91], v[120:123]\n"
    "s_add_u32 s52, s52, 4096\n"
    "v_mfma_f32_16x16x32_bf16 v[120:123], a[56:59], a[72:75], v[120:123]\n"
    "v_mfma_f32_16x16x32_bf16 v[116:119], a[56:59], a[68:71], v[116:119]\n"
    "v_mfma_f32_16x16x32_bf16 v[116:119], a[60:63], a[84:87], v[116:119]\n"
    "v_mfma_f32_16x16x32_bf16 v[112:115], a[60:63], a[80:83], v[112:115]\n"
    "v_mfma_f32_16x16x32_bf16 v[112:115], a[56:59], a[64:67], v[112:115]\n"
    "s_waitcnt vmcnt(12) lgkmcnt(0)\n"
    "v_mfma_f32_16x16x32_bf16 v[0:3], a[0:3], a[96:99], v[0:3]\n"
    "s_sub_u32 s59, s59, 1\n"
    "ds_read_b64_tr_b16 a[32:33], v128 offset:1024\n"
    "v_mfma_f32_16x16x32_bf16 v[0:3], a[4:7], a[112:115], v[0:3]\n"
    "s_cmp_gt_i32 s59, 0\n"
    "ds_read_b64_tr_b16 a[34:35], v128 offset:5120\n"
    "v_mfma_f32_16x16x32_bf16 v[4:7], a[4:7], a[116:119], v[4:7]\n"
    "s_cselect_b32 s38, s38, 0\n"
    "ds_read_b64_tr_b16 a[36:37], v128 offset:9216\n"
    "v_mfma_f32_16x16x32_bf16 v[4:7], a[0:3], a[100:103], v[4:7]\n"
    "ds_read_b64_tr_b16 a[38:39], v128 offset:13312\n"
    "v_mfma_f32_16x16x32_bf16 v[8:11], a[0:3], a[104:107], v[8:11]\n"
    "buffer_load_dwordx4 a[64:67], %12, s[36:39], s52 offen sc1\n"
    "v_mfma_f32_16x16x32_bf16 v[8:11], a[4:7], a[120:123], v[8:11]\n"
    "ds_read_b64_tr_b16 a[40:41], v129 offset:1024\n"
    "v_mfma_f32_16x16x32_bf16 v[12:15], a[4:7], a[124:127], v[12:15]\n"
    "s_add_u32 m0, m0, 1024\n"
    "ds_read_b64_tr_b16 a[42:43], v129 offset:5120\n"
    "v_mfma_f32_16x16x32_bf16 v[12:15], a[0:3], a[108:111], v[12:15]\n"
    "buffer_load_dwordx4 %17, s[40:43], s53 offen lds\n"
    "v_mfma_f32_16x16x32_bf16 v[28:31], a[8:11], a[108:111], v[28:31]\n"
    "ds_read_b64_tr_b16 a[44:45], v129 offset:9216\n"
    "v_mfma_f32_16x16x32_bf16 v[28:31], a[12:15], a[124:127], v[28:31]\n"
    "ds_read_b64_tr_b16 a[46:47], v129 offset:13312\n"
    "v_mfma_f32_16x16x32_bf16 v[24:27], a[12:15], a[120:123], v[24:27]\n"
    "buffer_load_dwordx4 a[68:71], %12, s[36:39], s52 offen offset:1024 sc1\n"
    "v_mfma_f32_16x16x32_bf16 v[24:27], a[8:11], a[104:107], v[24:27]\n"
    "ds_read_b64_tr_b16 a[48:49], v128 offset:1536\n"
    "v_mfma_f32_16x16x32_bf16 v[20:23], a[8:11], a[100:103], v[20:23]\n"
    "s_add_u32 m0, m0, 1024\n"
    "ds_read_b64_tr_b16 a[50:51], v128 offset:5632\n"
    "v_mfma_f32_16x16x32_bf16 v[20:23], a[12:15], a[116:119], v[20:23]\n"
    "buffer_load_dwordx4 %18, s[40:43], s53 offen lds\n"
    "v_mfma_f32_16x16x32_bf16 v[16:19], a[12:15], a[112:115], v[16:19]\n"
    "s_add_u32 s53, s53, 131072\n"
    "ds_read_b64_tr_b16 a[52:53], v128 offset:9728\n"
    "v_mfma_f32_16x16x32_bf16 v[16:19], a[8:11], a[96:99], v[16:19]\n"
    "s_add_u32 s57, s57, 16384\n"
    "ds_read_b64_tr_b16 a[54:55], v128 offset:13824\n"
    "v_mfma_f32_16x16x32_bf16 v[32:35], a[16:19], a[96:99], v[32:35]\n"
    "s_cmp_ge_u32 s57, 49152\n"
    "buffer_load_dwordx4 a[72:75], %12, s[36:39], s52 offen offset:2048 sc1\n"
    "v_mfma_f32_16x16x32_bf16 v[32:35], a[20:23], a[112:115], v[32:35]\n"
    "s_cselect_b32 s57, 0, s57\n"
    "ds_read_b64_tr_b16 a[56:57], v129 offset:1536\n"
    "v_mfma_f32_16x16x32_bf16 v[36:39], a[20:23], a[116:119], v[36:39]\n"
    "ds_read_b64_tr_b16 a[58:59], v129 offset:5632\n"
    "v_mfma_f32_16x16x32_bf16 v[36:39], a[16:19], a[100:103], v[36:39]\n"
    "buffer_load_dwordx4 a[76:79], %12, s[36:39], s52 offen offset:3072 sc1\n"
    "v_mfma_f32_16x16x32_bf16 v[40:43], a[16:19], a[104:107], v[40:43]\n"
    "s_add_u32 s52, s52, 4096\n"
    "ds_read_b64_tr_b16 a[60:61], v129 offset:9728\n"
    "v_mfma_f32_16x16x32_bf16 v[40:43], a[20:23], a[120:123], v[40:43]\n"
    "ds_read_b64_tr_b16 a[62:63], v129 offset:13824\n"
    "v_mfma_f32_16x16x32_bf16 v[44:47], a[20:23], a[124:127], v[44:47]\n"
    "s_add_u32 s58, s58, 16384\n"
    "v_mfma_f32_16x16x32_bf16 v[44:47], a[16:19], a[108:111], v[44:47]\n"
    "s_cmp_ge_u32 s58, 49152\n"
    "v_mfma_f32_16x16x32_bf16 v[60:63], a[24:27], a[108:111], v[60:63]\n"
    "s_cselect_b32 s58, 0, s58\n"
    "v_mfma_f32_16x16x32_bf16 v[60:63], a[28:31], a[124:127], v[60:63]\n"
    "v_add_u32 v128, s58, %13\n"
    "v_add_u32 v129, s58, %14\n"
    "v_mfma_f32_16x16x32_bf16 v[56:59], a[28:31], a[120:123], v[56:59]\n"
    "v_mfma_f32_16x16x32_bf16 v[56:59], a[24:27], a[104:107], v[56:59]\n"
    "v_mfma_f32_16x16x32_bf16 v[52:55], a[24:27], a[100:103], v[52:55]\n"
    "v_mfma_f32_16x16x32_bf16 v[52:55], a[28:31], a[116:119], v[52:55]\n"
    "v_mfma_f32_16x16x32_bf16 v[48:51], a[28:31], a[112:115], v[48:51]\n"
    "v_mfma_f32_16x16x32_bf16 v[48:51], a[24:27], a[96:99], v[48:51]\n"
    "s_waitcnt vmcnt(14) lgkmcnt(0)\n"
    "v_mfma_f32_16x16x32_bf16 v[64:67], a[32:35], a[96:99], v[64:67]\n"
    "s_barrier\n"
    "v_mfma_f32_16x16x32_bf16 v[64:67], a[36:39], a[112:115], v[64:67]\n"
    "s_sub_u32 s60, s60, 1\n"
    "ds_read_b64_tr_b16 a[0:1], v128 offset:0\n"
    "v_mfma_f32_16x16x32_bf16 v[68:71], a[36:39], a[116:119], v[68:71]\n"
    "s_cmp_gt_i32 s60, 0\n"
    "ds_read_b64_tr_b16 a[2:3], v128 offset:4096\n"
    "v_mfma_f32_16x16x32_bf16 v[68:71], a[32:35], a[100:103], v[68:71]\n"
    "s_cselect_b32 s42, s42, 0\n"
    "ds_read_b64_tr_b16 a[4:5], v128 offset:8192\n"
    "v_mfma_f32_16x16x32_bf16 v[72:75], a[32:35], a[104:107], v[72:75]\n"
    "ds_read_b64_tr_b16 a[6:7], v128 offset:12288\n"
    "v_mfma_f32_16x16x32_bf16 v[72:75], a[36:39], a[120:123], v[72:75]\n"
    "buffer_load_dwordx4 a[80:83], %12, s[36:39], s52 offen sc1\n"
    "v_mfma_f32_16x16x32_bf16 v[76:79], a[36:39], a[124:127], v[76:79]\n"
    "ds_read_b64_tr_b16 a[8:9], v129 offset:0\n"
    "v_mfma_f32_16x16x32_bf16 v[76:79], a[32:35], a[108:111], v[76:79]\n"
    "ds_read_b64_tr_b16 a[10:11], v129 offset:4096\n"
    "v_mfma_f32_16x16x32_bf16 v[92:95], a[40:43], a[108:111], v[92:95]\n"
    "s_add_u32 m0, s57, s56\n"
    "ds_read_b64_tr_b16 a[12:13], v129 offset:8192\n"
    "v_mfma_f32_16x16x32_bf16 v[92:95], a[44:47], a[124:127], v[92:95]\n"
    "buffer_load_dwordx4 %15, s[40:43], s53 offen lds\n"
    "v_mfma_f32_16x16x32_bf16 v[88:91], a[44:47], a[120:123], v[88:91]\n"
    "ds_read_b64_tr_b16 a[14:15], v129 offset:12288\n"
    "v_mfma_f32_16x16x32_bf16 v[88:91], a[40:43], a[104:107], v[88:91]\n"
    "ds_read_b64_tr_b16 a[16:17], v128 offset:512\n"
    "v_mfma_f32_16x16x32_bf16 v[84:87], a[40:43], a[100:103], v[84:87]\n"
    "ds_read_b64_tr_b16 a[18:19], v128 offset:4608\n"
    "v_mfma_f32_16x16x32_bf16 v[84:87], a[44:47], a[116:119], v[84:87]\n"
    "buffer_load_dwordx4 a[84:87], %12, s[36:39], s52 offen offset:1024 sc1\n"
    "v_mfma_f32_16x16x32_bf16 v[80:83], a[44:47], a[112:115], v[80:83]\n"
    "ds_read_b64_tr_b16 a[20:21], v128 offset:8704\n"
    "v_mfma_f32_16x16x32_bf16 v[80:83], a[40:43], a[96:99], v[80:83]\n"
    "ds_read_b64_tr_b16 a[22:23], v128 offset:12800\n"
    "v_mfma_f32_16x16x32_bf16 v[96:99], a[48:51], a[96:99], v[96:99]\n"
    "s_add_u32 m0, m0, 1024\n"
    "ds_read_b64_tr_b16 a[24:25], v129 offset:512\n"
    "v_mfma_f32_16x16x32_bf16 v[96:99], a[52:55], a[112:115], v[96:99]\n"
    "buffer_load_dwordx4 %16, s[40:43], s53 offen lds\n"
    "v_mfma_f32_16x16x32_bf16 v[100:103], a[52:55], a[116:119], v[100:103]\n"
    "ds_read_b64_tr_b16 a[26:27], v129 offset:4608\n"
    "v_mfma_f32_16x16x32_bf16 v[100:103], a[48:51], a[100:103], v[100:103]\n"
    "ds_read_b64_tr_b16 a[28:29], v129 offset:8704\n"
    "v_mfma_f32_16x16x32_bf16 v[104:107], a[48:51], a[104:107], v[104:107]\n"
    "ds_read_b64_tr_b16 a[30:31], v129 offset:12800\n"
    "v_mfma_f32_16x16x32_bf16 v[104:107], a[52:55], a[120:123], v[104:107]\n"
    "buffer_load_dwordx4 a[88:91], %12, s[36:39], s52 offen offset:2048 sc1\n"
    "v_mfma_f32_16x16x32_bf16 v[108:111], a[52:55], a[124:127], v[108:111]\n"
    "v_mfma_f32_16x16x32_bf16 v[108:111], a[48:51], a[108:111], v[108:111]\n"
    "v_mfma_f32_16x16x32_bf16 v[124:127], a[56:59], a[108:111], v[124:127]\n"
    "v_mfma_f32_16x16x32_bf16 v[124:127], a[60:63], a[124:127], v[124:127]\n"
    "buffer_load_dwordx4 a[92:95], %12, s[36:39], s52 offen offset:3072 sc1\n"
    "v_mfma_f32_16x16x32_bf16 v[120:123], a[60:63], a[120:123], v[120:123]\n"
    "s_add_u32 s52, s52, 4096\n"
    "v_mfma_f32_16x16x32_bf16 v[120:123], a[56:59], a[104:107], v[120:123]\n"
    "v_mfma_f32_16x16x32_bf16 v[116:119], a[56:59], a[100:103], v[116:119]\n"
    "v_mfma_f32_16x16x32_bf16 v[116:119], a[60:63], a[116:119], v[116:119]\n"
    "v_mfma_f32_16x16x32_bf16 v[112:115], a[60:63], a[112:115], v[112:115]\n"
    "v_mfma_f32_16x16x32_bf16 v[112:115], a[56:59], a[96:99], v[112:115]\n"
    "s_waitcnt vmcnt(12) lgkmcnt(0)\n"
    "v_mfma_f32_16x16x32_bf16 v[0:3], a[0:3], a[128:131], v[0:3]\n"
    "s_sub_u32 s59, s59, 1\n"
    "ds_read_b64_tr_b16 a[32:33], v128 offset:1024\n"
    "v_mfma_f32_16x16x32_bf16 v[0:3], a[4:7], a[144:147], v[0:3]\n"
    "s_cmp_gt_i32 s59, 0\n"
    "ds_read_b64_tr_b16 a[34:35], v128 offset:5120\n"
    "v_mfma_f32_16x16x32_bf16 v[4:7], a[4:7], a[148:151], v[4:7]\n"
    "s_cselect_b32 s38, s38, 0\n"
    "ds_read_b64_tr_b16 a[36:37], v128 offset:9216\n"
    "v_mfma_f32_16x16x32_bf16 v[4:7], a[0:3], a[132:135], v[4:7]\n"
    "ds_read_b64_tr_b16 a[38:39], v128 offset:13312\n"
    "v_mfma_f32_16x16x32_bf16 v[8:11], a[0:3], a[136:139], v[8:11]\n"
    "buffer_load_dwordx4 a[96:99], %12, s[36:39], s52 offen sc1\n"
    "v_mfma_f32_16x16x32_bf16 v[8:11], a[4:7], a[152:155], v[8:11]\n"
    "ds_read_b64_tr_b16 a[40:41], v129 offset:1024\n"
    "v_mfma_f32_16x16x32_bf16 v[12:15], a[4:7], a[156:159], v[12:15]\n"
    "s_add_u32 m0, m0, 1024\n"
    "ds_read_b64_tr_b16 a[42:43], v129 offset:5120\n"
    "v_mfma_f32_16x16x32_bf16 v[12:15], a[0:3], a[140:143], v[12:15]\n"
    "buffer_load_dwordx4 %17, s[40:43], s53 offen lds\n"
    "v_mfma_f32_16x16x32_bf16 v[28:31], a[8:11], a[140:143], v[28:31]\n"
    "ds_read_b64_tr_b16 a[44:45], v129 offset:9216\n"
    "v_mfma_f32_16x16x32_bf16 v[28:31], a[12:15], a[156:159], v[28:31]\n"
    "ds_read_b64_tr_b16 a[46:47], v129 offset:13312\n"
    "v_mfma_f32_16x16x32_bf16 v[24:27], a[12:15], a[152:155], v[24:27]\n"
    "buffer_load_dwordx4 a[100:103], %12, s[36:39], s52 offen offset:1024 sc1\n"
    "v_mfma_f32_16x16x32_bf16 v[24:27], a[8:11], a[136:139], v[24:27]\n"
    "ds_read_b64_tr_b16 a[48:49], v128 offset:1536\n"
    "v_mfma_f32_16x16x32_bf16 v[20:23], a[8:11], a[132:135], v[20:23]\n"
    "s_add_u32 m0, m0, 1024\n"
    "ds_read_b64_tr_b16 a[50:51], v128 offset:5632\n"
    "v_mfma_f32_16x16x32_bf16 v[20:23], a[12:15], a[148:151], v[20:23]\n"
    "buffer_load_dwordx4 %18, s[40:43], s53 offen lds\n"
    "v_mfma_f32_16x16x32_bf16 v[16:19], a[12:15], a[144:147], v[16:19]\n"
    "s_add_u32 s53, s53, 131072\n"
    "ds_read_b64_tr_b16 a[52:53], v128 offset:9728\n"
    "v_mfma_f32_16x16x32_bf16 v[16:19], a[8:11], a[128:131], v[16:19]\n"
    "s_add_u32 s57, s57, 16384\n"
    "ds_read_b64_tr_b16 a[54:55], v128 offset:13824\n"
    "v_mfma_f32_16x16x32_bf16 v[32:35], a[16:19], a[128:131], v[32:35]\n"
    "s_cmp_ge_u32 s57, 49152\n"
    "buffer_load_dwordx4 a[104:107], %12, s[36:39], s52 offen offset:2048 sc1\n"
    "v_mfma_f32_16x16x32_bf16 v[32:35], a[20:23], a[144:147], v[32:35]\n"
    "s_cselect_b32 s57, 0, s57\n"
    "ds_read_b64_tr_b16 a[56:57], v129 offset:1536\n"
    "v_mfma_f32_16x16x32_bf16 v[36:39], a[20:23], a[148:151], v[36:39]\n"
    "ds_read_b64_tr_b16 a[58:59], v129 offset:5632\n"
    "v_mfma_f32_16x16x32_bf16 v[36:39], a[16:19], a[132:135], v[36:39]\n"
    "buffer_load_dwordx4 a[108:111], %12, s[36:39], s52 offen offset:3072 sc1\n"
    "v_mfma_f32_16x16x32_bf16 v[40:43], a[16:19], a[136:139], v[40:43]\n"
    "s_add_u32 s52, s52, 4096\n"
    "ds_read_b64_tr_b16 a[60:61], v129 offset:9728\n"
    "v_mfma_f32_16x16x32_bf16 v[40:43], a[20:23], a[152:155], v[40:43]\n"
    "ds_read_b64_tr_b16 a[62:63], v129 offset:13824\n"
    "v_mfma_f32_16x16x32_bf16 v[44:47], a[20:23], a[156:159], v[44:47]\n"
    "s_add_u32 s58, s58, 16384\n"
    "v_mfma_f32_16x16x32_bf16 v[44:47], a[16:19], a[140:143], v[44:47]\n"
    "s_cmp_ge_u32 s58, 49152\n"
    "v_mfma_f32_16x16x32_bf16 v[60:63], a[24:27], a[140:143], v[60:63]\n"
    "s_cselect_b32 s58, 0, s58\n"
    "v_mfma_f32_16x16x32_bf16 v[60:63], a[28:31], a[156:159], v[60:63]\n"
    "v_add_u32 v128, s58, %13\n"
    "v_add_u32 v129, s58, %14\n"
    "v_mfma_f32_16x16x32_bf16 v[56:59], a[28:31], a[152:155], v[56:59]\n"
    "v_mfma_f32_16x16x32_bf16 v[56:59], a[24:27], a[136:139], v[56:59]\n"
    "v_mfma_f32_16x16x32_bf16 v[52:55], a[24:27], a[132:135], v[52:55]\n"
    "v_mfma_f32_16x16x32_bf16 v[52:55], a[28:31], a[148:151], v[52:55]\n"
    "v_mfma_f32_16x16x32_bf16 v[48:51], a[28:31], a[144:147], v[48:51]\n"
    "v_mfma_f32_16x16x32_bf16 v[48:51], a[24:27], a[128:131], v[48:51]\n"
    "s_waitcnt vmcnt(14) lgkmcnt(0)\n"
    "v_mfma_f32_16x16x32_bf16 v[64:67], a[32:35], a[128:131], v[64:67]\n"
    "s_barrier\n"
    "v_mfma_f32_16x16x32_bf16 v[64:67], a[36:39], a[144:147], v[64:67]\n"
    "s_sub_u32 s60, s60, 1\n"
    "ds_read_b64_tr_b16 a[0:1], v128 offset:0\n"
    "v_mfma_f32_16x16x32_bf16 v[68:71], a[36:39], a[148:151], v[68:71]\n"
    "s_cmp_gt_i32 s60, 0\n"
    "ds_read_b64_tr_b16 a[2:3], v128 offset:4096\n"
    "v_mfma_f32_16x16x32_bf16 v[68:71], a[32:35], a[132:135], v[68:71]\n"
    "s_cselect_b32 s42, s42, 0\n"
    "ds_read_b64_tr_b16 a[4:5], v128 offset:8192\n"
    "v_mfma_f32_16x16x32_bf16 v[72:75], a[32:35], a[136:139], v[72:75]\n"
    "ds_read_b64_tr_b16 a[6:7], v128 offset:12288\n"
    "v_mfma_f32_16x16x32_bf16 v[72:75], a[36:39], a[152:155], v[72:75]\n"
    "buffer_load_dwordx4 a[112:115], %12, s[36:39], s52 offen sc1\n"
    "v_mfma_f32_16x16x32_bf16 v[76:79], a[36:39], a[156:159], v[76:79]\n"
    "ds_read_b64_tr_b16 a[8:9], v129 offset:0\n"
    "v_mfma_f32_16x16x32_bf16 v[76:79], a[32:35], a[140:143], v[76:79]\n"
    "ds_read_b64_tr_b16 a[10:11], v129 offset:4096\n"
    "v_mfma_f32_16x16x32_bf16 v[92:95], a[40:43], a[140:143], v[92:95]\n"
    "s_add_u32 m0, s57, s56\n"
    "ds_read_b64_tr_b16 a[12:13], v129 offset:8192\n"
    "v_mfma_f32_16x16x32_bf16 v[92:95], a[44:47], a[156:159], v[92:95]\n"
    "buffer_load_dwordx4 %15, s[40:43], s53 offen lds\n"
    "v_mfma_f32_16x16x32_bf16 v[88:91], a[44:47], a[152:155], v[88:91]\n"
    "ds_read_b64_tr_b16 a[14:15], v129 offset:12288\n"
    "v_mfma_f32_16x16x32_bf16 v[88:91], a[40:43], a[136:139], v[88:91]\n"
    "ds_read_b64_tr_b16 a[16:17], v128 offset:512\n"
    "v_mfma_f32_16x16x32_bf16 v[84:87], a[40:43], a[132:135], v[84:87]\n"
    "ds_read_b64_tr_b16 a[18:19], v128 offset:4608\n"
    "v_mfma_f32_16x16x32_bf16 v[84:87], a[44:47], a[148:151], v[84:87]\n"
    "buffer_load_dwordx4 a[116:119], %12, s[36:39], s52 offen offset:1024 sc1\n"
    "v_mfma_f32_16x16x32_bf16 v[80:83], a[44:47], a[144:147], v[80:83]\n"
    "ds_read_b64_tr_b16 a[20:21], v128 offset:8704\n"
    "v_mfma_f32_16x16x32_bf16 v[80:83], a[40:43], a[128:131], v[80:83]\n"
    "ds_read_b64_tr_b16 a[22:23], v128 offset:12800\n"
    "v_mfma_f32_16x16x32_bf16 v[96:99], a[48:51], a[128:131], v[96:99]\n"
    "s_add_u32 m0, m0, 1024\n"
    "ds_read_b64_tr_b16 a[24:25], v129 offset:512\n"
    "v_mfma_f32_16x16x32_bf16 v[96:99], a[52:55], a[144:147], v[96:99]\n"
    "buffer_load_dwordx4 %16, s[40:43], s53 offen lds\n"
    "v_mfma_f32_16x16x32_bf16 v[100:103], a[52:55], a[148:151], v[100:103]\n"
    "ds_read_b64_tr_b16 a[26:27], v129 offset:4608\n"
    "v_mfma_f32_16x16x32_bf16 v[100:103], a[48:51], a[132:135], v[100:103]\n"
    "ds_read_b64_tr_b16 a[28:29], v129 offset:8704\n"
    "v_mfma_f32_16x16x32_bf16 v[104:107], a[48:51], a[136:139], v[104:107]\n"
    "ds_read_b64_tr_b16 a[30:31], v129 offset:12800\n"
    "v_mfma_f32_16x16x32_bf16 v[104:107], a[52:55], a[152:155], v[104:107]\n"
    "buffer_load_dwordx4 a[120:123], %12, s[36:39], s52 offen offset:2048 sc1\n"
    "v_mfma_f32_16x16x32_bf16 v[108:111], a[52:55], a[156:159], v[108:111]\n"
    "v_mfma_f32_16x16x32_bf16 v[108:111], a[48:51], a[140:143], v[108:111]\n"
    "v_mfma_f32_16x16x32_bf16 v[124:127], a[56:59], a[140:143], v[124:127]\n"
    "v_mfma_f32_16x16x32_bf16 v[124:127], a[60:63], a[156:159], v[124:127]\n"
    "buffer_load_dwordx4 a[124:127], %12, s[36:39], s52 offen offset:3072 sc1\n"
    "v_mfma_f32_16x16x32_bf16 v[120:123], a[60:63], a[152:155], v[120:123]\n"
    "s_add_u32 s52, s52, 4096\n"
    "v_mfma_f32_16x16x32_bf16 v[120:123], a[56:59], a[136:139], v[120:123]\n"
    "v_mfma_f32_16x16x32_bf16 v[116:119], a[56:59], a[132:135], v[116:119]\n"
    "v_mfma_f32_16x16x32_bf16 v[116:119], a[60:63], a[148:151], v[116:119]\n"
    "v_mfma_f32_16x16x32_bf16 v[112:115], a[60:63], a[144:147], v[112:115]\n"
    "v_mfma_f32_16x16x32_bf16 v[112:115], a[56:59], a[128:131], v[112:115]\n"
    "s_waitcnt vmcnt(12) lgkmcnt(0)\n"
    "s_sub_u32 s55, s55, 1\n"
    "s_cmp_lg_u32 s55, 0\n"
    "s_cbranch_scc1 L_k3_loop\n"
    "v_mfma_f32_16x16x32_bf16 v[0:3], a[0:3], a[64:67], v[0:3]\n"
    "s_sub_u32 s59, s59, 1\n"
    "ds_read_b64_tr_b16 a[32:33], v128 offset:1024\n"
    "v_mfma_f32_16x16x32_bf16 v[0:3], a[4:7], a[80:83], v[0:3]\n"
    "s_cmp_gt_i32 s59, 0\n"
    "ds_read_b64_tr_b16 a[34:35], v128 offset:5120\n"
    "v_mfma_f32_16x16x32_bf16 v[4:7], a[4:7], a[84:87], v[4:7]\n"
    "s_cselect_b32 s38, s38, 0\n"
    "ds_read_b64_tr_b16 a[36:37], v128 offset:9216\n"
    "v_mfma_f32_16x16x32_bf16 v[4:7], a[0:3], a[68:71], v[4:7]\n"
    "ds_read_b64_tr_b16 a[38:39], v128 offset:13312\n"
    "v_mfma_f32_16x16x32_bf16 v[8:11], a[0:3], a[72:75], v[8:11]\n"
    "buffer_load_dwordx4 a[128:131], %12, s[36:39], s52 offen sc1\n"
    "v_mfma_f32_16x16x32_bf16 v[8:11], a[4:7], a[88:91], v[8:11]\n"
    "ds_read_b64_tr_b16 a[40:41], v129 offset:1024\n"
    "v_mfma_f32_16x16x32_bf16 v[12:15], a[4:7], a[92:95], v[12:15]\n"
    "s_add_u32 m0, m0, 1024\n"
    "ds_read_b64_tr_b16 a[42:43], v129 offset:5120\n"
    "v_mfma_f32_16x16x32_bf16 v[12:15], a[0:3], a[76:79], v[12:15]\n"
    "buffer_load_dwordx4 %17, s[40:43], s53 offen lds\n"
    "v_mfma_f32_16x16x32_bf16 v[28:31], a[8:11], a[76:79], v[28:31]\n"
    "ds_read_b64_tr_b16 a[44:45], v129 offset:9216\n"
    "v_mfma_f32_16x16x32_bf16 v[28:31], a[12:15], a[92:95], v[28:31]\n"
    "ds_read_b64_tr_b16 a[46:47], v129 offset:13312\n"
    "v_mfma_f32_16x16x32_bf16 v[24:27], a[12:15], a[88:91], v[24:27]\n"
    "buffer_load_dwordx4 a[132:135], %12, s[36:39], s52 offen offset:1024 sc1\n"
    "v_mfma_f32_16x16x32_bf16 v[24:27], a[8:11], a[72:75], v[24:27]\n"
    "ds_read_b64_tr_b16 a[48:49], v128 offset:1536\n"
    "v_mfma_f32_16x16x32_bf16 v[20:23], a[8:11], a[68:71], v[20:23]\n"
    "s_add_u32 m0, m0, 1024\n"
    "ds_read_b64_tr_b16 a[50:51], v128 offset:5632\n"
    "v_mfma_f32_16x16x32_bf16 v[20:23], a[12:15], a[84:87], v[20:23]\n"
    "buffer_load_dwordx4 %18, s[40:43], s53 offen lds\n"
    "v_mfma_f32_16x16x32_bf16 v[16:19], a[12:15], a[80:83], v[16:19]\n"
    "s_add_u32 s53, s53, 131072\n"
    "ds_read_b64_tr_b16 a[52:53], v128 offset:9728\n"
    "v_mfma_f32_16x16x32_bf16 v[16:19], a[8:11], a[64:67], v[16:19]\n"
    "s_add_u32 s57, s57, 16384\n"
    "ds_read_b64_tr_b16 a[54:55], v128 offset:13824\n"
    "v_mfma_f32_16x16x32_bf16 v[32:35], a[16:19], a[64:67], v[32:35]\n"
    "s_cmp_ge_u32 s57, 49152\n"
    "buffer_load_dwordx4 a[136:139], %12, s[36:39], s52 offen offset:2048 sc1\n"
    "v_mfma_f32_16x16x32_bf16 v[32:35], a[20:23], a[80:83], v[32:35]\n"
    "s_cselect_b32 s57, 0, s57\n"
    "ds_read_b64_tr_b16 a[56:57], v129 offset:1536\n"
    "v_mfma_f32_16x16x32_bf16 v[36:39], a[20:23], a[84:87], v[36:39]\n"
    "ds_read_b64_tr_b16 a[58:59], v129 offset:5632\n"
    "v_mfma_f32_16x16x32_bf16 v[36:39], a[16:19], a[68:71], v[36:39]\n"
    "buffer_load_dwordx4 a[140:143], %12, s[36:39], s52 offen offset:3072 sc1\n"
    "v_mfma_f32_16x16x32_bf16 v[40:43], a[16:19], a[72:75], v[40:43]\n"
    "s_add_u32 s52, s52, 4096\n"
    "ds_read_b64_tr_b16 a[60:61], v129 offset:9728\n"
    "v_mfma_f32_16x16x32_bf16 v[40:43], a[20:23], a[88:91], v[40:43]\n"
    "ds_read_b64_tr_b16 a[62:63], v129 offset:13824\n"
    "v_mfma_f32_16x16x32_bf16 v[44:47], a[20:23], a[92:95], v[44:47]\n"
    "s_add_u32 s58, s58, 16384\n"
    "v_mfma_f32_16x16x32_bf16 v[44:47], a[16:19], a[76:79], v[44:47]\n"
    "s_cmp_ge_u32 s58, 49152\n"
    "v_mfma_f32_16x16x32_bf16 v[60:63], a[24:27], a[76:79], v[60:63]\n"
    "s_cselect_b32 s58, 0, s58\n"
    "v_mfma_f32_16x16x32_bf16 v[60:63], a[28:31], a[92:95], v[60:63]\n"
    "v_add_u32 v128, s58, %13\n"
    "v_add_u32 v129, s58, %14\n"
    "v_mfma_f32_16x16x32_bf16 v[56:59], a[28:31], a[88:91], v[56:59]\n"
    "v_mfma_f32_16x16x32_bf16 v[56:59], a[24:27], a[72:75], v[56:59]\n"
    "v_mfma_f32_16x16x32_bf16 v[52:55], a[24:27], a[68:71], v[52:55]\n"
    "v_mfma_f32_16x16x32_bf16 v[52:55], a[28:31], a[84:87], v[52:55]\n"
    "v_mfma_f32_16x16x32_bf16 v[48:51], a[28:31], a[80:83], v[48:51]\n"
    "v_mfma_f32_16x16x32_bf16 v[48:51], a[24:27], a[64:67], v[48:51]\n"
    "s_waitcnt vmcnt(14) lgkmcnt(0)\n"
    "v_mfma_f32_16x16x32_bf16 v[64:67], a[32:35], a[64:67], v[64:67]\n"
    "s_barrier\n"
    "v_mfma_f32_16x16x32_bf16 v[64:67], a[36:39], a[80:83], v[64:67]\n"
    "s_sub_u32 s60, s60, 1\n"
    "ds_read_b64_tr_b16 a[0:1], v128 offset:0\n"
    "v_mfma_f32_16x16x32_bf16 v[68:71], a[36:39], a[84:87], v[68:71]\n"
    "s_cmp_gt_i32 s60, 0\n"
    "ds_read_b64_tr_b16 a[2:3], v128 offset:4096\n"
    "v_mfma_f32_16x16x32_bf16 v[68:71], a[32:35], a[68:71], v[68:71]\n"
    "s_cselect_b32 s42, s42, 0\n"
    "ds_read_b64_tr_b16 a[4:5], v128 offset:8192\n"
    "v_mfma_f32_16x16x32_bf16 v[72:75], a[32:35], a[72:75], v[72:75]\n"
    "ds_read_b64_tr_b16 a[6:7], v128 offset:12288\n"
    "v_mfma_f32_16x16x32_bf16 v[72:75], a[36:39], a[88:91], v[72:75]\n"
    "buffer_load_dwordx4 a[144:147], %12, s[36:39], s52 offen sc1\n"
    "v_mfma_f32_16x16x32_bf16 v[76:79], a[36:39], a[92:95], v[76:79]\n"
    "ds_read_b64_tr_b16 a[8:9], v129 offset:0\n"
    "v_mfma_f32_16x16x32_bf16 v[76:79], a[32:35], a[76:79], v[76:79]\n"
    "ds_read_b64_tr_b16 a[10:11], v129 offset:4096\n"
    "v_mfma_f32_16x16x32_bf16 v[92:95], a[40:43], a[76:79], v[92:95]\n"
    "s_add_u32 m0, s57, s56\n"
    "ds_read_b64_tr_b16 a[12:13], v129 offset:8192\n"
    "v_mfma_f32_16x16x32_bf16 v[92:95], a[44:47], a[92:95], v[92:95]\n"
    "buffer_load_dwordx4 %15, s[40:43], s53 offen lds\n"
    "v_mfma_f32_16x16x32_bf16 v[88:91], a[44:47], a[88:91], v[88:91]\n"
    "ds_read_b64_tr_b16 a[14:15], v129 offset:12288\n"
    "v_mfma_f32_16x16x32_bf16 v[88:91], a[40:43], a[72:75], v[88:91]\n"
    "ds_read_b64_tr_b16 a[16:17], v128 offset:512\n"
    "v_mfma_f32_16x16x32_bf16 v[84:87], a[40:43], a[68:71], v[84:87]\n"
    "ds_read_b64_tr_b16 a[18:19], v128 offset:4608\n"
    "v_mfma_f32_16x16x32_bf16 v[84:87], a[44:47], a[84:87], v[84:87]\n"
    "buffer_load_dwordx4 a[148:151], %12, s[36:39], s52 offen offset:1024 sc1\n"
    "v_mfma_f32_16x16x32_bf16 v[80:83], a[44:47], a[80:83], v[80:83]\n"
    "ds_read_b64_tr_b16 a[20:21], v128 offset:8704\n"
    "v_mfma_f32_16x16x32_bf16 v[80:83], a[40:43], a[64:67], v[80:83]\n"
    "ds_read_b64_tr_b16 a[22:23], v128 offset:12800\n"
    "v_mfma_f32_16x16x32_bf16 v[96:99], a[48:51], a[64:67], v[96:99]\n"
    "s_add_u32 m0, m0, 1024\n"
    "ds_read_b64_tr_b16 a[24:25], v129 offset:512\n"
    "v_mfma_f32_16x16x32_bf16 v[96:99], a[52:55], a[80:83], v[96:99]\n"
    "buffer_load_dwordx4 %16, s[40:43], s53 offen lds\n"
    "v_mfma_f32_16x16x32_bf16 v[100:103], a[52:55], a[84:87], v[100:103]\n"
    "ds_read_b64_tr_b16 a[26:27], v129 offset:4608\n"
    "v_mfma_f32_16x16x32_bf16 v[100:103], a[48:51], a[68:71], v[100:103]\n"
    "ds_read_b64_tr_b16 a[28:29], v129 offset:8704\n"
    "v_mfma_f32_16x16x32_bf16 v[104:107], a[48:51], a[72:75], v[104:107]\n"
    "ds_read_b64_tr_b16 a[30:31], v129 offset:12800\n"
    "v_mfma_f32_16x16x32_bf16 v[104:107], a[52:55], a[88:91], v[104:107]\n"
    "buffer_load_dwordx4 a[152:155], %12, s[36:39], s52 offen offset:2048 sc1\n"
    "v_mfma_f32_16x16x32_bf16 v[108:111], a[52:55], a[92:95], v[108:111]\n"
    "v_mfma_f32_16x16x32_bf16 v[108:111], a[48:51], a[76:79], v[108:111]\n"
    "v_mfma_f32_16x16x32_bf16 v[124:127], a[56:59], a[76:79], v[124:127]\n"
    "v_mfma_f32_16x16x32_bf16 v[124:127], a[60:63], a[92:95], v[124:127]\n"
    "buffer_load_dwordx4 a[156:159], %12, s[36:39], s52 offen offset:3072 sc1\n"
    "v_mfma_f32_16x16x32_bf16 v[120:123], a[60:63], a[88:91], v[120:123]\n"
    "s_add_u32 s52, s52, 4096\n"
    "v_mfma_f32_16x16x32_bf16 v[120:123], a[56:59], a[72:75], v[120:123]\n"
    "v_mfma_f32_16x16x32_bf16 v[116:119], a[56:59], a[68:71], v[116:119]\n"
    "v_mfma_f32_16x16x32_bf16 v[116:119], a[60:63], a[84:87], v[116:119]\n"
    "v_mfma_f32_16x16x32_bf16 v[112:115], a[60:63], a[80:83], v[112:115]\n"
    "v_mfma_f32_16x16x32_bf16 v[112:115], a[56:59], a[64:67], v[112:115]\n"
    "s_waitcnt vmcnt(12) lgkmcnt(0)\n"
    "s_waitcnt vmcnt(0)\n"
    "s_nop 7\n"
    "s_nop 7\n"
      : "=&{v[0:31]}"(a0), "=&{v[32:63]}"(a1), "=&{v[64:95]}"(a2), "=&{v[96:127]}"(a3), "=&{v[132:147]}"(xr)
      : "s"(ws+WS_O), "s"(M_*8192u), "s"(Wo), "s"(4096u*2048u), "s"(offX), "s"(offW), "s"(wdst),
        "v"(vXl), "v"(vWr0), "v"(vWr1), "v"(vWd[0]), "v"(vWd[1]), "v"(vWd[2]), "v"(vWd[3]), "s"(ws+WS_XF), "v"(fo), "s"(cofs), "s"(CNT(rt,C_O+sp)), "s"(needO)
      : "memory", "vcc", "scc", "m0", "s36", "s37", "s38", "s39", "s40", "s41", "s42", "s43", "s44", "s45", "s46", "s47", "s48", "s49", "s50", "s51", "s52", "s53", "s54", "s55", "s56", "s57", "s58", "s59", "s60", "s61", "s62", "s63", "s64", "s65", "s66", "s67", "s68", "s69", "s70", "s71", "s72", "s73", "s74", "s75", "s76", "s77", "s78", "s79", "s80", "s81", "s82", "s83", "s84", "s85", "s86", "s87", "s88", "s89", "s90", "s91", "s92", "s93", "s94", "s95", "s96", "s97", "s98", "s99", "v128", "v129", "v130", "v131", "a0", "a1", "a2", "a3", "a4", "a5", "a6", "a7", "a8", "a9", "a10", "a11", "a12", "a13", "a14", "a15", "a16", "a17", "a18", "a19", "a20", "a21", "a22", "a23", "a24", "a25", "a26", "a27", "a28", "a29", "a30", "a31", "a32", "a33", "a34", "a35", "a36", "a37", "a38", "a39", "a40", "a41", "a42", "a43", "a44", "a45", "a46", "a47", "a48", "a49", "a50", "a51", "a52", "a53", "a54", "a55", "a56", "a57", "a58", "a59", "a60", "a61", "a62", "a63", "a64", "a65", "a66", "a67", "a68", "a69", "a70", "a71", "a72", "a73", "a74", "a75", "a76", "a77", "a78", "a79", "a80", "a81", "a82", "a83", "a84", "a85", "a86", "a87", "a88", "a89", "a90", "a91", "a92", "a93", "a94", "a95", "a96", "a97", "a98", "a99", "a100", "a101", "a102", "a103", "a104", "a105", "a106", "a107", "a108", "a109", "a110", "a111", "a112", "a113", "a114", "a115", "a116", "a117", "a118", "a119", "a120", "a121", "a122", "a123", "a124", "a125", "a126", "a127", "a128", "a129", "a130", "a131", "a132", "a133", "a134", "a135", "a136", "a137", "a138", "a139", "a140", "a141", "a142", "a143", "a144", "a145", "a146", "a147", "a148", "a149", "a150", "a151", "a152", "a153", "a154", "a155", "a156", "a157", "a158", "a159");
   { PHASE() LANE() X_FRAG() (void)q;(void)p;(void)vXl;(void)i16; const unsigned T=rt*4+w;

    if(w==0) PRE_G112()
    X_PUBLISH(C_X3)

    char* tbl=smem_+65536;
    { const __amdgpu_buffer_rsrc_t rM=mkrsrc(ws+WS_MODF,65536u); u32x4 gm[4][2][2]; unsigned spins=0;
      for(;;){ unsigned bad=0;
        #pragma unroll
        for(int v=0;v<4;v++)
          #pragma unroll
          for(int k=0;k<4;k++) gm[v][k>>1][k&1]=ld16_sc1(rM,((k>>1)*4096+v*1024+tid_*4)*8+(k&1)*16);
        #pragma unroll
        for(int v=0;v<4;v++)
          #pragma unroll
          for(int k=0;k<4;k++){ const u32x4 t=gm[v][k>>1][k&1]; bad|=(t[0]^tagLo)|(t[2]^tagLo)|(((t[1]^tagHi)|(t[3]^tagHi))&0xffu); }
        if(__builtin_amdgcn_ballot_w64(bad!=0)==0ull || ++spins>(1u<<16)) break;
        __builtin_amdgcn_s_sleep(1); }
      #pragma unroll
      for(int v=0;v<4;v++){ float t[4];
        #pragma unroll
        for(int e=0;e<4;e++){ t[e]=rb(__uint_as_float(gm[v][0][e>>1][1+2*(e&1)]&0xffffff00u)+__uint_as_float(gm[v][1][e>>1][1+2*(e&1)]&0xffffff00u)); if(v==2) t[e]=1.f+t[e]; }
        *(u32x2*)(tbl+v*2048+tid_*8)=(u32x2){cvtpk(t[0],t[1]),cvtpk(t[2],t[3])}; } }
    X_GATHER(C_X3)

    u32x4 mt_[3][4];
    #pragma unroll
    for(int v=0;v<3;v++)
      #pragma unroll
      for(int j=0;j<4;j++) mt_[v][j]=*(const u32x4*)(tbl+v*2048+(8*w+2*j+k2)*64+g*16);
    const __amdgpu_buffer_rsrc_t rX1=mkrsrc(ws+WS_X1,M_*2048u);
    float v[4][8]; f32x2 s2[4]={{0,0},{0,0},{0,0},{0,0}};
    u32x4 x1v[4];
    #pragma unroll
    for(int j=0;j<4;j++){ u32x4 o1;
      #pragma unroll
      for(int e=0;e<4;e++){ o1[e]=cvtpk(bflo(xr[j*4+e])+bflo(mt_[0][j][e])*y[j][2*e], bfhi(xr[j*4+e])+bfhi(mt_[0][j][e])*y[j][2*e+1]); v[j][2*e]=bflo(o1[e]); v[j][2*e+1]=bfhi(o1[e]); s2[e]+=(f32x2){v[j][2*e],v[j][2*e+1]}; }
      x1v[j]=o1; }
    const f32x2 st_=(s2[0]+s2[1])+(s2[2]+s2[3]);
    float sum=sumg(st_[0]+st_[1]); sum+=dppf<0x128>(sum);
    const float mw_=sum*(1.f/256.f); f32x2 q2[4]={{0,0},{0,0},{0,0},{0,0}};
    #pragma unroll
    for(int j=0;j<4;j++)
      #pragma unroll
      for(int e=0;e<4;e++){ const f32x2 d=(f32x2){v[j][2*e],v[j][2*e+1]}-mw_; q2[e]+=d*d; }
    const f32x2 qt=(q2[0]+q2[1])+(q2[2]+q2[3]);
    float qq=sumg(qt[0]+qt[1]); qq+=dppf<0x128>(qq);
    f32x2* sx=(f32x2*)smem_;
    if(lane<8) sx[w*8+lane]=(f32x2){mw_,qq};
    BAR_();
    float mean, rstd;
    { const f32x2 u0=sx[r8], u1=sx[8+r8], u2=sx[16+r8], u3=sx[24+r8];
      mean=((u0[0]+u1[0])+(u2[0]+u3[0]))*0.25f;
      const float d0=u0[0]-mean, d1=u1[0]-mean, d2=u2[0]-mean, d3=u3[0]-mean;
      rstd=rsqrtf((((u0[1]+u1[1])+(u2[1]+u3[1]))+256.f*((d0*d0+d1*d1)+(d2*d2+d3*d3)))*(1.f/1024.f)+1e-5f); }
    const __amdgpu_buffer_rsrc_t rH2=mkrsrc(ws+WS_H2,M_*2048u);
    #pragma unroll
    for(int j=0;j<4;j++){ unsigned h[4];
      #pragma unroll
      for(int e=0;e<4;e++) h[e]=cvtpk((v[j][2*e]-mean)*rstd*bflo(mt_[2][j][e])+bflo(mt_[1][j][e]), (v[j][2*e+1]-mean)*rstd*bfhi(mt_[2][j][e])+bfhi(mt_[1][j][e]));
      st16_sc1(rH2,fo+j*8192u,(u32x4){h[0],h[1],h[2],h[3]}); }

    DRAIN_BAR(); if(tid_<4) __hip_atomic_fetch_add((gu32*)CNT(rt,C_H2+tid_), 1u, __ATOMIC_RELAXED, __HIP_MEMORY_SCOPE_AGENT);

    #pragma unroll
    for(int j=0;j<4;j++) st16_sc1(rX1,fo+j*8192u,x1v[j]);
    if(w!=0) PRE_W128(Wd,3584u*2048u,sp5*896)
   }
  }
  { PHASE() LANE() G112_DMA()
    const unsigned T=rt4*4+w;
    const unsigned vWr0=(g>>1)*512+(4*(g&1)+q)*64+((0^(g&1))*32)+p*8, vWr1=(g>>1)*512+(4*(g&1)+q)*64+((1^(g&1))*32)+p*8, vWr2=(4*g+q)*32+p*8;
    const unsigned paircol=112*ct4+(odd4?16:0), lonecol=112*ct4+(odd4?0:96);
    const unsigned offX=rfl(T*32*4096), offW=rfl(16*w*7168+paircol*2), offL=rfl(32*(w&1)*7168+lonecol*2), wdst=rfl(w*3072), ldst=rfl(24576+w*1024);
    const u16* Wl=(w>>1)?Wu:Wg;

    f32x32 a0,a1,a2,a3,a4,a5,a6; const unsigned cofs=rfl((loc*4+w)*1024);
    asm volatile(
    "s_mov_b64 s[36:37], %7\n"
    "s_mov_b32 s38, %8\n"
    "s_mov_b32 s39, 0x20000\n"
    "s_mov_b64 s[40:41], %9\n"
    "s_mov_b32 s42, %12\n"
    "s_mov_b32 s43, 0x20000\n"
    "s_mov_b64 s[44:45], %10\n"
    "s_mov_b32 s46, %12\n"
    "s_mov_b32 s47, 0x20000\n"
    "s_mov_b64 s[48:49], %11\n"
    "s_mov_b32 s50, %12\n"
    "s_mov_b32 s51, 0x20000\n"
    "s_mov_b32 s52, %13\n"
    "s_mov_b32 s53, %14\n"
    "s_mov_b32 s56, %16\n"
    "s_mov_b32 s54, %15\n"
    "s_mov_b32 s61, %17\n"
    "s_mov_b32 s57, 73728\n"
    "s_mov_b32 s58, 73728\n"
    "v_add_u32 v224, 73728, %19\n"
    "v_add_u32 v225, 73728, %20\n"
    "v_add_u32 v226, 73728, %21\n"
    "v_mov_b32 v0, 0\n"
    "v_mov_b32 v1, 0\n"
    "v_mov_b32 v2, 0\n"
    "v_mov_b32 v3, 0\n"
    "v_mov_b32 v4, 0\n"
    "v_mov_b32 v5, 0\n"
    "v_mov_b32 v6, 0\n"
    "v_mov_b32 v7, 0\n"
    "v_mov_b32 v8, 0\n"
    "v_mov_b32 v9, 0\n"
    "v_mov_b32 v10, 0\n"
    "v_mov_b32 v11, 0\n"
    "v_mov_b32 v12, 0\n"
    "v_mov_b32 v13, 0\n"
    "v_mov_b32 v14, 0\n"
    "v_mov_b32 v15, 0\n"
    "v_mov_b32 v16, 0\n"
    "v_mov_b32 v17, 0\n"
    "v_mov_b32 v18, 0\n"
    "v_mov_b32 v19, 0\n"
    "v_mov_b32 v20, 0\n"
    "v_mov_b32 v21, 0\n"
    "v_mov_b32 v22, 0\n"
    "v_mov_b32 v23, 0\n"
    "v_mov_b32 v24, 0\n"
    "v_mov_b32 v25, 0\n"
    "v_mov_b32 v26, 0\n"
    "v_mov_b32 v27, 0\n"
    "v_mov_b32 v28, 0\n"
    "v_mov_b32 v29, 0\n"
    "v_mov_b32 v30, 0\n"
    "v_mov_b32 v31, 0\n"
    "v_mov_b32 v32, 0\n"
    "v_mov_b32 v33, 0\n"
    "v_mov_b32 v34, 0\n"
    "v_mov_b32 v35, 0\n"
    "v_mov_b32 v36, 0\n"
    "v_mov_b32 v37, 0\n"
    "v_mov_b32 v38, 0\n"
    "v_mov_b32 v39, 0\n"
    "v_mov_b32 v40, 0\n"
    "v_mov_b32 v41, 0\n"
    "v_mov_b32 v42, 0\n"
    "v_mov_b32 v43, 0\n"
    "v_mov_b32 v44, 0\n"
    "v_mov_b32 v45, 0\n"
    "v_mov_b32 v46, 0\n"
    "v_mov_b32 v47, 0\n"
    "v_mov_b32 v48, 0\n"
    "v_mov_b32 v49, 0\n"
    "v_mov_b32 v50, 0\n"
    "v_mov_b32 v51, 0\n"
    "v_mov_b32 v52, 0\n"
    "v_mov_b32 v53, 0\n"
    "v_mov_b32 v54, 0\n"
    "v_mov_b32 v55, 0\n"
    "v_mov_b32 v56, 0\n"
    "v_mov_b32 v57, 0\n"
    "v_mov_b32 v58, 0\n"
    "v_mov_b32 v59, 0\n"
    "v_mov_b32 v60, 0\n"
    "v_mov_b32 v61, 0\n"
    "v_mov_b32 v62, 0\n"
    "v_mov_b32 v63, 0\n"
    "v_mov_b32 v64, 0\n"
    "v_mov_b32 v65, 0\n"
    "v_mov_b32 v66, 0\n"
    "v_mov_b32 v67, 0\n"
    "v_mov_b32 v68, 0\n"
    "v_mov_b32 v69, 0\n"
    "v_mov_b32 v70, 0\n"
    "v_mov_b32 v71, 0\n"
    "v_mov_b32 v72, 0\n"
    "v_mov_b32 v73, 0\n"
    "v_mov_b32 v74, 0\n"
    "v_mov_b32 v75, 0\n"
    "v_mov_b32 v76, 0\n"
    "v_mov_b32 v77, 0\n"
    "v_mov_b32 v78, 0\n"
    "v_mov_b32 v79, 0\n"
    "v_mov_b32 v80, 0\n"
    "v_mov_b32 v81, 0\n"
    "v_mov_b32 v82, 0\n"
    "v_mov_b32 v83, 0\n"
    "v_mov_b32 v84, 0\n"
    "v_mov_b32 v85, 0\n"
    "v_mov_b32 v86, 0\n"
    "v_mov_b32 v87, 0\n"
    "v_mov_b32 v88, 0\n"
    "v_mov_b32 v89, 0\n"
    "v_mov_b32 v90, 0\n"
    "v_mov_b32 v91, 0\n"
    "v_mov_b32 v92, 0\n"
    "v_mov_b32 v93, 0\n"
    "v_mov_b32 v94, 0\n"
    "v_mov_b32 v95, 0\n"
    "v_mov_b32 v96, 0\n"
    "v_mov_b32 v97, 0\n"
    "v_mov_b32 v98, 0\n"
    "v_mov_b32 v99, 0\n"
    "v_mov_b32 v100, 0\n"
    "v_mov_b32 v101, 0\n"
    "v_mov_b32 v102, 0\n"
    "v_mov_b32 v103, 0\n"
    "v_mov_b32 v104, 0\n"
    "v_mov_b32 v105, 0\n"
    "v_mov_b32 v106, 0\n"
    "v_mov_b32 v107, 0\n"
    "v_mov_b32 v108, 0\n"
    "v_mov_b32 v109, 0\n"
    "v_mov_b32 v110, 0\n"
    "v_mov_b32 v111, 0\n"
    "v_mov_b32 v112, 0\n"
    "v_mov_b32 v113, 0\n"
    "v_mov_b32 v114, 0\n"
    "v_mov_b32 v115, 0\n"
    "v_mov_b32 v116, 0\n"
    "v_mov_b32 v117, 0\n"
    "v_mov_b32 v118, 0\n"
    "v_mov_b32 v119, 0\n"
    "v_mov_b32 v120, 0\n"
    "v_mov_b32 v121, 0\n"
    "v_mov_b32 v122, 0\n"
    "v_mov_b32 v123, 0\n"
    "v_mov_b32 v124, 0\n"
    "v_mov_b32 v125, 0\n"
    "v_mov_b32 v126, 0\n"
    "v_mov_b32 v127, 0\n"
    "v_mov_b32 v128, 0\n"
    "v_mov_b32 v129, 0\n"
    "v_mov_b32 v130, 0\n"
    "v_mov_b32 v131, 0\n"
    "v_mov_b32 v132, 0\n"
    "v_mov_b32 v133, 0\n"
    "v_mov_b32 v134, 0\n"
    "v_mov_b32 v135, 0\n"
    "v_mov_b32 v136, 0\n"
    "v_mov_b32 v137, 0\n"
    "v_mov_b32 v138, 0\n"
    "v_mov_b32 v139, 0\n"
    "v_mov_b32 v140, 0\n"
    "v_mov_b32 v141, 0\n"
    "v_mov_b32 v142, 0\n"
    "v_mov_b32 v143, 0\n"
    "v_mov_b32 v144, 0\n"
    "v_mov_b32 v145, 0\n"
    "v_mov_b32 v146, 0\n"
    "v_mov_b32 v147, 0\n"
    "v_mov_b32 v148, 0\n"
    "v_mov_b32 v149, 0\n"
    "v_mov_b32 v150, 0\n"
    "v_mov_b32 v151, 0\n"
    "v_mov_b32 v152, 0\n"
    "v_mov_b32 v153, 0\n"
    "v_mov_b32 v154, 0\n"
    "v_mov_b32 v155, 0\n"
    "v_mov_b32 v156, 0\n"
    "v_mov_b32 v157, 0\n"
    "v_mov_b32 v158, 0\n"
    "v_mov_b32 v159, 0\n"
    "v_mov_b32 v160, 0\n"
    "v_mov_b32 v161, 0\n"
    "v_mov_b32 v162, 0\n"
    "v_mov_b32 v163, 0\n"
    "v_mov_b32 v164, 0\n"
    "v_mov_b32 v165, 0\n"
    "v_mov_b32 v166, 0\n"
    "v_mov_b32 v167, 0\n"
    "v_mov_b32 v168, 0\n"
    "v_mov_b32 v169, 0\n"
    "v_mov_b32 v170, 0\n"
    "v_mov_b32 v171, 0\n"
    "v_mov_b32 v172, 0\n"
    "v_mov_b32 v173, 0\n"
    "v_mov_b32 v174, 0\n"
    "v_mov_b32 v175, 0\n"
    "v_mov_b32 v176, 0\n"
    "v_mov_b32 v177, 0\n"
    "v_mov_b32 v178, 0\n"
    "v_mov_b32 v179, 0\n"
    "v_mov_b32 v180, 0\n"
    "v_mov_b32 v181, 0\n"
    "v_mov_b32 v182, 0\n"
    "v_mov_b32 v183, 0\n"
    "v_mov_b32 v184, 0\n"
    "v_mov_b32 v185, 0\n"
    "v_mov_b32 v186, 0\n"
    "v_mov_b32 v187, 0\n"
    "v_mov_b32 v188, 0\n"
    "v_mov_b32 v189, 0\n"
    "v_mov_b32 v190, 0\n"
    "v_mov_b32 v191, 0\n"
    "v_mov_b32 v192, 0\n"
    "v_mov_b32 v193, 0\n"
    "v_mov_b32 v194, 0\n"
    "v_mov_b32 v195, 0\n"
    "v_mov_b32 v196, 0\n"
    "v_mov_b32 v197, 0\n"
    "v_mov_b32 v198, 0\n"
    "v_mov_b32 v199, 0\n"
    "v_mov_b32 v200, 0\n"
    "v_mov_b32 v201, 0\n"
    "v_mov_b32 v202, 0\n"
    "v_mov_b32 v203, 0\n"
    "v_mov_b32 v204, 0\n"
    "v_mov_b32 v205, 0\n"
    "v_mov_b32 v206, 0\n"
    "v_mov_b32 v207, 0\n"
    "v_mov_b32 v208, 0\n"
    "v_mov_b32 v209, 0\n"
    "v_mov_b32 v210, 0\n"
    "v_mov_b32 v211, 0\n"
    "v_mov_b32 v212, 0\n"
    "v_mov_b32 v213, 0\n"
    "v_mov_b32 v214, 0\n"
    "v_mov_b32 v215, 0\n"
    "v_mov_b32 v216, 0\n"
    "v_mov_b32 v217, 0\n"
    "v_mov_b32 v218, 0\n"
    "v_mov_b32 v219, 0\n"
    "v_mov_b32 v220, 0\n"
    "v_mov_b32 v221, 0\n"
    "v_mov_b32 v222, 0\n"
    "v_mov_b32 v223, 0\n"
    "s_cmp_lg_u32 %16, 0\n"
    "s_cbranch_scc1 L_k4_g_done\n"
    "s_mov_b32 s90, 4\n"
    "s_mov_b32 s91, 0x20000\n"
    "s_mov_b32 s92, 131072\n"
    "s_mov_b64 s[88:89], %25\n"
    "L_k4_g_poll0:\n"
    "buffer_load_dword v227, off, s[88:91], 0 sc1\n"
    "s_waitcnt vmcnt(0)\n"
    "v_readfirstlane_b32 s93, v227\n"
    "s_nop 1\n"
    "s_cmp_ge_u32 s93, 32\n"
    "s_cbranch_scc1 L_k4_g_ok0\n"
    "s_sleep 1\n"
    "s_sub_u32 s92, s92, 1\n"
    "s_cmp_gt_i32 s92, 0\n"
    "s_cbranch_scc1 L_k4_g_poll0\n"
    "L_k4_g_ok0:\n"
    "L_k4_g_done:\n"
    "s_barrier\n"
    "s_getpc_b64 s[80:81]\n"
    "L_pc3:\n"
    "s_sub_u32 s80, s80, L_pc3-L_mega_code\n"
    "s_subb_u32 s81, s81, 0\n"
    "s_mov_b32 s82, L_text_end-L_mega_code\n"
    "s_mov_b32 s83, 0x20000\n"
    "s_and_b32 s84, %24, 3072\n"
    "s_add_u32 m0, s84, 159744\n"
    "s_nop 0\n"
    "buffer_load_dwordx4 %18, s[80:83], %24 offen lds\n"
    "s_add_u32 m0, s57, s56\n"
    "s_add_u32 m0, m0, 960\n"
    "s_add_u32 m0, m0, 960\n"
    "s_add_u32 m0, m0, 10368\n"
    "s_add_u32 m0, m0, 960\n"
    "s_add_u32 m0, m0, 960\n"
    "s_add_u32 m0, s57, s61\n"
    "s_add_u32 s53, s53, 458752\n"
    "s_add_u32 s54, s54, 458752\n"
    "s_add_u32 s57, s57, 28672\n"
    "s_cmp_ge_u32 s57, 159744\n"
    "s_cselect_b32 s57, 73728, s57\n"
    "buffer_load_dwordx4 a[112:115], %18, s[36:39], s52 offen sc1\n"
    "buffer_load_dwordx4 a[116:119], %18, s[36:39], s52 offen offset:1024 sc1\n"
    "buffer_load_dwordx4 a[120:123], %18, s[36:39], s52 offen offset:2048 sc1\n"
    "buffer_load_dwordx4 a[124:127], %18, s[36:39], s52 offen offset:3072 sc1\n"
    "s_add_u32 s52, s52, 4096\n"
    "buffer_load_dwordx4 a[128:131], %18, s[36:39], s52 offen sc1\n"
    "buffer_load_dwordx4 a[132:135], %18, s[36:39], s52 offen offset:1024 sc1\n"
    "buffer_load_dwordx4 a[136:139], %18, s[36:39], s52 offen offset:2048 sc1\n"
    "buffer_load_dwordx4 a[140:143], %18, s[36:39], s52 offen offset:3072 sc1\n"
    "s_add_u32 s52, s52, 4096\n"
    "s_add_u32 m0, s57, s56\n"
    "s_add_u32 m0, m0, 960\n"
    "s_add_u32 m0, m0, 960\n"
    "s_add_u32 m0, m0, 10368\n"
    "s_add_u32 m0, m0, 960\n"
    "s_add_u32 m0, m0, 960\n"
    "s_add_u32 m0, s57, s61\n"
    "s_add_u32 s53, s53, 458752\n"
    "s_add_u32 s54, s54, 458752\n"
    "s_add_u32 s57, s57, 28672\n"
    "s_cmp_ge_u32 s57, 159744\n"
    "s_cselect_b32 s57, 73728, s57\n"
    "buffer_load_dwordx4 a[144:147], %18, s[36:39], s52 offen sc1\n"
    "buffer_load_dwordx4 a[148:151], %18, s[36:39], s52 offen offset:1024 sc1\n"
    "buffer_load_dwordx4 a[152:155], %18, s[36:39], s52 offen offset:2048 sc1\n"
    "buffer_load_dwordx4 a[156:159], %18, s[36:39], s52 offen offset:3072 sc1\n"
    "s_add_u32 s52, s52, 4096\n"
    "buffer_load_dwordx4 a[160:163], %18, s[36:39], s52 offen sc1\n"
    "buffer_load_dwordx4 a[164:167], %18, s[36:39], s52 offen offset:1024 sc1\n"
    "buffer_load_dwordx4 a[168:171], %18, s[36:39], s52 offen offset:2048 sc1\n"
    "buffer_load_dwordx4 a[172:175], %18, s[36:39], s52 offen offset:3072 sc1\n"
    "s_add_u32 s52, s52, 4096\n"
    "s_add_u32 m0, s57, s56\n"
    "s_add_u32 m0, m0, 960\n"
    "s_add_u32 m0, m0, 960\n"
    "s_add_u32 m0, m0, 10368\n"
    "s_mov_b32 s60, 14\n"
    "s_mov_b32 s59, 15\n"
    "s_mov_b32 s55, 5\n"
    "s_waitcnt vmcnt(8)\n"
    "s_barrier\n"
    "ds_read_b64_tr_b16 a[0:1], v224 offset:0\n"
    "ds_read_b64_tr_b16 a[2:3], v224 offset:3072\n"
    "ds_read_b64_tr_b16 a[4:5], v224 offset:6144\n"
    "ds_read_b64_tr_b16 a[6:7], v224 offset:9216\n"
    "ds_read_b64_tr_b16 a[8:9], v225 offset:0\n"
    "ds_read_b64_tr_b16 a[10:11], v225 offset:3072\n"
    "ds_read_b64_tr_b16 a[12:13], v225 offset:6144\n"
    "ds_read_b64_tr_b16 a[14:15], v225 offset:9216\n"
    "ds_read_b64_tr_b16 a[16:17], v224 offset:1024\n"
    "ds_read_b64_tr_b16 a[18:19], v224 offset:4096\n"
    "ds_read_b64_tr_b16 a[20:21], v224 offset:7168\n"
    "ds_read_b64_tr_b16 a[22:23], v224 offset:10240\n"
    "ds_read_b64_tr_b16 a[24:25], v225 offset:1024\n"
    "ds_read_b64_tr_b16 a[26:27], v225 offset:4096\n"
    "ds_read_b64_tr_b16 a[28:29], v225 offset:7168\n"
    "ds_read_b64_tr_b16 a[30:31], v225 offset:10240\n"
    "ds_read_b64_tr_b16 a[32:33], v224 offset:2048\n"
    "ds_read_b64_tr_b16 a[34:35], v224 offset:5120\n"
    "ds_read_b64_tr_b16 a[36:37], v224 offset:8192\n"
    "ds_read_b64_tr_b16 a[38:39], v224 offset:11264\n"
    "ds_read_b64_tr_b16 a[40:41], v225 offset:2048\n"
    "ds_read_b64_tr_b16 a[42:43], v225 offset:5120\n"
    "ds_read_b64_tr_b16 a[44:45], v225 offset:8192\n"
    "ds_read_b64_tr_b16 a[46:47], v225 offset:11264\n"
    "ds_read_b64_tr_b16 a[48:49], v226 offset:24576\n"
    "ds_read_b64_tr_b16 a[50:51], v226 offset:25088\n"
    "ds_read_b64_tr_b16 a[52:53], v226 offset:25600\n"
    "ds_read_b64_tr_b16 a[54:55], v226 offset:26112\n"
    "s_waitcnt lgkmcnt(0)\n"
    ".p2align 6\n"
    "L_k4_loop:\n"
    "v_mfma_f32_16x16x32_bf16 v[0:3], a[0:3], a[112:115], v[0:3]\n"
    "s_sub_u32 s59, s59, 1\n"
    "ds_read_b64_tr_b16 a[56:57], v224 offset:12288\n"
    "v_mfma_f32_16x16x32_bf16 v[0:3], a[4:7], a[128:131], v[0:3]\n"
    "s_cmp_gt_i32 s59, 0\n"
    "ds_read_b64_tr_b16 a[58:59], v224 offset:15360\n"
    "v_mfma_f32_16x16x32_bf16 v[4:7], a[4:7], a[132:135], v[4:7]\n"
    "s_cselect_b32 s38, s38, 0\n"
    "ds_read_b64_tr_b16 a[60:61], v224 offset:18432\n"
    "v_mfma_f32_16x16x32_bf16 v[4:7], a[0:3], a[116:119], v[4:7]\n"
    "ds_read_b64_tr_b16 a[62:63], v224 offset:21504\n"
    "v_mfma_f32_16x16x32_bf16 v[8:11], a[0:3], a[120:123], v[8:11]\n"
    "buffer_load_dwordx4 a[176:179], %18, s[36:39], s52 offen sc1\n"
    "v_mfma_f32_16x16x32_bf16 v[8:11], a[4:7], a[136:139], v[8:11]\n"
    "ds_read_b64_tr_b16 a[64:65], v225 offset:12288\n"
    "v_mfma_f32_16x16x32_bf16 v[12:15], a[4:7], a[140:143], v[12:15]\n"
    "ds_read_b64_tr_b16 a[66:67], v225 offset:15360\n"
    "v_mfma_f32_16x16x32_bf16 v[12:15], a[0:3], a[124:127], v[12:15]\n"
    "ds_read_b64_tr_b16 a[68:69], v225 offset:18432\n"
    "v_mfma_f32_16x16x32_bf16 v[28:31], a[8:11], a[124:127], v[28:31]\n"
    "ds_read_b64_tr_b16 a[70:71], v225 offset:21504\n"
    "v_mfma_f32_16x16x32_bf16 v[28:31], a[12:15], a[140:143], v[28:31]\n"
    "s_add_u32 m0, m0, 960\n"
    "ds_read_b64_tr_b16 a[72:73], v224 offset:13312\n"
    "v_mfma_f32_16x16x32_bf16 v[24:27], a[12:15], a[136:139], v[24:27]\n"
    "buffer_load_dwordx4 %22, s[44:47], s53 offen offset:64 lds\n"
    "v_mfma_f32_16x16x32_bf16 v[24:27], a[8:11], a[120:123], v[24:27]\n"
    "ds_read_b64_tr_b16 a[74:75], v224 offset:16384\n"
    "v_mfma_f32_16x16x32_bf16 v[20:23], a[8:11], a[116:119], v[20:23]\n"
    "ds_read_b64_tr_b16 a[76:77], v224 offset:19456\n"
    "v_mfma_f32_16x16x32_bf16 v[20:23], a[12:15], a[132:135], v[20:23]\n"
    "ds_read_b64_tr_b16 a[78:79], v224 offset:22528\n"
    "v_mfma_f32_16x16x32_bf16 v[16:19], a[12:15], a[128:131], v[16:19]\n"
    "ds_read_b64_tr_b16 a[80:81], v225 offset:13312\n"
    "v_mfma_f32_16x16x32_bf16 v[16:19], a[8:11], a[112:115], v[16:19]\n"
    "ds_read_b64_tr_b16 a[82:83], v225 offset:16384\n"
    "v_mfma_f32_16x16x32_bf16 v[32:35], a[16:19], a[112:115], v[32:35]\n"
    "buffer_load_dwordx4 a[180:183], %18, s[36:39], s52 offen offset:1024 sc1\n"
    "v_mfma_f32_16x16x32_bf16 v[32:35], a[20:23], a[128:131], v[32:35]\n"
    "ds_read_b64_tr_b16 a[84:85], v225 offset:19456\n"
    "v_mfma_f32_16x16x32_bf16 v[36:39], a[20:23], a[132:135], v[36:39]\n"
    "ds_read_b64_tr_b16 a[86:87], v225 offset:22528\n"
    "v_mfma_f32_16x16x32_bf16 v[36:39], a[16:19], a[116:119], v[36:39]\n"
    "ds_read_b64_tr_b16 a[88:89], v224 offset:14336\n"
    "v_mfma_f32_16x16x32_bf16 v[40:43], a[16:19], a[120:123], v[40:43]\n"
    "ds_read_b64_tr_b16 a[90:91], v224 offset:17408\n"
    "v_mfma_f32_16x16x32_bf16 v[40:43], a[20:23], a[136:139], v[40:43]\n"
    "s_add_u32 m0, m0, 960\n"
    "ds_read_b64_tr_b16 a[92:93], v224 offset:20480\n"
    "v_mfma_f32_16x16x32_bf16 v[44:47], a[20:23], a[140:143], v[44:47]\n"
    "buffer_load_dwordx4 %22, s[44:47], s53 offen offset:128 lds\n"
    "v_mfma_f32_16x16x32_bf16 v[44:47], a[16:19], a[124:127], v[44:47]\n"
    "ds_read_b64_tr_b16 a[94:95], v224 offset:23552\n"
    "v_mfma_f32_16x16x32_bf16 v[60:63], a[24:27], a[124:127], v[60:63]\n"
    "ds_read_b64_tr_b16 a[96:97], v225 offset:14336\n"
    "v_mfma_f32_16x16x32_bf16 v[60:63], a[28:31], a[140:143], v[60:63]\n"
    "ds_read_b64_tr_b16 a[98:99], v225 offset:17408\n"
    "v_mfma_f32_16x16x32_bf16 v[56:59], a[28:31], a[136:139], v[56:59]\n"
    "ds_read_b64_tr_b16 a[100:101], v225 offset:20480\n"
    "v_mfma_f32_16x16x32_bf16 v[56:59], a[24:27], a[120:123], v[56:59]\n"
    "ds_read_b64_tr_b16 a[102:103], v225 offset:23552\n"
    "v_mfma_f32_16x16x32_bf16 v[52:55], a[24:27], a[116:119], v[52:55]\n"
    "buffer_load_dwordx4 a[184:187], %18, s[36:39], s52 offen offset:2048 sc1\n"
    "v_mfma_f32_16x16x32_bf16 v[52:55], a[28:31], a[132:135], v[52:55]\n"
    "ds_read_b64_tr_b16 a[104:105], v226 offset:26624\n"
    "v_mfma_f32_16x16x32_bf16 v[48:51], a[28:31], a[128:131], v[48:51]\n"
    "ds_read_b64_tr_b16 a[106:107], v226 offset:27136\n"
    "v_mfma_f32_16x16x32_bf16 v[48:51], a[24:27], a[112:115], v[48:51]\n"
    "ds_read_b64_tr_b16 a[108:109], v226 offset:27648\n"
    "v_mfma_f32_16x16x32_bf16 v[64:67], a[32:35], a[112:115], v[64:67]\n"
    "ds_read_b64_tr_b16 a[110:111], v226 offset:28160\n"
    "v_mfma_f32_16x16x32_bf16 v[64:67], a[36:39], a[128:131], v[64:67]\n"
    "s_add_u32 m0, s57, s61\n"
    "v_mfma_f32_16x16x32_bf16 v[68:71], a[36:39], a[132:135], v[68:71]\n"
    "buffer_load_dwordx4 %23, s[48:51], s54 offen lds\n"
    "v_mfma_f32_16x16x32_bf16 v[68:71], a[32:35], a[116:119], v[68:71]\n"
    "s_add_u32 s53, s53, 458752\n"
    "v_mfma_f32_16x16x32_bf16 v[72:75], a[32:35], a[120:123], v[72:75]\n"
    "s_add_u32 s54, s54, 458752\n"
    "v_mfma_f32_16x16x32_bf16 v[72:75], a[36:39], a[136:139], v[72:75]\n"
    "s_add_u32 s57, s57, 28672\n"
    "v_mfma_f32_16x16x32_bf16 v[76:79], a[36:39], a[140:143], v[76:79]\n"
    "s_cmp_ge_u32 s57, 159744\n"
    "v_mfma_f32_16x16x32_bf16 v[76:79], a[32:35], a[124:127], v[76:79]\n"
    "s_cselect_b32 s57, 73728, s57\n"
    "v_mfma_f32_16x16x32_bf16 v[92:95], a[40:43], a[124:127], v[92:95]\n"
    "buffer_load_dwordx4 a[188:191], %18, s[36:39], s52 offen offset:3072 sc1\n"
    "v_mfma_f32_16x16x32_bf16 v[92:95], a[44:47], a[140:143], v[92:95]\n"
    "s_add_u32 s52, s52, 4096\n"
    "v_mfma_f32_16x16x32_bf16 v[88:91], a[44:47], a[136:139], v[88:91]\n"
    "s_add_u32 s58, s58, 28672\n"
    "v_mfma_f32_16x16x32_bf16 v[88:91], a[40:43], a[120:123], v[88:91]\n"
    "s_cmp_ge_u32 s58, 159744\n"
    "v_mfma_f32_16x16x32_bf16 v[84:87], a[40:43], a[116:119], v[84:87]\n"
    "s_cselect_b32 s58, 73728, s58\n"
    "v_mfma_f32_16x16x32_bf16 v[84:87], a[44:47], a[132:135], v[84:87]\n"
    "v_add_u32 v224, s58, %19\n"
    "v_add_u32 v225, s58, %20\n"
    "v_add_u32 v226, s58, %21\n"
    "v_mfma_f32_16x16x32_bf16 v[80:83], a[44:47], a[128:131], v[80:83]\n"
    "v_mfma_f32_16x16x32_bf16 v[80:83], a[40:43], a[112:115], v[80:83]\n"
    "v_mfma_f32_16x16x32_bf16 v[96:99], a[48:51], a[112:115], v[96:99]\n"
    "v_mfma_f32_16x16x32_bf16 v[96:99], a[52:55], a[128:131], v[96:99]\n"
    "v_mfma_f32_16x16x32_bf16 v[100:103], a[52:55], a[132:135], v[100:103]\n"
    "v_mfma_f32_16x16x32_bf16 v[100:103], a[48:51], a[116:119], v[100:103]\n"
    "v_mfma_f32_16x16x32_bf16 v[104:107], a[48:51], a[120:123], v[104:107]\n"
    "v_mfma_f32_16x16x32_bf16 v[104:107], a[52:55], a[136:139], v[104:107]\n"
    "v_mfma_f32_16x16x32_bf16 v[108:111], a[52:55], a[140:143], v[108:111]\n"
    "v_mfma_f32_16x16x32_bf16 v[108:111], a[48:51], a[124:127], v[108:111]\n"
    "s_waitcnt vmcnt(16) lgkmcnt(0)\n"
    "v_mfma_f32_16x16x32_bf16 v[112:115], a[56:59], a[112:115], v[112:115]\n"
    "s_barrier\n"
    "v_mfma_f32_16x16x32_bf16 v[112:115], a[60:63], a[128:131], v[112:115]\n"
    "s_sub_u32 s60, s60, 1\n"
    "ds_read_b64_tr_b16 a[0:1], v224 offset:0\n"
    "v_mfma_f32_16x16x32_bf16 v[116:119], a[60:63], a[132:135], v[116:119]\n"
    "s_cmp_gt_i32 s60, 0\n"
    "ds_read_b64_tr_b16 a[2:3], v224 offset:3072\n"
    "v_mfma_f32_16x16x32_bf16 v[116:119], a[56:59], a[116:119], v[116:119]\n"
    "s_cselect_b32 s42, s42, 0\n"
    "ds_read_b64_tr_b16 a[4:5], v224 offset:6144\n"
    "v_mfma_f32_16x16x32_bf16 v[120:123], a[56:59], a[120:123], v[120:123]\n"
    "s_cselect_b32 s46, s46, 0\n"
    "ds_read_b64_tr_b16 a[6:7], v224 offset:9216\n"
    "v_mfma_f32_16x16x32_bf16 v[120:123], a[60:63], a[136:139], v[120:123]\n"
    "s_cselect_b32 s50, s50, 0\n"
    "ds_read_b64_tr_b16 a[8:9], v225 offset:0\n"
    "v_mfma_f32_16x16x32_bf16 v[124:127], a[60:63], a[140:143], v[124:127]\n"
    "ds_read_b64_tr_b16 a[10:11], v225 offset:3072\n"
    "v_mfma_f32_16x16x32_bf16 v[124:127], a[56:59], a[124:127], v[124:127]\n"
    "buffer_load_dwordx4 a[192:195], %18, s[36:39], s52 offen sc1\n"
    "v_mfma_f32_16x16x32_bf16 v[140:143], a[64:67], a[124:127], v[140:143]\n"
    "ds_read_b64_tr_b16 a[12:13], v225 offset:6144\n"
    "v_mfma_f32_16x16x32_bf16 v[140:143], a[68:71], a[140:143], v[140:143]\n"
    "ds_read_b64_tr_b16 a[14:15], v225 offset:9216\n"
    "v_mfma_f32_16x16x32_bf16 v[136:139], a[68:71], a[136:139], v[136:139]\n"
    "ds_read_b64_tr_b16 a[16:17], v224 offset:1024\n"
    "v_mfma_f32_16x16x32_bf16 v[136:139], a[64:67], a[120:123], v[136:139]\n"
    "s_add_u32 m0, s57, s56\n"
    "ds_read_b64_tr_b16 a[18:19], v224 offset:4096\n"
    "v_mfma_f32_16x16x32_bf16 v[132:135], a[64:67], a[116:119], v[132:135]\n"
    "buffer_load_dwordx4 %22, s[40:43], s53 offen lds\n"
    "v_mfma_f32_16x16x32_bf16 v[132:135], a[68:71], a[132:135], v[132:135]\n"
    "ds_read_b64_tr_b16 a[20:21], v224 offset:7168\n"
    "v_mfma_f32_16x16x32_bf16 v[128:131], a[68:71], a[128:131], v[128:131]\n"
    "ds_read_b64_tr_b16 a[22:23], v224 offset:10240\n"
    "v_mfma_f32_16x16x32_bf16 v[128:131], a[64:67], a[112:115], v[128:131]\n"
    "ds_read_b64_tr_b16 a[24:25], v225 offset:1024\n"
    "v_mfma_f32_16x16x32_bf16 v[144:147], a[72:75], a[112:115], v[144:147]\n"
    "ds_read_b64_tr_b16 a[26:27], v225 offset:4096\n"
    "v_mfma_f32_16x16x32_bf16 v[144:147], a[76:79], a[128:131], v[144:147]\n"
    "buffer_load_dwordx4 a[196:199], %18, s[36:39], s52 offen offset:1024 sc1\n"
    "v_mfma_f32_16x16x32_bf16 v[148:151], a[76:79], a[132:135], v[148:151]\n"
    "ds_read_b64_tr_b16 a[28:29], v225 offset:7168\n"
    "v_mfma_f32_16x16x32_bf16 v[148:151], a[72:75], a[116:119], v[148:151]\n"
    "ds_read_b64_tr_b16 a[30:31], v225 offset:10240\n"
    "v_mfma_f32_16x16x32_bf16 v[152:155], a[72:75], a[120:123], v[152:155]\n"
    "ds_read_b64_tr_b16 a[32:33], v224 offset:2048\n"
    "v_mfma_f32_16x16x32_bf16 v[152:155], a[76:79], a[136:139], v[152:155]\n"
    "s_add_u32 m0, m0, 960\n"
    "ds_read_b64_tr_b16 a[34:35], v224 offset:5120\n"
    "v_mfma_f32_16x16x32_bf16 v[156:159], a[76:79], a[140:143], v[156:159]\n"
    "buffer_load_dwordx4 %22, s[40:43], s53 offen offset:64 lds\n"
    "v_mfma_f32_16x16x32_bf16 v[156:159], a[72:75], a[124:127], v[156:159]\n"
    "ds_read_b64_tr_b16 a[36:37], v224 offset:8192\n"
    "v_mfma_f32_16x16x32_bf16 v[172:175], a[80:83], a[124:127], v[172:175]\n"
    "ds_read_b64_tr_b16 a[38:39], v224 offset:11264\n"
    "v_mfma_f32_16x16x32_bf16 v[172:175], a[84:87], a[140:143], v[172:175]\n"
    "ds_read_b64_tr_b16 a[40:41], v225 offset:2048\n"
    "v_mfma_f32_16x16x32_bf16 v[168:171], a[84:87], a[136:139], v[168:171]\n"
    "ds_read_b64_tr_b16 a[42:43], v225 offset:5120\n"
    "v_mfma_f32_16x16x32_bf16 v[168:171], a[80:83], a[120:123], v[168:171]\n"
    "buffer_load_dwordx4 a[200:203], %18, s[36:39], s52 offen offset:2048 sc1\n"
    "v_mfma_f32_16x16x32_bf16 v[164:167], a[80:83], a[116:119], v[164:167]\n"
    "ds_read_b64_tr_b16 a[44:45], v225 offset:8192\n"
    "v_mfma_f32_16x16x32_bf16 v[164:167], a[84:87], a[132:135], v[164:167]\n"
    "ds_read_b64_tr_b16 a[46:47], v225 offset:11264\n"
    "v_mfma_f32_16x16x32_bf16 v[160:163], a[84:87], a[128:131], v[160:163]\n"
    "ds_read_b64_tr_b16 a[48:49], v226 offset:24576\n"
    "v_mfma_f32_16x16x32_bf16 v[160:163], a[80:83], a[112:115], v[160:163]\n"
    "s_add_u32 m0, m0, 960\n"
    "ds_read_b64_tr_b16 a[50:51], v226 offset:25088\n"
    "v_mfma_f32_16x16x32_bf16 v[176:179], a[88:91], a[112:115], v[176:179]\n"
    "buffer_load_dwordx4 %22, s[40:43], s53 offen offset:128 lds\n"
    "v_mfma_f32_16x16x32_bf16 v[176:179], a[92:95], a[128:131], v[176:179]\n"
    "ds_read_b64_tr_b16 a[52:53], v226 offset:25600\n"
    "v_mfma_f32_16x16x32_bf16 v[180:183], a[92:95], a[132:135], v[180:183]\n"
    "ds_read_b64_tr_b16 a[54:55], v226 offset:26112\n"
    "v_mfma_f32_16x16x32_bf16 v[180:183], a[88:91], a[116:119], v[180:183]\n"
    "v_mfma_f32_16x16x32_bf16 v[184:187], a[88:91], a[120:123], v[184:187]\n"
    "v_mfma_f32_16x16x32_bf16 v[184:187], a[92:95], a[136:139], v[184:187]\n"
    "buffer_load_dwordx4 a[204:207], %18, s[36:39], s52 offen offset:3072 sc1\n"
    "v_mfma_f32_16x16x32_bf16 v[188:191], a[92:95], a[140:143], v[188:191]\n"
    "s_add_u32 s52, s52, 4096\n"
    "v_mfma_f32_16x16x32_bf16 v[188:191], a[88:91], a[124:127], v[188:191]\n"
    "v_mfma_f32_16x16x32_bf16 v[204:207], a[96:99], a[124:127], v[204:207]\n"
    "v_mfma_f32_16x16x32_bf16 v[204:207], a[100:103], a[140:143], v[204:207]\n"
    "s_add_u32 m0, m0, 10368\n"
    "v_mfma_f32_16x16x32_bf16 v[200:203], a[100:103], a[136:139], v[200:203]\n"
    "buffer_load_dwordx4 %22, s[44:47], s53 offen lds\n"
    "v_mfma_f32_16x16x32_bf16 v[200:203], a[96:99], a[120:123], v[200:203]\n"
    "v_mfma_f32_16x16x32_bf16 v[196:199], a[96:99], a[116:119], v[196:199]\n"
    "v_mfma_f32_16x16x32_bf16 v[196:199], a[100:103], a[132:135], v[196:199]\n"
    "v_mfma_f32_16x16x32_bf16 v[192:195], a[100:103], a[128:131], v[192:195]\n"
    "v_mfma_f32_16x16x32_bf16 v[192:195], a[96:99], a[112:115], v[192:195]\n"
    "v_mfma_f32_16x16x32_bf16 v[208:211], a[104:107], a[112:115], v[208:211]\n"
    "v_mfma_f32_16x16x32_bf16 v[208:211], a[108:111], a[128:131], v[208:211]\n"
    "v_mfma_f32_16x16x32_bf16 v[212:215], a[108:111], a[132:135], v[212:215]\n"
    "v_mfma_f32_16x16x32_bf16 v[212:215], a[104:107], a[116:119], v[212:215]\n"
    "v_mfma_f32_16x16x32_bf16 v[216:219], a[104:107], a[120:123], v[216:219]\n"
    "v_mfma_f32_16x16x32_bf16 v[216:219], a[108:111], a[136:139], v[216:219]\n"
    "v_mfma_f32_16x16x32_bf16 v[220:223], a[108:111], a[140:143], v[220:223]\n"
    "v_mfma_f32_16x16x32_bf16 v[220:223], a[104:107], a[124:127], v[220:223]\n"
    "s_waitcnt vmcnt(15) lgkmcnt(0)\n"
    "v_mfma_f32_16x16x32_bf16 v[0:3], a[0:3], a[144:147], v[0:3]\n"
    "s_sub_u32 s59, s59, 1\n"
    "ds_read_b64_tr_b16 a[56:57], v224 offset:12288\n"
    "v_mfma_f32_16x16x32_bf16 v[0:3], a[4:7], a[160:163], v[0:3]\n"
    "s_cmp_gt_i32 s59, 0\n"
    "ds_read_b64_tr_b16 a[58:59], v224 offset:15360\n"
    "v_mfma_f32_16x16x32_bf16 v[4:7], a[4:7], a[164:167], v[4:7]\n"
    "s_cselect_b32 s38, s38, 0\n"
    "ds_read_b64_tr_b16 a[60:61], v224 offset:18432\n"
    "v_mfma_f32_16x16x32_bf16 v[4:7], a[0:3], a[148:151], v[4:7]\n"
    "ds_read_b64_tr_b16 a[62:63], v224 offset:21504\n"
    "v_mfma_f32_16x16x32_bf16 v[8:11], a[0:3], a[152:155], v[8:11]\n"
    "buffer_load_dwordx4 a[112:115], %18, s[36:39], s52 offen sc1\n"
    "v_mfma_f32_16x16x32_bf16 v[8:11], a[4:7], a[168:171], v[8:11]\n"
    "ds_read_b64_tr_b16 a[64:65], v225 offset:12288\n"
    "v_mfma_f32_16x16x32_bf16 v[12:15], a[4:7], a[172:175], v[12:15]\n"
    "ds_read_b64_tr_b16 a[66:67], v225 offset:15360\n"
    "v_mfma_f32_16x16x32_bf16 v[12:15], a[0:3], a[156:159], v[12:15]\n"
    "ds_read_b64_tr_b16 a[68:69], v225 offset:18432\n"
    "v_mfma_f32_16x16x32_bf16 v[28:31], a[8:11], a[156:159], v[28:31]\n"
    "ds_read_b64_tr_b16 a[70:71], v225 offset:21504\n"
    "v_mfma_f32_16x16x32_bf16 v[28:31], a[12:15], a[172:175], v[28:31]\n"
    "s_add_u32 m0, m0, 960\n"
    "ds_read_b64_tr_b16 a[72:73], v224 offset:13312\n"
    "v_mfma_f32_16x16x32_bf16 v[24:27], a[12:15], a[168:171], v[24:27]\n"
    "buffer_load_dwordx4 %22, s[44:47], s53 offen offset:64 lds\n"
    "v_mfma_f32_16x16x32_bf16 v[24:27], a[8:11], a[152:155], v[24:27]\n"
    "ds_read_b64_tr_b16 a[74:75], v224 offset:16384\n"
    "v_mfma_f32_16x16x32_bf16 v[20:23], a[8:11], a[148:151], v[20:23]\n"
    "ds_read_b64_tr_b16 a[76:77], v224 offset:19456\n"
    "v_mfma_f32_16x16x32_bf16 v[20:23], a[12:15], a[164:167], v[20:23]\n"
    "ds_read_b64_tr_b16 a[78:79], v224 offset:22528\n"
    "v_mfma_f32_16x16x32_bf16 v[16:19], a[12:15], a[160:163], v[16:19]\n"
    "ds_read_b64_tr_b16 a[80:81], v225 offset:13312\n"
    "v_mfma_f32_16x16x32_bf16 v[16:19], a[8:11], a[144:147], v[16:19]\n"
    "ds_read_b64_tr_b16 a[82:83], v225 offset:16384\n"
    "v_mfma_f32_16x16x32_bf16 v[32:35], a[16:19], a[144:147], v[32:35]\n"
    "buffer_load_dwordx4 a[116:119], %18, s[36:39], s52 offen offset:1024 sc1\n"
    "v_mfma_f32_16x16x32_bf16 v[32:35], a[20:23], a[160:163], v[32:35]\n"
    "ds_read_b64_tr_b16 a[84:85], v225 offset:19456\n"
    "v_mfma_f32_16x16x32_bf16 v[36:39], a[20:23], a[164:167], v[36:39]\n"
    "ds_read_b64_tr_b16 a[86:87], v225 offset:22528\n"
    "v_mfma_f32_16x16x32_bf16 v[36:39], a[16:19], a[148:151], v[36:39]\n"
    "ds_read_b64_tr_b16 a[88:89], v224 offset:14336\n"
    "v_mfma_f32_16x16x32_bf16 v[40:43], a[16:19], a[152:155], v[40:43]\n"
    "ds_read_b64_tr_b16 a[90:91], v224 offset:17408\n"
    "v_mfma_f32_16x16x32_bf16 v[40:43], a[20:23], a[168:171], v[40:43]\n"
    "s_add_u32 m0, m0, 960\n"
    "ds_read_b64_tr_b16 a[92:93], v224 offset:20480\n"
    "v_mfma_f32_16x16x32_bf16 v[44:47], a[20:23], a[172:175], v[44:47]\n"
    "buffer_load_dwordx4 %22, s[44:47], s53 offen offset:128 lds\n"
    "v_mfma_f32_16x16x32_bf16 v[44:47], a[16:19], a[156:159], v[44:47]\n"
    "ds_read_b64_tr_b16 a[94:95], v224 offset:23552\n"
    "v_mfma_f32_16x16x32_bf16 v[60:63], a[24:27], a[156:159], v[60:63]\n"
    "ds_read_b64_tr_b16 a[96:97], v225 offset:14336\n"
    "v_mfma_f32_16x16x32_bf16 v[60:63], a[28:31], a[172:175], v[60:63]\n"
    "ds_read_b64_tr_b16 a[98:99], v225 offset:17408\n"
    "v_mfma_f32_16x16x32_bf16 v[56:59], a[28:31], a[168:171], v[56:59]\n"
    "ds_read_b64_tr_b16 a[100:101], v225 offset:20480\n"
    "v_mfma_f32_16x16x32_bf16 v[56:59], a[24:27], a[152:155], v[56:59]\n"
    "ds_read_b64_tr_b16 a[102:103], v225 offset:23552\n"
    "v_mfma_f32_16x16x32_bf16 v[52:55], a[24:27], a[148:151], v[52:55]\n"
    "buffer_load_dwordx4 a[120:123], %18, s[36:39], s52 offen offset:2048 sc1\n"
    "v_mfma_f32_16x16x32_bf16 v[52:55], a[28:31], a[164:167], v[52:55]\n"
    "ds_read_b64_tr_b16 a[104:105], v226 offset:26624\n"
    "v_mfma_f32_16x16x32_bf16 v[48:51], a[28:31], a[160:163], v[48:51]\n"
    "ds_read_b64_tr_b16 a[106:107], v226 offset:27136\n"
    "v_mfma_f32_16x16x32_bf16 v[48:51], a[24:27], a[144:147], v[48:51]\n"
    "ds_read_b64_tr_b16 a[108:109], v226 offset:27648\n"
    "v_mfma_f32_16x16x32_bf16 v[64:67], a[32:35], a[144:147], v[64:67]\n"
    "ds_read_b64_tr_b16 a[110:111], v226 offset:28160\n"
    "v_mfma_f32_16x16x32_bf16 v[64:67], a[36:39], a[160:163], v[64:67]\n"
    "s_add_u32 m0, s57, s61\n"
    "v_mfma_f32_16x16x32_bf16 v[68:71], a[36:39], a[164:167], v[68:71]\n"
    "buffer_load_dwordx4 %23, s[48:51], s54 offen lds\n"
    "v_mfma_f32_16x16x32_bf16 v[68:71], a[32:35], a[148:151], v[68:71]\n"
    "s_add_u32 s53, s53, 458752\n"
    "v_mfma_f32_16x16x32_bf16 v[72:75], a[32:35], a[152:155], v[72:75]\n"
    "s_add_u32 s54, s54, 458752\n"
    "v_mfma_f32_16x16x32_bf16 v[72:75], a[36:39], a[168:171], v[72:75]\n"
    "s_add_u32 s57, s57, 28672\n"
    "v_mfma_f32_16x16x32_bf16 v[76:79], a[36:39], a[172:175], v[76:79]\n"
    "s_cmp_ge_u32 s57, 159744\n"
    "v_mfma_f32_16x16x32_bf16 v[76:79], a[32:35], a[156:159], v[76:79]\n"
    "s_cselect_b32 s57, 73728, s57\n"
    "v_mfma_f32_16x16x32_bf16 v[92:95], a[40:43], a[156:159], v[92:95]\n"
    "buffer_load_dwordx4 a[124:127], %18, s[36:39], s52 offen offset:3072 sc1\n"
    "v_mfma_f32_16x16x32_bf16 v[92:95], a[44:47], a[172:175], v[92:95]\n"
    "s_add_u32 s52, s52, 4096\n"
    "v_mfma_f32_16x16x32_bf16 v[88:91], a[44:47], a[168:171], v[88:91]\n"
    "s_add_u32 s58, s58, 28672\n"
    "v_mfma_f32_16x16x32_bf16 v[88:91], a[40:43], a[152:155], v[88:91]\n"
    "s_cmp_ge_u32 s58, 159744\n"
    "v_mfma_f32_16x16x32_bf16 v[84:87], a[40:43], a[148:151], v[84:87]\n"
    "s_cselect_b32 s58, 73728, s58\n"
    "v_mfma_f32_16x16x32_bf16 v[84:87], a[44:47], a[164:167], v[84:87]\n"
    "v_add_u32 v224, s58, %19\n"
    "v_add_u32 v225, s58, %20\n"
    "v_add_u32 v226, s58, %21\n"
    "v_mfma_f32_16x16x32_bf16 v[80:83], a[44:47], a[160:163], v[80:83]\n"
    "v_mfma_f32_16x16x32_bf16 v[80:83], a[40:43], a[144:147], v[80:83]\n"
    "v_mfma_f32_16x16x32_bf16 v[96:99], a[48:51], a[144:147], v[96:99]\n"
    "v_mfma_f32_16x16x32_bf16 v[96:99], a[52:55], a[160:163], v[96:99]\n"
    "v_mfma_f32_16x16x32_bf16 v[100:103], a[52:55], a[164:167], v[100:103]\n"
    "v_mfma_f32_16x16x32_bf16 v[100:103], a[48:51], a[148:151], v[100:103]\n"
    "v_mfma_f32_16x16x32_bf16 v[104:107], a[48:51], a[152:155], v[104:107]\n"
    "v_mfma_f32_16x16x32_bf16 v[104:107], a[52:55], a[168:171], v[104:107]\n"
    "v_mfma_f32_16x16x32_bf16 v[108:111], a[52:55], a[172:175], v[108:111]\n"
    "v_mfma_f32_16x16x32_bf16 v[108:111], a[48:51], a[156:159], v[108:111]\n"
    "s_waitcnt vmcnt(16) lgkmcnt(0)\n"
    "v_mfma_f32_16x16x32_bf16 v[112:115], a[56:59], a[144:147], v[112:115]\n"
    "s_barrier\n"
    "v_mfma_f32_16x16x32_bf16 v[112:115], a[60:63], a[160:163], v[112:115]\n"
    "s_sub_u32 s60, s60, 1\n"
    "ds_read_b64_tr_b16 a[0:1], v224 offset:0\n"
    "v_mfma_f32_16x16x32_bf16 v[116:119], a[60:63], a[164:167], v[116:119]\n"
    "s_cmp_gt_i32 s60, 0\n"
    "ds_read_b64_tr_b16 a[2:3], v224 offset:3072\n"
    "v_mfma_f32_16x16x32_bf16 v[116:119], a[56:59], a[148:151], v[116:119]\n"
    "s_cselect_b32 s42, s42, 0\n"
    "ds_read_b64_tr_b16 a[4:5], v224 offset:6144\n"
    "v_mfma_f32_16x16x32_bf16 v[120:123], a[56:59], a[152:155], v[120:123]\n"
    "s_cselect_b32 s46, s46, 0\n"
    "ds_read_b64_tr_b16 a[6:7], v224 offset:9216\n"
    "v_mfma_f32_16x16x32_bf16 v[120:123], a[60:63], a[168:171], v[120:123]\n"
    "s_cselect_b32 s50, s50, 0\n"
    "ds_read_b64_tr_b16 a[8:9], v225 offset:0\n"
    "v_mfma_f32_16x16x32_bf16 v[124:127], a[60:63], a[172:175], v[124:127]\n"
    "ds_read_b64_tr_b16 a[10:11], v225 offset:3072\n"
    "v_mfma_f32_16x16x32_bf16 v[124:127], a[56:59], a[156:159], v[124:127]\n"
    "buffer_load_dwordx4 a[128:131], %18, s[36:39], s52 offen sc1\n"
    "v_mfma_f32_16x16x32_bf16 v[140:143], a[64:67], a[156:159], v[140:143]\n"
    "ds_read_b64_tr_b16 a[12:13], v225 offset:6144\n"
    "v_mfma_f32_16x16x32_bf16 v[140:143], a[68:71], a[172:175], v[140:143]\n"
    "ds_read_b64_tr_b16 a[14:15], v225 offset:9216\n"
    "v_mfma_f32_16x16x32_bf16 v[136:139], a[68:71], a[168:171], v[136:139]\n"
    "ds_read_b64_tr_b16 a[16:17], v224 offset:1024\n"
    "v_mfma_f32_16x16x32_bf16 v[136:139], a[64:67], a[152:155], v[136:139]\n"
    "s_add_u32 m0, s57, s56\n"
    "ds_read_b64_tr_b16 a[18:19], v224 offset:4096\n"
    "v_mfma_f32_16x16x32_bf16 v[132:135], a[64:67], a[148:151], v[132:135]\n"
    "buffer_load_dwordx4 %22, s[40:43], s53 offen lds\n"
    "v_mfma_f32_16x16x32_bf16 v[132:135], a[68:71], a[164:167], v[132:135]\n"
    "ds_read_b64_tr_b16 a[20:21], v224 offset:7168\n"
    "v_mfma_f32_16x16x32_bf16 v[128:131], a[68:71], a[160:163], v[128:131]\n"
    "ds_read_b64_tr_b16 a[22:23], v224 offset:10240\n"
    "v_mfma_f32_16x16x32_bf16 v[128:131], a[64:67], a[144:147], v[128:131]\n"
    "ds_read_b64_tr_b16 a[24:25], v225 offset:1024\n"
    "v_mfma_f32_16x16x32_bf16 v[144:147], a[72:75], a[144:147], v[144:147]\n"
    "ds_read_b64_tr_b16 a[26:27], v225 offset:4096\n"
    "v_mfma_f32_16x16x32_bf16 v[144:147], a[76:79], a[160:163], v[144:147]\n"
    "buffer_load_dwordx4 a[132:135], %18, s[36:39], s52 offen offset:1024 sc1\n"
    "v_mfma_f32_16x16x32_bf16 v[148:151], a[76:79], a[164:167], v[148:151]\n"
    "ds_read_b64_tr_b16 a[28:29], v225 offset:7168\n"
    "v_mfma_f32_16x16x32_bf16 v[148:151], a[72:75], a[148:151], v[148:151]\n"
    "ds_read_b64_tr_b16 a[30:31], v225 offset:10240\n"
    "v_mfma_f32_16x16x32_bf16 v[152:155], a[72:75], a[152:155], v[152:155]\n"
    "ds_read_b64_tr_b16 a[32:33], v224 offset:2048\n"
    "v_mfma_f32_16x16x32_bf16 v[152:155], a[76:79], a[168:171], v[152:155]\n"
    "s_add_u32 m0, m0, 960\n"
    "ds_read_b64_tr_b16 a[34:35], v224 offset:5120\n"
    "v_mfma_f32_16x16x32_bf16 v[156:159], a[76:79], a[172:175], v[156:159]\n"
    "buffer_load_dwordx4 %22, s[40:43], s53 offen offset:64 lds\n"
    "v_mfma_f32_16x16x32_bf16 v[156:159], a[72:75], a[156:159], v[156:159]\n"
    "ds_read_b64_tr_b16 a[36:37], v224 offset:8192\n"
    "v_mfma_f32_16x16x32_bf16 v[172:175], a[80:83], a[156:159], v[172:175]\n"
    "ds_read_b64_tr_b16 a[38:39], v224 offset:11264\n"
    "v_mfma_f32_16x16x32_bf16 v[172:175], a[84:87], a[172:175], v[172:175]\n"
    "ds_read_b64_tr_b16 a[40:41], v225 offset:2048\n"
    "v_mfma_f32_16x16x32_bf16 v[168:171], a[84:87], a[168:171], v[168:171]\n"
    "ds_read_b64_tr_b16 a[42:43], v225 offset:5120\n"
    "v_mfma_f32_16x16x32_bf16 v[168:171], a[80:83], a[152:155], v[168:171]\n"
    "buffer_load_dwordx4 a[136:139], %18, s[36:39], s52 offen offset:2048 sc1\n"
    "v_mfma_f32_16x16x32_bf16 v[164:167], a[80:83], a[148:151], v[164:167]\n"
    "ds_read_b64_tr_b16 a[44:45], v225 offset:8192\n"
    "v_mfma_f32_16x16x32_bf16 v[164:167], a[84:87], a[164:167], v[164:167]\n"
    "ds_read_b64_tr_b16 a[46:47], v225 offset:11264\n"
    "v_mfma_f32_16x16x32_bf16 v[160:163], a[84:87], a[160:163], v[160:163]\n"
    "ds_read_b64_tr_b16 a[48:49], v226 offset:24576\n"
    "v_mfma_f32_16x16x32_bf16 v[160:163], a[80:83], a[144:147], v[160:163]\n"
    "s_add_u32 m0, m0, 960\n"
    "ds_read_b64_tr_b16 a[50:51], v226 offset:25088\n"
    "v_mfma_f32_16x16x32_bf16 v[176:179], a[88:91], a[144:147], v[176:179]\n"
    "buffer_load_dwordx4 %22, s[40:43], s53 offen offset:128 lds\n"
    "v_mfma_f32_16x16x32_bf16 v[176:179], a[92:95], a[160:163], v[176:179]\n"
    "ds_read_b64_tr_b16 a[52:53], v226 offset:25600\n"
    "v_mfma_f32_16x16x32_bf16 v[180:183], a[92:95], a[164:167], v[180:183]\n"
    "ds_read_b64_tr_b16 a[54:55], v226 offset:26112\n"
    "v_mfma_f32_16x16x32_bf16 v[180:183], a[88:91], a[148:151], v[180:183]\n"
    "v_mfma_f32_16x16x32_bf16 v[184:187], a[88:91], a[152:155], v[184:187]\n"
    "v_mfma_f32_16x16x32_bf16 v[184:187], a[92:95], a[168:171], v[184:187]\n"
    "buffer_load_dwordx4 a[140:143], %18, s[36:39], s52 offen offset:3072 sc1\n"
    "v_mfma_f32_16x16x32_bf16 v[188:191], a[92:95], a[172:175], v[188:191]\n"
    "s_add_u32 s52, s52, 4096\n"
    "v_mfma_f32_16x16x32_bf16 v[188:191], a[88:91], a[156:159], v[188:191]\n"
    "v_mfma_f32_16x16x32_bf16 v[204:207], a[96:99], a[156:159], v[204:207]\n"
    "v_mfma_f32_16x16x32_bf16 v[204:207], a[100:103], a[172:175], v[204:207]\n"
    "s_add_u32 m0, m0, 10368\n"
    "v_mfma_f32_16x16x32_bf16 v[200:203], a[100:103], a[168:171], v[200:203]\n"
    "buffer_load_dwordx4 %22, s[44:47], s53 offen lds\n"
    "v_mfma_f32_16x16x32_bf16 v[200:203], a[96:99], a[152:155], v[200:203]\n"
    "v_mfma_f32_16x16x32_bf16 v[196:199], a[96:99], a[148:151], v[196:199]\n"
    "v_mfma_f32_16x16x32_bf16 v[196:199], a[100:103], a[164:167], v[196:199]\n"
    "v_mfma_f32_16x16x32_bf16 v[192:195], a[100:103], a[160:163], v[192:195]\n"
    "v_mfma_f32_16x16x32_bf16 v[192:195], a[96:99], a[144:147], v[192:195]\n"
    "v_mfma_f32_16x16x32_bf16 v[208:211], a[104:107], a[144:147], v[208:211]\n"
    "v_mfma_f32_16x16x32_bf16 v[208:211], a[108:111], a[160:163], v[208:211]\n"
    "v_mfma_f32_16x16x32_bf16 v[212:215], a[108:111], a[164:167], v[212:215]\n"
    "v_mfma_f32_16x16x32_bf16 v[212:215], a[104:107], a[148:151], v[212:215]\n"
    "v_mfma_f32_16x16x32_bf16 v[216:219], a[104:107], a[152:155], v[216:219]\n"
    "v_mfma_f32_16x16x32_bf16 v[216:219], a[108:111], a[168:171], v[216:219]\n"
    "v_mfma_f32_16x16x32_bf16 v[220:223], a[108:111], a[172:175], v[220:223]\n"
    "v_mfma_f32_16x16x32_bf16 v[220:223], a[104:107], a[156:159], v[220:223]\n"
    "s_waitcnt vmcnt(16) lgkmcnt(0)\n"
    "v_mfma_f32_16x16x32_bf16 v[0:3], a[0:3], a[176:179], v[0:3]\n"
    "s_sub_u32 s59, s59, 1\n"
    "ds_read_b64_tr_b16 a[56:57], v224 offset:12288\n"
    "v_mfma_f32_16x16x32_bf16 v[0:3], a[4:7], a[192:195], v[0:3]\n"
    "s_cmp_gt_i32 s59, 0\n"
    "ds_read_b64_tr_b16 a[58:59], v224 offset:15360\n"
    "v_mfma_f32_16x16x32_bf16 v[4:7], a[4:7], a[196:199], v[4:7]\n"
    "s_cselect_b32 s38, s38, 0\n"
    "ds_read_b64_tr_b16 a[60:61], v224 offset:18432\n"
    "v_mfma_f32_16x16x32_bf16 v[4:7], a[0:3], a[180:183], v[4:7]\n"
    "ds_read_b64_tr_b16 a[62:63], v224 offset:21504\n"
    "v_mfma_f32_16x16x32_bf16 v[8:11], a[0:3], a[184:187], v[8:11]\n"
    "buffer_load_dwordx4 a[144:147], %18, s[36:39], s52 offen sc1\n"
    "v_mfma_f32_16x16x32_bf16 v[8:11], a[4:7], a[200:203], v[8:11]\n"
    "ds_read_b64_tr_b16 a[64:65], v225 offset:12288\n"
    "v_mfma_f32_16x16x32_bf16 v[12:15], a[4:7], a[204:207], v[12:15]\n"
    "ds_read_b64_tr_b16 a[66:67], v225 offset:15360\n"
    "v_mfma_f32_16x16x32_bf16 v[12:15], a[0:3], a[188:191], v[12:15]\n"
    "ds_read_b64_tr_b16 a[68:69], v225 offset:18432\n"
    "v_mfma_f32_16x16x32_bf16 v[28:31], a[8:11], a[188:191], v[28:31]\n"
    "ds_read_b64_tr_b16 a[70:71], v225 offset:21504\n"
    "v_mfma_f32_16x16x32_bf16 v[28:31], a[12:15], a[204:207], v[28:31]\n"
    "s_add_u32 m0, m0, 960\n"
    "ds_read_b64_tr_b16 a[72:73], v224 offset:13312\n"
    "v_mfma_f32_16x16x32_bf16 v[24:27], a[12:15], a[200:203], v[24:27]\n"
    "buffer_load_dwordx4 %22, s[44:47], s53 offen offset:64 lds\n"
    "v_mfma_f32_16x16x32_bf16 v[24:27], a[8:11], a[184:187], v[24:27]\n"
    "ds_read_b64_tr_b16 a[74:75], v224 offset:16384\n"
    "v_mfma_f32_16x16x32_bf16 v[20:23], a[8:11], a[180:183], v[20:23]\n"
    "ds_read_b64_tr_b16 a[76:77], v224 offset:19456\n"
    "v_mfma_f32_16x16x32_bf16 v[20:23], a[12:15], a[196:199], v[20:23]\n"
    "ds_read_b64_tr_b16 a[78:79], v224 offset:22528\n"
    "v_mfma_f32_16x16x32_bf16 v[16:19], a[12:15], a[192:195], v[16:19]\n"
    "ds_read_b64_tr_b16 a[80:81], v225 offset:13312\n"
    "v_mfma_f32_16x16x32_bf16 v[16:19], a[8:11], a[176:179], v[16:19]\n"
    "ds_read_b64_tr_b16 a[82:83], v225 offset:16384\n"
    "v_mfma_f32_16x16x32_bf16 v[32:35], a[16:19], a[176:179], v[32:35]\n"
    "buffer_load_dwordx4 a[148:151], %18, s[36:39], s52 offen offset:1024 sc1\n"
    "v_mfma_f32_16x16x32_bf16 v[32:35], a[20:23], a[192:195], v[32:35]\n"
    "ds_read_b64_tr_b16 a[84:85], v225 offset:19456\n"
    "v_mfma_f32_16x16x32_bf16 v[36:39], a[20:23], a[196:199], v[36:39]\n"
    "ds_read_b64_tr_b16 a[86:87], v225 offset:22528\n"
    "v_mfma_f32_16x16x32_bf16 v[36:39], a[16:19], a[180:183], v[36:39]\n"
    "ds_read_b64_tr_b16 a[88:89], v224 offset:14336\n"
    "v_mfma_f32_16x16x32_bf16 v[40:43], a[16:19], a[184:187], v[40:43]\n"
    "ds_read_b64_tr_b16 a[90:91], v224 offset:17408\n"
    "v_mfma_f32_16x16x32_bf16 v[40:43], a[20:23], a[200:203], v[40:43]\n"
    "s_add_u32 m0, m0, 960\n"
    "ds_read_b64_tr_b16 a[92:93], v224 offset:20480\n"
    "v_mfma_f32_16x16x32_bf16 v[44:47], a[20:23], a[204:207], v[44:47]\n"
    "buffer_load_dwordx4 %22, s[44:47], s53 offen offset:128 lds\n"
    "v_mfma_f32_16x16x32_bf16 v[44:47], a[16:19], a[188:191], v[44:47]\n"
    "ds_read_b64_tr_b16 a[94:95], v224 offset:23552\n"
    "v_mfma_f32_16x16x32_bf16 v[60:63], a[24:27], a[188:191], v[60:63]\n"
    "ds_read_b64_tr_b16 a[96:97], v225 offset:14336\n"
    "v_mfma_f32_16x16x32_bf16 v[60:63], a[28:31], a[204:207], v[60:63]\n"
    "ds_read_b64_tr_b16 a[98:99], v225 offset:17408\n"
    "v_mfma_f32_16x16x32_bf16 v[56:59], a[28:31], a[200:203], v[56:59]\n"
    "ds_read_b64_tr_b16 a[100:101], v225 offset:20480\n"
    "v_mfma_f32_16x16x32_bf16 v[56:59], a[24:27], a[184:187], v[56:59]\n"
    "ds_read_b64_tr_b16 a[102:103], v225 offset:23552\n"
    "v_mfma_f32_16x16x32_bf16 v[52:55], a[24:27], a[180:183], v[52:55]\n"
    "buffer_load_dwordx4 a[152:155], %18, s[36:39], s52 offen offset:2048 sc1\n"
    "v_mfma_f32_16x16x32_bf16 v[52:55], a[28:31], a[196:199], v[52:55]\n"
    "ds_read_b64_tr_b16 a[104:105], v226 offset:26624\n"
    "v_mfma_f32_16x16x32_bf16 v[48:51], a[28:31], a[192:195], v[48:51]\n"
    "ds_read_b64_tr_b16 a[106:107], v226 offset:27136\n"
    "v_mfma_f32_16x16x32_bf16 v[48:51], a[24:27], a[176:179], v[48:51]\n"
    "ds_read_b64_tr_b16 a[108:109], v226 offset:27648\n"
    "v_mfma_f32_16x16x32_bf16 v[64:67], a[32:35], a[176:179], v[64:67]\n"
    "ds_read_b64_tr_b16 a[110:111], v226 offset:28160\n"
    "v_mfma_f32_16x16x32_bf16 v[64:67], a[36:39], a[192:195], v[64:67]\n"
    "s_add_u32 m0, s57, s61\n"
    "v_mfma_f32_16x16x32_bf16 v[68:71], a[36:39], a[196:199], v[68:71]\n"
    "buffer_load_dwordx4 %23, s[48:51], s54 offen lds\n"
    "v_mfma_f32_16x16x32_bf16 v[68:71], a[32:35], a[180:183], v[68:71]\n"
    "s_add_u32 s53, s53, 458752\n"
    "v_mfma_f32_16x16x32_bf16 v[72:75], a[32:35], a[184:187], v[72:75]\n"
    "s_add_u32 s54, s54, 458752\n"
    "v_mfma_f32_16x16x32_bf16 v[72:75], a[36:39], a[200:203], v[72:75]\n"
    "s_add_u32 s57, s57, 28672\n"
    "v_mfma_f32_16x16x32_bf16 v[76:79], a[36:39], a[204:207], v[76:79]\n"
    "s_cmp_ge_u32 s57, 159744\n"
    "v_mfma_f32_16x16x32_bf16 v[76:79], a[32:35], a[188:191], v[76:79]\n"
    "s_cselect_b32 s57, 73728, s57\n"
    "v_mfma_f32_16x16x32_bf16 v[92:95], a[40:43], a[188:191], v[92:95]\n"
    "buffer_load_dwordx4 a[156:159], %18, s[36:39], s52 offen offset:3072 sc1\n"
    "v_mfma_f32_16x16x32_bf16 v[92:95], a[44:47], a[204:207], v[92:95]\n"
    "s_add_u32 s52, s52, 4096\n"
    "v_mfma_f32_16x16x32_bf16 v[88:91], a[44:47], a[200:203], v[88:91]\n"
    "s_add_u32 s58, s58, 28672\n"
    "v_mfma_f32_16x16x32_bf16 v[88:91], a[40:43], a[184:187], v[88:91]\n"
    "s_cmp_ge_u32 s58, 159744\n"
    "v_mfma_f32_16x16x32_bf16 v[84:87], a[40:43], a[180:183], v[84:87]\n"
    "s_cselect_b32 s58, 73728, s58\n"
    "v_mfma_f32_16x16x32_bf16 v[84:87], a[44:47], a[196:199], v[84:87]\n"
    "v_add_u32 v224, s58, %19\n"
    "v_add_u32 v225, s58, %20\n"
    "v_add_u32 v226, s58, %21\n"
    "v_mfma_f32_16x16x32_bf16 v[80:83], a[44:47], a[192:195], v[80:83]\n"
    "v_mfma_f32_16x16x32_bf16 v[80:83], a[40:43], a[176:179], v[80:83]\n"
    "v_mfma_f32_16x16x32_bf16 v[96:99], a[48:51], a[176:179], v[96:99]\n"
    "v_mfma_f32_16x16x32_bf16 v[96:99], a[52:55], a[192:195], v[96:99]\n"
    "v_mfma_f32_16x16x32_bf16 v[100:103], a[52:55], a[196:199], v[100:103]\n"
    "v_mfma_f32_16x16x32_bf16 v[100:103], a[48:51], a[180:183], v[100:103]\n"
    "v_mfma_f32_16x16x32_bf16 v[104:107], a[48:51], a[184:187], v[104:107]\n"
    "v_mfma_f32_16x16x32_bf16 v[104:107], a[52:55], a[200:203], v[104:107]\n"
    "v_mfma_f32_16x16x32_bf16 v[108:111], a[52:55], a[204:207], v[108:111]\n"
    "v_mfma_f32_16x16x32_bf16 v[108:111], a[48:51], a[188:191], v[108:111]\n"
    "s_waitcnt vmcnt(16) lgkmcnt(0)\n"
    "v_mfma_f32_16x16x32_bf16 v[112:115], a[56:59], a[176:179], v[112:115]\n"
    "s_barrier\n"
    "v_mfma_f32_16x16x32_bf16 v[112:115], a[60:63], a[192:195], v[112:115]\n"
    "s_sub_u32 s60, s60, 1\n"
    "ds_read_b64_tr_b16 a[0:1], v224 offset:0\n"
    "v_mfma_f32_16x16x32_bf16 v[116:119], a[60:63], a[196:199], v[116:119]\n"
    "s_cmp_gt_i32 s60, 0\n"
    "ds_read_b64_tr_b16 a[2:3], v224 offset:3072\n"
    "v_mfma_f32_16x16x32_bf16 v[116:119], a[56:59], a[180:183], v[116:119]\n"
    "s_cselect_b32 s42, s42, 0\n"
    "ds_read_b64_tr_b16 a[4:5], v224 offset:6144\n"
    "v_mfma_f32_16x16x32_bf16 v[120:123], a[56:59], a[184:187], v[120:123]\n"
    "s_cselect_b32 s46, s46, 0\n"
    "ds_read_b64_tr_b16 a[6:7], v224 offset:9216\n"
    "v_mfma_f32_16x16x32_bf16 v[120:123], a[60:63], a[200:203], v[120:123]\n"
    "s_cselect_b32 s50, s50, 0\n"
    "ds_read_b64_tr_b16 a[8:9], v225 offset:0\n"
    "v_mfma_f32_16x16x32_bf16 v[124:127], a[60:63], a[204:207], v[124:127]\n"
    "ds_read_b64_tr_b16 a[10:11], v225 offset:3072\n"
    "v_mfma_f32_16x16x32_bf16 v[124:127], a[56:59], a[188:191], v[124:127]\n"
    "buffer_load_dwordx4 a[160:163], %18, s[36:39], s52 offen sc1\n"
    "v_mfma_f32_16x16x32_bf16 v[140:143], a[64:67], a[188:191], v[140:143]\n"
    "ds_read_b64_tr_b16 a[12:13], v225 offset:6144\n"
    "v_mfma_f32_16x16x32_bf16 v[140:143], a[68:71], a[204:207], v[140:143]\n"
    "ds_read_b64_tr_b16 a[14:15], v225 offset:9216\n"
    "v_mfma_f32_16x16x32_bf16 v[136:139], a[68:71], a[200:203], v[136:139]\n"
    "ds_read_b64_tr_b16 a[16:17], v224 offset:1024\n"
    "v_mfma_f32_16x16x32_bf16 v[136:139], a[64:67], a[184:187], v[136:139]\n"
    "s_add_u32 m0, s57, s56\n"
    "ds_read_b64_tr_b16 a[18:19], v224 offset:4096\n"
    "v_mfma_f32_16x16x32_bf16 v[132:135], a[64:67], a[180:183], v[132:135]\n"
    "buffer_load_dwordx4 %22, s[40:43], s53 offen lds\n"
    "v_mfma_f32_16x16x32_bf16 v[132:135], a[68:71], a[196:199], v[132:135]\n"
    "ds_read_b64_tr_b16 a[20:21], v224 offset:7168\n"
    "v_mfma_f32_16x16x32_bf16 v[128:131], a[68:71], a[192:195], v[128:131]\n"
    "ds_read_b64_tr_b16 a[22:23], v224 offset:10240\n"
    "v_mfma_f32_16x16x32_bf16 v[128:131], a[64:67], a[176:179], v[128:131]\n"
    "ds_read_b64_tr_b16 a[24:25], v225 offset:1024\n"
    "v_mfma_f32_16x16x32_bf16 v[144:147], a[72:75], a[176:179], v[144:147]\n"
    "ds_read_b64_tr_b16 a[26:27], v225 offset:4096\n"
    "v_mfma_f32_16x16x32_bf16 v[144:147], a[76:79], a[192:195], v[144:147]\n"
    "buffer_load_dwordx4 a[164:167], %18, s[36:39], s52 offen offset:1024 sc1\n"
    "v_mfma_f32_16x16x32_bf16 v[148:151], a[76:79], a[196:199], v[148:151]\n"
    "ds_read_b64_tr_b16 a[28:29], v225 offset:7168\n"
    "v_mfma_f32_16x16x32_bf16 v[148:151], a[72:75], a[180:183], v[148:151]\n"
    "ds_read_b64_tr_b16 a[30:31], v225 offset:10240\n"
    "v_mfma_f32_16x16x32_bf16 v[152:155], a[72:75], a[184:187], v[152:155]\n"
    "ds_read_b64_tr_b16 a[32:33], v224 offset:2048\n"
    "v_mfma_f32_16x16x32_bf16 v[152:155], a[76:79], a[200:203], v[152:155]\n"
    "s_add_u32 m0, m0, 960\n"
    "ds_read_b64_tr_b16 a[34:35], v224 offset:5120\n"
    "v_mfma_f32_16x16x32_bf16 v[156:159], a[76:79], a[204:207], v[156:159]\n"
    "buffer_load_dwordx4 %22, s[40:43], s53 offen offset:64 lds\n"
    "v_mfma_f32_16x16x32_bf16 v[156:159], a[72:75], a[188:191], v[156:159]\n"
    "ds_read_b64_tr_b16 a[36:37], v224 offset:8192\n"
    "v_mfma_f32_16x16x32_bf16 v[172:175], a[80:83], a[188:191], v[172:175]\n"
    "ds_read_b64_tr_b16 a[38:39], v224 offset:11264\n"
    "v_mfma_f32_16x16x32_bf16 v[172:175], a[84:87], a[204:207], v[172:175]\n"
    "ds_read_b64_tr_b16 a[40:41], v225 offset:2048\n"
    "v_mfma_f32_16x16x32_bf16 v[168:171], a[84:87], a[200:203], v[168:171]\n"
    "ds_read_b64_tr_b16 a[42:43], v225 offset:5120\n"
    "v_mfma_f32_16x16x32_bf16 v[168:171], a[80:83], a[184:187], v[168:171]\n"
    "buffer_load_dwordx4 a[168:171], %18, s[36:39], s52 offen offset:2048 sc1\n"
    "v_mfma_f32_16x16x32_bf16 v[164:167], a[80:83], a[180:183], v[164:167]\n"
    "ds_read_b64_tr_b16 a[44:45], v225 offset:8192\n"
    "v_mfma_f32_16x16x32_bf16 v[164:167], a[84:87], a[196:199], v[164:167]\n"
    "ds_read_b64_tr_b16 a[46:47], v225 offset:11264\n"
    "v_mfma_f32_16x16x32_bf16 v[160:163], a[84:87], a[192:195], v[160:163]\n"
    "ds_read_b64_tr_b16 a[48:49], v226 offset:24576\n"
    "v_mfma_f32_16x16x32_bf16 v[160:163], a[80:83], a[176:179], v[160:163]\n"
    "s_add_u32 m0, m0, 960\n"
    "ds_read_b64_tr_b16 a[50:51], v226 offset:25088\n"
    "v_mfma_f32_16x16x32_bf16 v[176:179], a[88:91], a[176:179], v[176:179]\n"
    "buffer_load_dwordx4 %22, s[40:43], s53 offen offset:128 lds\n"
    "v_mfma_f32_16x16x32_bf16 v[176:179], a[92:95], a[192:195], v[176:179]\n"
    "ds_read_b64_tr_b16 a[52:53], v226 offset:25600\n"
    "v_mfma_f32_16x16x32_bf16 v[180:183], a[92:95], a[196:199], v[180:183]\n"
    "ds_read_b64_tr_b16 a[54:55], v226 offset:26112\n"
    "v_mfma_f32_16x16x32_bf16 v[180:183], a[88:91], a[180:183], v[180:183]\n"
    "v_mfma_f32_16x16x32_bf16 v[184:187], a[88:91], a[184:187], v[184:187]\n"
    "v_mfma_f32_16x16x32_bf16 v[184:187], a[92:95], a[200:203], v[184:187]\n"
    "buffer_load_dwordx4 a[172:175], %18, s[36:39], s52 offen offset:3072 sc1\n"
    "v_mfma_f32_16x16x32_bf16 v[188:191], a[92:95], a[204:207], v[188:191]\n"
    "s_add_u32 s52, s52, 4096\n"
    "v_mfma_f32_16x16x32_bf16 v[188:191], a[88:91], a[188:191], v[188:191]\n"
    "v_mfma_f32_16x16x32_bf16 v[204:207], a[96:99], a[188:191], v[204:207]\n"
    "v_mfma_f32_16x16x32_bf16 v[204:207], a[100:103], a[204:207], v[204:207]\n"
    "s_add_u32 m0, m0, 10368\n"
    "v_mfma_f32_16x16x32_bf16 v[200:203], a[100:103], a[200:203], v[200:203]\n"
    "buffer_load_dwordx4 %22, s[44:47], s53 offen lds\n"
    "v_mfma_f32_16x16x32_bf16 v[200:203], a[96:99], a[184:187], v[200:203]\n"
    "v_mfma_f32_16x16x32_bf16 v[196:199], a[96:99], a[180:183], v[196:199]\n"
    "v_mfma_f32_16x16x32_bf16 v[196:199], a[100:103], a[196:199], v[196:199]\n"
    "v_mfma_f32_16x16x32_bf16 v[192:195], a[100:103], a[192:195], v[192:195]\n"
    "v_mfma_f32_16x16x32_bf16 v[192:195], a[96:99], a[176:179], v[192:195]\n"
    "v_mfma_f32_16x16x32_bf16 v[208:211], a[104:107], a[176:179], v[208:211]\n"
    "v_mfma_f32_16x16x32_bf16 v[208:211], a[108:111], a[192:195], v[208:211]\n"
    "v_mfma_f32_16x16x32_bf16 v[212:215], a[108:111], a[196:199], v[212:215]\n"
    "v_mfma_f32_16x16x32_bf16 v[212:215], a[104:107], a[180:183], v[212:215]\n"
    "v_mfma_f32_16x16x32_bf16 v[216:219], a[104:107], a[184:187], v[216:219]\n"
    "v_mfma_f32_16x16x32_bf16 v[216:219], a[108:111], a[200:203], v[216:219]\n"
    "v_mfma_f32_16x16x32_bf16 v[220:223], a[108:111], a[204:207], v[220:223]\n"
    "v_mfma_f32_16x16x32_bf16 v[220:223], a[104:107], a[188:191], v[220:223]\n"
    "s_waitcnt vmcnt(16) lgkmcnt(0)\n"
    "s_sub_u32 s55, s55, 1\n"
    "s_cmp_lg_u32 s55, 0\n"
    "s_cbranch_scc1 L_k4_loop\n"
    "v_mfma_f32_16x16x32_bf16 v[0:3], a[0:3], a[112:115], v[0:3]\n"
    "s_sub_u32 s59, s59, 1\n"
    "ds_read_b64_tr_b16 a[56:57], v224 offset:12288\n"
    "v_mfma_f32_16x16x32_bf16 v[0:3], a[4:7], a[128:131], v[0:3]\n"
    "s_cmp_gt_i32 s59, 0\n"
    "ds_read_b64_tr_b16 a[58:59], v224 offset:15360\n"
    "v_mfma_f32_16x16x32_bf16 v[4:7], a[4:7], a[132:135], v[4:7]\n"
    "s_cselect_b32 s38, s38, 0\n"
    "ds_read_b64_tr_b16 a[60:61], v224 offset:18432\n"
    "v_mfma_f32_16x16x32_bf16 v[4:7], a[0:3], a[116:119], v[4:7]\n"
    "ds_read_b64_tr_b16 a[62:63], v224 offset:21504\n"
    "v_mfma_f32_16x16x32_bf16 v[8:11], a[0:3], a[120:123], v[8:11]\n"
    "buffer_load_dwordx4 a[176:179], %18, s[36:39], s52 offen sc1\n"
    "v_mfma_f32_16x16x32_bf16 v[8:11], a[4:7], a[136:139], v[8:11]\n"
    "ds_read_b64_tr_b16 a[64:65], v225 offset:12288\n"
    "v_mfma_f32_16x16x32_bf16 v[12:15], a[4:7], a[140:143], v[12:15]\n"
    "ds_read_b64_tr_b16 a[66:67], v225 offset:15360\n"
    "v_mfma_f32_16x16x32_bf16 v[12:15], a[0:3], a[124:127], v[12:15]\n"
    "ds_read_b64_tr_b16 a[68:69], v225 offset:18432\n"
    "v_mfma_f32_16x16x32_bf16 v[28:31], a[8:11], a[124:127], v[28:31]\n"
    "ds_read_b64_tr_b16 a[70:71], v225 offset:21504\n"
    "v_mfma_f32_16x16x32_bf16 v[28:31], a[12:15], a[140:143], v[28:31]\n"
    "s_add_u32 m0, m0, 960\n"
    "ds_read_b64_tr_b16 a[72:73], v224 offset:13312\n"
    "v_mfma_f32_16x16x32_bf16 v[24:27], a[12:15], a[136:139], v[24:27]\n"
    "buffer_load_dwordx4 %22, s[44:47], s53 offen offset:64 lds\n"
    "v_mfma_f32_16x16x32_bf16 v[24:27], a[8:11], a[120:123], v[24:27]\n"
    "ds_read_b64_tr_b16 a[74:75], v224 offset:16384\n"
    "v_mfma_f32_16x16x32_bf16 v[20:23], a[8:11], a[116:119], v[20:23]\n"
    "ds_read_b64_tr_b16 a[76:77], v224 offset:19456\n"
    "v_mfma_f32_16x16x32_bf16 v[20:23], a[12:15], a[132:135], v[20:23]\n"
    "ds_read_b64_tr_b16 a[78:79], v224 offset:22528\n"
    "v_mfma_f32_16x16x32_bf16 v[16:19], a[12:15], a[128:131], v[16:19]\n"
    "ds_read_b64_tr_b16 a[80:81], v225 offset:13312\n"
    "v_mfma_f32_16x16x32_bf16 v[16:19], a[8:11], a[112:115], v[16:19]\n"
    "ds_read_b64_tr_b16 a[82:83], v225 offset:16384\n"
    "v_mfma_f32_16x16x32_bf16 v[32:35], a[16:19], a[112:115], v[32:35]\n"
    "buffer_load_dwordx4 a[180:183], %18, s[36:39], s52 offen offset:1024 sc1\n"
    "v_mfma_f32_16x16x32_bf16 v[32:35], a[20:23], a[128:131], v[32:35]\n"
    "ds_read_b64_tr_b16 a[84:85], v225 offset:19456\n"
    "v_mfma_f32_16x16x32_bf16 v[36:39], a[20:23], a[132:135], v[36:39]\n"
    "ds_read_b64_tr_b16 a[86:87], v225 offset:22528\n"
    "v_mfma_f32_16x16x32_bf16 v[36:39], a[16:19], a[116:119], v[36:39]\n"
    "ds_read_b64_tr_b16 a[88:89], v224 offset:14336\n"
    "v_mfma_f32_16x16x32_bf16 v[40:43], a[16:19], a[120:123], v[40:43]\n"
    "ds_read_b64_tr_b16 a[90:91], v224 offset:17408\n"
    "v_mfma_f32_16x16x32_bf16 v[40:43], a[20:23], a[136:139], v[40:43]\n"
    "s_add_u32 m0, m0, 960\n"
    "ds_read_b64_tr_b16 a[92:93], v224 offset:20480\n"
    "v_mfma_f32_16x16x32_bf16 v[44:47], a[20:23], a[140:143], v[44:47]\n"
    "buffer_load_dwordx4 %22, s[44:47], s53 offen offset:128 lds\n"
    "v_mfma_f32_16x16x32_bf16 v[44:47], a[16:19], a[124:127], v[44:47]\n"
    "ds_read_b64_tr_b16 a[94:95], v224 offset:23552\n"
    "v_mfma_f32_16x16x32_bf16 v[60:63], a[24:27], a[124:127], v[60:63]\n"
    "ds_read_b64_tr_b16 a[96:97], v225 offset:14336\n"
    "v_mfma_f32_16x16x32_bf16 v[60:63], a[28:31], a[140:143], v[60:63]\n"
    "ds_read_b64_tr_b16 a[98:99], v225 offset:17408\n"
    "v_mfma_f32_16x16x32_bf16 v[56:59], a[28:31], a[136:139], v[56:59]\n"
    "ds_read_b64_tr_b16 a[100:101], v225 offset:20480\n"
    "v_mfma_f32_16x16x32_bf16 v[56:59], a[24:27], a[120:123], v[56:59]\n"
    "ds_read_b64_tr_b16 a[102:103], v225 offset:23552\n"
    "v_mfma_f32_16x16x32_bf16 v[52:55], a[24:27], a[116:119], v[52:55]\n"
    "buffer_load_dwordx4 a[184:187], %18, s[36:39], s52 offen offset:2048 sc1\n"
    "v_mfma_f32_16x16x32_bf16 v[52:55], a[28:31], a[132:135], v[52:55]\n"
    "ds_read_b64_tr_b16 a[104:105], v226 offset:26624\n"
    "v_mfma_f32_16x16x32_bf16 v[48:51], a[28:31], a[128:131], v[48:51]\n"
    "ds_read_b64_tr_b16 a[106:107], v226 offset:27136\n"
    "v_mfma_f32_16x16x32_bf16 v[48:51], a[24:27], a[112:115], v[48:51]\n"
    "ds_read_b64_tr_b16 a[108:109], v226 offset:27648\n"
    "v_mfma_f32_16x16x32_bf16 v[64:67], a[32:35], a[112:115], v[64:67]\n"
    "ds_read_b64_tr_b16 a[110:111], v226 offset:28160\n"
    "v_mfma_f32_16x16x32_bf16 v[64:67], a[36:39], a[128:131], v[64:67]\n"
    "s_add_u32 m0, s57, s61\n"
    "v_mfma_f32_16x16x32_bf16 v[68:71], a[36:39], a[132:135], v[68:71]\n"
    "buffer_load_dwordx4 %23, s[48:51], s54 offen lds\n"
    "v_mfma_f32_16x16x32_bf16 v[68:71], a[32:35], a[116:119], v[68:71]\n"
    "s_add_u32 s53, s53, 458752\n"
    "v_mfma_f32_16x16x32_bf16 v[72:75], a[32:35], a[120:123], v[72:75]\n"
    "s_add_u32 s54, s54, 458752\n"
    "v_mfma_f32_16x16x32_bf16 v[72:75], a[36:39], a[136:139], v[72:75]\n"
    "s_add_u32 s57, s57, 28672\n"
    "v_mfma_f32_16x16x32_bf16 v[76:79], a[36:39], a[140:143], v[76:79]\n"
    "s_cmp_ge_u32 s57, 159744\n"
    "v_mfma_f32_16x16x32_bf16 v[76:79], a[32:35], a[124:127], v[76:79]\n"
    "s_cselect_b32 s57, 73728, s57\n"
    "v_mfma_f32_16x16x32_bf16 v[92:95], a[40:43], a[124:127], v[92:95]\n"
    "buffer_load_dwordx4 a[188:191], %18, s[36:39], s52 offen offset:3072 sc1\n"
    "v_mfma_f32_16x16x32_bf16 v[92:95], a[44:47], a[140:143], v[92:95]\n"
    "s_add_u32 s52, s52, 4096\n"
    "v_mfma_f32_16x16x32_bf16 v[88:91], a[44:47], a[136:139], v[88:91]\n"
    "s_add_u32 s58, s58, 28672\n"
    "v_mfma_f32_16x16x32_bf16 v[88:91], a[40:43], a[120:123], v[88:91]\n"
    "s_cmp_ge_u32 s58, 159744\n"
    "v_mfma_f32_16x16x32_bf16 v[84:87], a[40:43], a[116:119], v[84:87]\n"
    "s_cselect_b32 s58, 73728, s58\n"
    "v_mfma_f32_16x16x32_bf16 v[84:87], a[44:47], a[132:135], v[84:87]\n"
    "v_add_u32 v224, s58, %19\n"
    "v_add_u32 v225, s58, %20\n"
    "v_add_u32 v226, s58, %21\n"
    "v_mfma_f32_16x16x32_bf16 v[80:83], a[44:47], a[128:131], v[80:83]\n"
    "v_mfma_f32_16x16x32_bf16 v[80:83], a[40:43], a[112:115], v[80:83]\n"
    "v_mfma_f32_16x16x32_bf16 v[96:99], a[48:51], a[112:115], v[96:99]\n"
    "v_mfma_f32_16x16x32_bf16 v[96:99], a[52:55], a[128:131], v[96:99]\n"
    "v_mfma_f32_16x16x32_bf16 v[100:103], a[52:55], a[132:135], v[100:103]\n"
    "v_mfma_f32_16x16x32_bf16 v[100:103], a[48:51], a[116:119], v[100:103]\n"
    "v_mfma_f32_16x16x32_bf16 v[104:107], a[48:51], a[120:123], v[104:107]\n"
    "v_mfma_f32_16x16x32_bf16 v[104:107], a[52:55], a[136:139], v[104:107]\n"
    "v_mfma_f32_16x16x32_bf16 v[108:111], a[52:55], a[140:143], v[108:111]\n"
    "v_mfma_f32_16x16x32_bf16 v[108:111], a[48:51], a[124:127], v[108:111]\n"
    "s_waitcnt vmcnt(16) lgkmcnt(0)\n"
    "v_mfma_f32_16x16x32_bf16 v[112:115], a[56:59], a[112:115], v[112:115]\n"
    "s_barrier\n"
    "v_mfma_f32_16x16x32_bf16 v[112:115], a[60:63], a[128:131], v[112:115]\n"
    "s_sub_u32 s60, s60, 1\n"
    "ds_read_b64_tr_b16 a[0:1], v224 offset:0\n"
    "v_mfma_f32_16x16x32_bf16 v[116:119], a[60:63], a[132:135], v[116:119]\n"
    "s_cmp_gt_i32 s60, 0\n"
    "ds_read_b64_tr_b16 a[2:3], v224 offset:3072\n"
    "v_mfma_f32_16x16x32_bf16 v[116:119], a[56:59], a[116:119], v[116:119]\n"
    "s_cselect_b32 s42, s42, 0\n"
    "ds_read_b64_tr_b16 a[4:5], v224 offset:6144\n"
    "v_mfma_f32_16x16x32_bf16 v[120:123], a[56:59], a[120:123], v[120:123]\n"
    "s_cselect_b32 s46, s46, 0\n"
    "ds_read_b64_tr_b16 a[6:7], v224 offset:9216\n"
    "v_mfma_f32_16x16x32_bf16 v[120:123], a[60:63], a[136:139], v[120:123]\n"
    "s_cselect_b32 s50, s50, 0\n"
    "ds_read_b64_tr_b16 a[8:9], v225 offset:0\n"
    "v_mfma_f32_16x16x32_bf16 v[124:127], a[60:63], a[140:143], v[124:127]\n"
    "ds_read_b64_tr_b16 a[10:11], v225 offset:3072\n"
    "v_mfma_f32_16x16x32_bf16 v[124:127], a[56:59], a[124:127], v[124:127]\n"
    "buffer_load_dwordx4 a[192:195], %18, s[36:39], s52 offen sc1\n"
    "v_mfma_f32_16x16x32_bf16 v[140:143], a[64:67], a[124:127], v[140:143]\n"
    "ds_read_b64_tr_b16 a[12:13], v225 offset:6144\n"
    "v_mfma_f32_16x16x32_bf16 v[140:143], a[68:71], a[140:143], v[140:143]\n"
    "ds_read_b64_tr_b16 a[14:15], v225 offset:9216\n"
    "v_mfma_f32_16x16x32_bf16 v[136:139], a[68:71], a[136:139], v[136:139]\n"
    "ds_read_b64_tr_b16 a[16:17], v224 offset:1024\n"
    "v_mfma_f32_16x16x32_bf16 v[136:139], a[64:67], a[120:123], v[136:139]\n"
    "s_add_u32 m0, s57, s56\n"
    "ds_read_b64_tr_b16 a[18:19], v224 offset:4096\n"
    "v_mfma_f32_16x16x32_bf16 v[132:135], a[64:67], a[116:119], v[132:135]\n"
    "buffer_load_dwordx4 %22, s[40:43], s53 offen lds\n"
    "v_mfma_f32_16x16x32_bf16 v[132:135], a[68:71], a[132:135], v[132:135]\n"
    "ds_read_b64_tr_b16 a[20:21], v224 offset:7168\n"
    "v_mfma_f32_16x16x32_bf16 v[128:131], a[68:71], a[128:131], v[128:131]\n"
    "ds_read_b64_tr_b16 a[22:23], v224 offset:10240\n"
    "v_mfma_f32_16x16x32_bf16 v[128:131], a[64:67], a[112:115], v[128:131]\n"
    "ds_read_b64_tr_b16 a[24:25], v225 offset:1024\n"
    "v_mfma_f32_16x16x32_bf16 v[144:147], a[72:75], a[112:115], v[144:147]\n"
    "ds_read_b64_tr_b16 a[26:27], v225 offset:4096\n"
    "v_mfma_f32_16x16x32_bf16 v[144:147], a[76:79], a[128:131], v[144:147]\n"
    "buffer_load_dwordx4 a[196:199], %18, s[36:39], s52 offen offset:1024 sc1\n"
    "v_mfma_f32_16x16x32_bf16 v[148:151], a[76:79], a[132:135], v[148:151]\n"
    "ds_read_b64_tr_b16 a[28:29], v225 offset:7168\n"
    "v_mfma_f32_16x16x32_bf16 v[148:151], a[72:75], a[116:119], v[148:151]\n"
    "ds_read_b64_tr_b16 a[30:31], v225 offset:10240\n"
    "v_mfma_f32_16x16x32_bf16 v[152:155], a[72:75], a[120:123], v[152:155]\n"
    "ds_read_b64_tr_b16 a[32:33], v224 offset:2048\n"
    "v_mfma_f32_16x16x32_bf16 v[152:155], a[76:79], a[136:139], v[152:155]\n"
    "s_add_u32 m0, m0, 960\n"
    "ds_read_b64_tr_b16 a[34:35], v224 offset:5120\n"
    "v_mfma_f32_16x16x32_bf16 v[156:159], a[76:79], a[140:143], v[156:159]\n"
    "buffer_load_dwordx4 %22, s[40:43], s53 offen offset:64 lds\n"
    "v_mfma_f32_16x16x32_bf16 v[156:159], a[72:75], a[124:127], v[156:159]\n"
    "ds_read_b64_tr_b16 a[36:37], v224 offset:8192\n"
    "v_mfma_f32_16x16x32_bf16 v[172:175], a[80:83], a[124:127], v[172:175]\n"
    "ds_read_b64_tr_b16 a[38:39], v224 offset:11264\n"
    "v_mfma_f32_16x16x32_bf16 v[172:175], a[84:87], a[140:143], v[172:175]\n"
    "ds_read_b64_tr_b16 a[40:41], v225 offset:2048\n"
    "v_mfma_f32_16x16x32_bf16 v[168:171], a[84:87], a[136:139], v[168:171]\n"
    "ds_read_b64_tr_b16 a[42:43], v225 offset:5120\n"
    "v_mfma_f32_16x16x32_bf16 v[168:171], a[80:83], a[120:123], v[168:171]\n"
    "buffer_load_dwordx4 a[200:203], %18, s[36:39], s52 offen offset:2048 sc1\n"
    "v_mfma_f32_16x16x32_bf16 v[164:167], a[80:83], a[116:119], v[164:167]\n"
    "ds_read_b64_tr_b16 a[44:45], v225 offset:8192\n"
    "v_mfma_f32_16x16x32_bf16 v[164:167], a[84:87], a[132:135], v[164:167]\n"
    "ds_read_b64_tr_b16 a[46:47], v225 offset:11264\n"
    "v_mfma_f32_16x16x32_bf16 v[160:163], a[84:87], a[128:131], v[160:163]\n"
    "ds_read_b64_tr_b16 a[48:49], v226 offset:24576\n"
    "v_mfma_f32_16x16x32_bf16 v[160:163], a[80:83], a[112:115], v[160:163]\n"
    "s_add_u32 m0, m0, 960\n"
    "ds_read_b64_tr_b16 a[50:51], v226 offset:25088\n"
    "v_mfma_f32_16x16x32_bf16 v[176:179], a[88:91], a[112:115], v[176:179]\n"
    "buffer_load_dwordx4 %22, s[40:43], s53 offen offset:128 lds\n"
    "v_mfma_f32_16x16x32_bf16 v[176:179], a[92:95], a[128:131], v[176:179]\n"
    "ds_read_b64_tr_b16 a[52:53], v226 offset:25600\n"
    "v_mfma_f32_16x16x32_bf16 v[180:183], a[92:95], a[132:135], v[180:183]\n"
    "ds_read_b64_tr_b16 a[54:55], v226 offset:26112\n"
    "v_mfma_f32_16x16x32_bf16 v[180:183], a[88:91], a[116:119], v[180:183]\n"
    "v_mfma_f32_16x16x32_bf16 v[184:187], a[88:91], a[120:123], v[184:187]\n"
    "v_mfma_f32_16x16x32_bf16 v[184:187], a[92:95], a[136:139], v[184:187]\n"
    "buffer_load_dwordx4 a[204:207], %18, s[36:39], s52 offen offset:3072 sc1\n"
    "v_mfma_f32_16x16x32_bf16 v[188:191], a[92:95], a[140:143], v[188:191]\n"
    "s_add_u32 s52, s52, 4096\n"
    "v_mfma_f32_16x16x32_bf16 v[188:191], a[88:91], a[124:127], v[188:191]\n"
    "v_mfma_f32_16x16x32_bf16 v[204:207], a[96:99], a[124:127], v[204:207]\n"
    "v_mfma_f32_16x16x32_bf16 v[204:207], a[100:103], a[140:143], v[204:207]\n"
    "s_add_u32 m0, m0, 10368\n"
    "v_mfma_f32_16x16x32_bf16 v[200:203], a[100:103], a[136:139], v[200:203]\n"
    "buffer_load_dwordx4 %22, s[44:47], s53 offen lds\n"
    "v_mfma_f32_16x16x32_bf16 v[200:203], a[96:99], a[120:123], v[200:203]\n"
    "v_mfma_f32_16x16x32_bf16 v[196:199], a[96:99], a[116:119], v[196:199]\n"
    "v_mfma_f32_16x16x32_bf16 v[196:199], a[100:103], a[132:135], v[196:199]\n"
    "v_mfma_f32_16x16x32_bf16 v[192:195], a[100:103], a[128:131], v[192:195]\n"
    "v_mfma_f32_16x16x32_bf16 v[192:195], a[96:99], a[112:115], v[192:195]\n"
    "v_mfma_f32_16x16x32_bf16 v[208:211], a[104:107], a[112:115], v[208:211]\n"
    "v_mfma_f32_16x16x32_bf16 v[208:211], a[108:111], a[128:131], v[208:211]\n"
    "v_mfma_f32_16x16x32_bf16 v[212:215], a[108:111], a[132:135], v[212:215]\n"
    "v_mfma_f32_16x16x32_bf16 v[212:215], a[104:107], a[116:119], v[212:215]\n"
    "v_mfma_f32_16x16x32_bf16 v[216:219], a[104:107], a[120:123], v[216:219]\n"
    "v_mfma_f32_16x16x32_bf16 v[216:219], a[108:111], a[136:139], v[216:219]\n"
    "v_mfma_f32_16x16x32_bf16 v[220:223], a[108:111], a[140:143], v[220:223]\n"
    "v_mfma_f32_16x16x32_bf16 v[220:223], a[104:107], a[124:127], v[220:223]\n"
    "s_waitcnt vmcnt(15) lgkmcnt(0)\n"
    "s_waitcnt vmcnt(0)\n"
    "s_nop 7\n"
    "s_nop 7\n"
      : "=&{v[0:31]}"(a0), "=&{v[32:63]}"(a1), "=&{v[64:95]}"(a2), "=&{v[96:127]}"(a3), "=&{v[128:159]}"(a4), "=&{v[160:191]}"(a5), "=&{v[192:223]}"(a6)
      : "s"(ws+WS_H2), "s"(M_*2048u), "s"(Wg), "s"(Wu), "s"(Wl), "s"(1024u*7168u), "s"(offX), "s"(offW), "s"(offL), "s"(wdst), "s"(ldst),
        "v"(vXl), "v"(vWr0), "v"(vWr1), "v"(vWr2), "v"(vWd0), "v"(vWd1), "s"(cofs), "s"(CNT(rt4,C_H2+(ct4&3)))
      : "memory", "vcc", "scc", "m0", "s36", "s37", "s38", "s39", "s40", "s41", "s42", "s43", "s44", "s45", "s46", "s47", "s48", "s49", "s50", "s51", "s52", "s53", "s54", "s55", "s56", "s57", "s58", "s59", "s60", "s61", "s62", "s63", "s64", "s65", "s66", "s67", "s68", "s69", "s70", "s71", "s72", "s73", "s74", "s75", "s76", "s77", "s78", "s79", "s80", "s81", "s82", "s83", "s84", "s85", "s86", "s87", "s88", "s89", "s90", "s91", "s92", "s93", "s94", "s95", "s96", "s97", "s98", "s99", "v224", "v225", "v226", "v227", "a0", "a1", "a2", "a3", "a4", "a5", "a6", "a7", "a8", "a9", "a10", "a11", "a12", "a13", "a14", "a15", "a16", "a17", "a18", "a19", "a20", "a21", "a22", "a23", "a24", "a25", "a26", "a27", "a28", "a29", "a30", "a31", "a32", "a33", "a34", "a35", "a36", "a37", "a38", "a39", "a40", "a41", "a42", "a43", "a44", "a45", "a46", "a47", "a48", "a49", "a50", "a51", "a52", "a53", "a54", "a55", "a56", "a57", "a58", "a59", "a60", "a61", "a62", "a63", "a64", "a65", "a66", "a67", "a68", "a69", "a70", "a71", "a72", "a73", "a74", "a75", "a76", "a77", "a78", "a79", "a80", "a81", "a82", "a83", "a84", "a85", "a86", "a87", "a88", "a89", "a90", "a91", "a92", "a93", "a94", "a95", "a96", "a97", "a98", "a99", "a100", "a101", "a102", "a103", "a104", "a105", "a106", "a107", "a108", "a109", "a110", "a111", "a112", "a113", "a114", "a115", "a116", "a117", "a118", "a119", "a120", "a121", "a122", "a123", "a124", "a125", "a126", "a127", "a128", "a129", "a130", "a131", "a132", "a133", "a134", "a135", "a136", "a137", "a138", "a139", "a140", "a141", "a142", "a143", "a144", "a145", "a146", "a147", "a148", "a149", "a150", "a151", "a152", "a153", "a154", "a155", "a156", "a157", "a158", "a159", "a160", "a161", "a162", "a163", "a164", "a165", "a166", "a167", "a168", "a169", "a170", "a171", "a172", "a173", "a174", "a175", "a176", "a177", "a178", "a179", "a180", "a181", "a182", "a183", "a184", "a185", "a186", "a187", "a188", "a189", "a190", "a191", "a192", "a193", "a194", "a195", "a196", "a197", "a198", "a199", "a200", "a201", "a202", "a203", "a204", "a205", "a206", "a207");
   { PHASE() LANE() (void)g;(void)i16;(void)q;(void)p;(void)vXl; const unsigned T=rt4*4+w;

    if(w==0) PRE_W128(Wd,3584u*2048u,sp5*896)
    const unsigned kp0=(7*ct4+odd4)>>1, kl=odd4?((7*ct4-1)>>1):((7*ct4+6)>>1);
    const __amdgpu_buffer_rsrc_t rA=mkrsrc(ws+WS_ACT,M_*7168u); const unsigned ob=(T*112*4*64+lane)*16;
    #pragma unroll
    for(int tb=0;tb<4;tb++){
#define GL2(jj,e) gelu_mul2((f32x2){ACC7(((jj)*4+tb)*4+2*(e)),ACC7(((jj)*4+tb)*4+2*(e)+1)}, (f32x2){ACC7(((7+(jj))*4+tb)*4+2*(e)),ACC7(((7+(jj))*4+tb)*4+2*(e)+1)})
      #pragma unroll
      for(int m=0;m<3;m++){ st16_sc1(rA, ob+((kp0+m)*4+tb)*1024, (u32x4){GL2(2*m,0),GL2(2*m,1),GL2(2*m+1,0),GL2(2*m+1,1)}); SB_(); }
      st8_sc1(rA, ob+(kl*4+tb)*1024+odd4*8, (u32x2){GL2(6,0),GL2(6,1)}); SB_();
    }
    DRAIN_BAR(); if(tid_<2) __hip_atomic_fetch_add((gu32*)(tid_?CNT(rt,C_X1+sp):CNT(rt4,C_A+(ct4>>3))), 1u, __ATOMIC_RELAXED, __HIP_MEMORY_SCOPE_AGENT);

   }
  }
  { PHASE() LANE() W128_RD() W128_DMA(2048u)
   { const unsigned rt=rt5, sp=sp5, To=rt*4+sp;
    const unsigned T=rt*4+w, col0=ct*128;
    const unsigned offX=rfl((T*112+sp*28)*4096), offW=rfl((sp*896+16*w)*2048+col0*2), wdst=rfl(w*4096);
    const unsigned vRes=((To*32+ct*4)*4+w)*1024+lane*16;

    f32x32 a0,a1,a2,a3; u32x16 xr; const unsigned cofs=rfl((loc*4+w)*1024);
    asm volatile(
    "s_mov_b64 s[36:37], %5\n"
    "s_mov_b32 s38, %6\n"
    "s_mov_b32 s39, 0x20000\n"
    "s_mov_b64 s[40:41], %7\n"
    "s_mov_b32 s42, %8\n"
    "s_mov_b32 s43, 0x20000\n"
    "s_mov_b32 s52, %9\n"
    "s_mov_b32 s53, %10\n"
    "s_mov_b32 s56, %11\n"
    "s_mov_b32 s57, 0\n"
    "s_mov_b32 s58, 0\n"
    "v_add_u32 v128, 0, %13\n"
    "v_add_u32 v129, 0, %14\n"
    "v_mov_b32 v0, 0\n"
    "v_mov_b32 v1, 0\n"
    "v_mov_b32 v2, 0\n"
    "v_mov_b32 v3, 0\n"
    "v_mov_b32 v4, 0\n"
    "v_mov_b32 v5, 0\n"
    "v_mov_b32 v6, 0\n"
    "v_mov_b32 v7, 0\n"
    "v_mov_b32 v8, 0\n"
    "v_mov_b32 v9, 0\n"
    "v_mov_b32 v10, 0\n"
    "v_mov_b32 v11, 0\n"
    "v_mov_b32 v12, 0\n"
    "v_mov_b32 v13, 0\n"
    "v_mov_b32 v14, 0\n"
    "v_mov_b32 v15, 0\n"
    "v_mov_b32 v16, 0\n"
    "v_mov_b32 v17, 0\n"
    "v_mov_b32 v18, 0\n"
    "v_mov_b32 v19, 0\n"
    "v_mov_b32 v20, 0\n"
    "v_mov_b32 v21, 0\n"
    "v_mov_b32 v22, 0\n"
    "v_mov_b32 v23, 0\n"
    "v_mov_b32 v24, 0\n"
    "v_mov_b32 v25, 0\n"
    "v_mov_b32 v26, 0\n"
    "v_mov_b32 v27, 0\n"
    "v_mov_b32 v28, 0\n"
    "v_mov_b32 v29, 0\n"
    "v_mov_b32 v30, 0\n"
    "v_mov_b32 v31, 0\n"
    "v_mov_b32 v32, 0\n"
    "v_mov_b32 v33, 0\n"
    "v_mov_b32 v34, 0\n"
    "v_mov_b32 v35, 0\n"
    "v_mov_b32 v36, 0\n"
    "v_mov_b32 v37, 0\n"
    "v_mov_b32 v38, 0\n"
    "v_mov_b32 v39, 0\n"
    "v_mov_b32 v40, 0\n"
    "v_mov_b32 v41, 0\n"
    "v_mov_b32 v42, 0\n"
    "v_mov_b32 v43, 0\n"
    "v_mov_b32 v44, 0\n"
    "v_mov_b32 v45, 0\n"
    "v_mov_b32 v46, 0\n"
    "v_mov_b32 v47, 0\n"
    "v_mov_b32 v48, 0\n"
    "v_mov_b32 v49, 0\n"
    "v_mov_b32 v50, 0\n"
    "v_mov_b32 v51, 0\n"
    "v_mov_b32 v52, 0\n"
    "v_mov_b32 v53, 0\n"
    "v_mov_b32 v54, 0\n"
    "v_mov_b32 v55, 0\n"
    "v_mov_b32 v56, 0\n"
    "v_mov_b32 v57, 0\n"
    "v_mov_b32 v58, 0\n"
    "v_mov_b32 v59, 0\n"
    "v_mov_b32 v60, 0\n"
    "v_mov_b32 v61, 0\n"
    "v_mov_b32 v62, 0\n"
    "v_mov_b32 v63, 0\n"
    "v_mov_b32 v64, 0\n"
    "v_mov_b32 v65, 0\n"
    "v_mov_b32 v66, 0\n"
    "v_mov_b32 v67, 0\n"
    "v_mov_b32 v68, 0\n"
    "v_mov_b32 v69, 0\n"
    "v_mov_b32 v70, 0\n"
    "v_mov_b32 v71, 0\n"
    "v_mov_b32 v72, 0\n"
    "v_mov_b32 v73, 0\n"
    "v_mov_b32 v74, 0\n"
    "v_mov_b32 v75, 0\n"
    "v_mov_b32 v76, 0\n"
    "v_mov_b32 v77, 0\n"
    "v_mov_b32 v78, 0\n"
    "v_mov_b32 v79, 0\n"
    "v_mov_b32 v80, 0\n"
    "v_mov_b32 v81, 0\n"
    "v_mov_b32 v82, 0\n"
    "v_mov_b32 v83, 0\n"
    "v_mov_b32 v84, 0\n"
    "v_mov_b32 v85, 0\n"
    "v_mov_b32 v86, 0\n"
    "v_mov_b32 v87, 0\n"
    "v_mov_b32 v88, 0\n"
    "v_mov_b32 v89, 0\n"
    "v_mov_b32 v90, 0\n"
    "v_mov_b32 v91, 0\n"
    "v_mov_b32 v92, 0\n"
    "v_mov_b32 v93, 0\n"
    "v_mov_b32 v94, 0\n"
    "v_mov_b32 v95, 0\n"
    "v_mov_b32 v96, 0\n"
    "v_mov_b32 v97, 0\n"
    "v_mov_b32 v98, 0\n"
    "v_mov_b32 v99, 0\n"
    "v_mov_b32 v100, 0\n"
    "v_mov_b32 v101, 0\n"
    "v_mov_b32 v102, 0\n"
    "v_mov_b32 v103, 0\n"
    "v_mov_b32 v104, 0\n"
    "v_mov_b32 v105, 0\n"
    "v_mov_b32 v106, 0\n"
    "v_mov_b32 v107, 0\n"
    "v_mov_b32 v108, 0\n"
    "v_mov_b32 v109, 0\n"
    "v_mov_b32 v110, 0\n"
    "v_mov_b32 v111, 0\n"
    "v_mov_b32 v112, 0\n"
    "v_mov_b32 v113, 0\n"
    "v_mov_b32 v114, 0\n"
    "v_mov_b32 v115, 0\n"
    "v_mov_b32 v116, 0\n"
    "v_mov_b32 v117, 0\n"
    "v_mov_b32 v118, 0\n"
    "v_mov_b32 v119, 0\n"
    "v_mov_b32 v120, 0\n"
    "v_mov_b32 v121, 0\n"
    "v_mov_b32 v122, 0\n"
    "v_mov_b32 v123, 0\n"
    "v_mov_b32 v124, 0\n"
    "v_mov_b32 v125, 0\n"
    "v_mov_b32 v126, 0\n"
    "v_mov_b32 v127, 0\n"
    "s_cmp_lg_u32 %11, 0\n"
    "s_cbranch_scc1 L_k5_g_done\n"
    "s_mov_b64 s[88:89], %22\n"
    "s_mov_b32 s90, 12804\n"
    "s_mov_b32 s91, 0x20000\n"
    "s_mov_b32 s92, 131072\n"
    "v_bfe_u32 v131, %12, 4, 1\n"
    "v_mul_u32_u24 v131, 12800, v131\n"
    "L_k5_g_poll:\n"
    "buffer_load_dword v130, v131, s[88:91], 0 offen sc1\n"
    "s_waitcnt vmcnt(0)\n"
    "v_cmp_gt_u32 vcc, 8, v130\n"
    "s_nop 1\n"
    "s_cbranch_vccz L_k5_g_done\n"
    "s_sleep 1\n"
    "s_sub_u32 s92, s92, 1\n"
    "s_cmp_gt_i32 s92, 0\n"
    "s_cbranch_scc1 L_k5_g_poll\n"
    "L_k5_g_done:\n"
    "s_barrier\n"
    "s_getpc_b64 s[80:81]\n"
    "L_pc2:\n"
    "s_sub_u32 s80, s80, L_pc2-L_mega_code\n"
    "s_subb_u32 s81, s81, 0\n"
    "s_mov_b32 s82, L_text_end-L_mega_code\n"
    "s_mov_b32 s83, 0x20000\n"
    "s_and_b32 s84, %21, 3072\n"
    "s_add_u32 m0, s84, 159744\n"
    "s_nop 0\n"
    "buffer_load_dwordx4 %12, s[80:83], %21 offen lds\n"
    "s_add_u32 m0, s57, s56\n"
    "s_add_u32 m0, m0, 1024\n"
    "s_add_u32 m0, m0, 1024\n"
    "s_add_u32 m0, m0, 1024\n"
    "s_add_u32 s53, s53, 131072\n"
    "s_add_u32 s57, s57, 16384\n"
    "s_cmp_ge_u32 s57, 49152\n"
    "s_cselect_b32 s57, 0, s57\n"
    "buffer_load_dwordx4 a[64:67], %12, s[36:39], s52 offen sc1\n"
    "buffer_load_dwordx4 a[68:71], %12, s[36:39], s52 offen offset:1024 sc1\n"
    "buffer_load_dwordx4 a[72:75], %12, s[36:39], s52 offen offset:2048 sc1\n"
    "buffer_load_dwordx4 a[76:79], %12, s[36:39], s52 offen offset:3072 sc1\n"
    "s_add_u32 s52, s52, 4096\n"
    "buffer_load_dwordx4 a[80:83], %12, s[36:39], s52 offen sc1\n"
    "buffer_load_dwordx4 a[84:87], %12, s[36:39], s52 offen offset:1024 sc1\n"
    "buffer_load_dwordx4 a[88:91], %12, s[36:39], s52 offen offset:2048 sc1\n"
    "buffer_load_dwordx4 a[92:95], %12, s[36:39], s52 offen offset:3072 sc1\n"
    "s_add_u32 s52, s52, 4096\n"
    "s_add_u32 m0, s57, s56\n"
    "s_add_u32 m0, m0, 1024\n"
    "s_add_u32 m0, m0, 1024\n"
    "s_add_u32 m0, m0, 1024\n"
    "s_add_u32 s53, s53, 131072\n"
    "s_add_u32 s57, s57, 16384\n"
    "s_cmp_ge_u32 s57, 49152\n"
    "s_cselect_b32 s57, 0, s57\n"
    "buffer_load_dwordx4 a[96:99], %12, s[36:39], s52 offen sc1\n"
    "buffer_load_dwordx4 a[100:103], %12, s[36:39], s52 offen offset:1024 sc1\n"
    "buffer_load_dwordx4 a[104:107], %12, s[36:39], s52 offen offset:2048 sc1\n"
    "buffer_load_dwordx4 a[108:111], %12, s[36:39], s52 offen offset:3072 sc1\n"
    "s_add_u32 s52, s52, 4096\n"
    "buffer_load_dwordx4 a[112:115], %12, s[36:39], s52 offen sc1\n"
    "buffer_load_dwordx4 a[116:119], %12, s[36:39], s52 offen offset:1024 sc1\n"
    "buffer_load_dwordx4 a[120:123], %12, s[36:39], s52 offen offset:2048 sc1\n"
    "buffer_load_dwordx4 a[124:127], %12, s[36:39], s52 offen offset:3072 sc1\n"
    "s_add_u32 s52, s52, 4096\n"
    "s_add_u32 m0, s57, s56\n"
    "s_add_u32 m0, m0, 1024\n"
    "s_mov_b32 s60, 12\n"
    "s_mov_b32 s59, 13\n"
    "s_mov_b32 s55, 4\n"
    "s_mov_b64 s[68:69], %19\n"
    "s_mov_b32 s70, 4194304\n"
    "s_mov_b32 s71, 0x20000\n"
    "s_mov_b32 s76, 0\n"
    "buffer_load_dwordx4 v[132:135], %20, s[68:71], s76 offen sc1\n"
    "s_add_u32 s76, s76, 4096\n"
    "buffer_load_dwordx4 v[136:139], %20, s[68:71], s76 offen sc1\n"
    "s_add_u32 s76, s76, 4096\n"
    "buffer_load_dwordx4 v[140:143], %20, s[68:71], s76 offen sc1\n"
    "s_add_u32 s76, s76, 4096\n"
    "buffer_load_dwordx4 v[144:147], %20, s[68:71], s76 offen sc1\n"
    "s_add_u32 s76, s76, 4096\n"
    "s_waitcnt vmcnt(12)\n"
    "s_barrier\n"
    "ds_read_b64_tr_b16 a[0:1], v128 offset:0\n"
    "ds_read_b64_tr_b16 a[2:3], v128 offset:4096\n"
    "ds_read_b64_tr_b16 a[4:5], v128 offset:8192\n"
    "ds_read_b64_tr_b16 a[6:7], v128 offset:12288\n"
    "ds_read_b64_tr_b16 a[8:9], v129 offset:0\n"
    "ds_read_b64_tr_b16 a[10:11], v129 offset:4096\n"
    "ds_read_b64_tr_b16 a[12:13], v129 offset:8192\n"
    "ds_read_b64_tr_b16 a[14:15], v129 offset:12288\n"
    "ds_read_b64_tr_b16 a[16:17], v128 offset:512\n"
    "ds_read_b64_tr_b16 a[18:19], v128 offset:4608\n"
    "ds_read_b64_tr_b16 a[20:21], v128 offset:8704\n"
    "ds_read_b64_tr_b16 a[22:23], v128 offset:12800\n"
    "ds_read_b64_tr_b16 a[24:25], v129 offset:512\n"
    "ds_read_b64_tr_b16 a[26:27], v129 offset:4608\n"
    "ds_read_b64_tr_b16 a[28:29], v129 offset:8704\n"
    "ds_read_b64_tr_b16 a[30:31], v129 offset:12800\n"
    "s_waitcnt lgkmcnt(0)\n"
    ".p2align 6\n"
    "L_k5_loop:\n"
    "v_mfma_f32_16x16x32_bf16 v[0:3], a[0:3], a[64:67], v[0:3]\n"
    "s_sub_u32 s59, s59, 1\n"
    "ds_read_b64_tr_b16 a[32:33], v128 offset:1024\n"
    "v_mfma_f32_16x16x32_bf16 v[0:3], a[4:7], a[80:83], v[0:3]\n"
    "s_cmp_gt_i32 s59, 0\n"
    "ds_read_b64_tr_b16 a[34:35], v128 offset:5120\n"
    "v_mfma_f32_16x16x32_bf16 v[4:7], a[4:7], a[84:87], v[4:7]\n"
    "s_cselect_b32 s38, s38, 0\n"
    "ds_read_b64_tr_b16 a[36:37], v128 offset:9216\n"
    "v_mfma_f32_16x16x32_bf16 v[4:7], a[0:3], a[68:71], v[4:7]\n"
    "ds_read_b64_tr_b16 a[38:39], v128 offset:13312\n"
    "v_mfma_f32_16x16x32_bf16 v[8:11], a[0:3], a[72:75], v[8:11]\n"
    "buffer_load_dwordx4 a[128:131], %12, s[36:39], s52 offen sc1\n"
    "v_mfma_f32_16x16x32_bf16 v[8:11], a[4:7], a[88:91], v[8:11]\n"
    "ds_read_b64_tr_b16 a[40:41], v129 offset:1024\n"
    "v_mfma_f32_16x16x32_bf16 v[12:15], a[4:7], a[92:95], v[12:15]\n"
    "s_add_u32 m0, m0, 1024\n"
    "ds_read_b64_tr_b16 a[42:43], v129 offset:5120\n"
    "v_mfma_f32_16x16x32_bf16 v[12:15], a[0:3], a[76:79], v[12:15]\n"
    "buffer_load_dwordx4 %17, s[40:43], s53 offen lds\n"
    "v_mfma_f32_16x16x32_bf16 v[28:31], a[8:11], a[76:79], v[28:31]\n"
    "ds_read_b64_tr_b16 a[44:45], v129 offset:9216\n"
    "v_mfma_f32_16x16x32_bf16 v[28:31], a[12:15], a[92:95], v[28:31]\n"
    "ds_read_b64_tr_b16 a[46:47], v129 offset:13312\n"
    "v_mfma_f32_16x16x32_bf16 v[24:27], a[12:15], a[88:91], v[24:27]\n"
    "buffer_load_dwordx4 a[132:135], %12, s[36:39], s52 offen offset:1024 sc1\n"
    "v_mfma_f32_16x16x32_bf16 v[24:27], a[8:11], a[72:75], v[24:27]\n"
    "ds_read_b64_tr_b16 a[48:49], v128 offset:1536\n"
    "v_mfma_f32_16x16x32_bf16 v[20:23], a[8:11], a[68:71], v[20:23]\n"
    "s_add_u32 m0, m0, 1024\n"
    "ds_read_b64_tr_b16 a[50:51], v128 offset:5632\n"
    "v_mfma_f32_16x16x32_bf16 v[20:23], a[12:15], a[84:87], v[20:23]\n"
    "buffer_load_dwordx4 %18, s[40:43], s53 offen lds\n"
    "v_mfma_f32_16x16x32_bf16 v[16:19], a[12:15], a[80:83], v[16:19]\n"
    "s_add_u32 s53, s53, 131072\n"
    "ds_read_b64_tr_b16 a[52:53], v128 offset:9728\n"
    "v_mfma_f32_16x16x32_bf16 v[16:19], a[8:11], a[64:67], v[16:19]\n"
    "s_add_u32 s57, s57, 16384\n"
    "ds_read_b64_tr_b16 a[54:55], v128 offset:13824\n"
    "v_mfma_f32_16x16x32_bf16 v[32:35], a[16:19], a[64:67], v[32:35]\n"
    "s_cmp_ge_u32 s57, 49152\n"
    "buffer_load_dwordx4 a[136:139], %12, s[36:39], s52 offen offset:2048 sc1\n"
    "v_mfma_f32_16x16x32_bf16 v[32:35], a[20:23], a[80:83], v[32:35]\n"
    "s_cselect_b32 s57, 0, s57\n"
    "ds_read_b64_tr_b16 a[56:57], v129 offset:1536\n"
    "v_mfma_f32_16x16x32_bf16 v[36:39], a[20:23], a[84:87], v[36:39]\n"
    "ds_read_b64_tr_b16 a[58:59], v129 offset:5632\n"
    "v_mfma_f32_16x16x32_bf16 v[36:39], a[16:19], a[68:71], v[36:39]\n"
    "buffer_load_dwordx4 a[140:143], %12, s[36:39], s52 offen offset:3072 sc1\n"
    "v_mfma_f32_16x16x32_bf16 v[40:43], a[16:19], a[72:75], v[40:43]\n"
    "s_add_u32 s52, s52, 4096\n"
    "ds_read_b64_tr_b16 a[60:61], v129 offset:9728\n"
    "v_mfma_f32_16x16x32_bf16 v[40:43], a[20:23], a[88:91], v[40:43]\n"
    "ds_read_b64_tr_b16 a[62:63], v129 offset:13824\n"
    "v_mfma_f32_16x16x32_bf16 v[44:47], a[20:23], a[92:95], v[44:47]\n"
    "s_add_u32 s58, s58, 16384\n"
    "v_mfma_f32_16x16x32_bf16 v[44:47], a[16:19], a[76:79], v[44:47]\n"
    "s_cmp_ge_u32 s58, 49152\n"
    "v_mfma_f32_16x16x32_bf16 v[60:63], a[24:27], a[76:79], v[60:63]\n"
    "s_cselect_b32 s58, 0, s58\n"
    "v_mfma_f32_16x16x32_bf16 v[60:63], a[28:31], a[92:95], v[60:63]\n"
    "v_add_u32 v128, s58, %13\n"
    "v_add_u32 v129, s58, %14\n"
    "v_mfma_f32_16x16x32_bf16 v[56:59], a[28:31], a[88:91], v[56:59]\n"
    "v_mfma_f32_16x16x32_bf16 v[56:59], a[24:27], a[72:75], v[56:59]\n"
    "v_mfma_f32_16x16x32_bf16 v[52:55], a[24:27], a[68:71], v[52:55]\n"
    "v_mfma_f32_16x16x32_bf16 v[52:55], a[28:31], a[84:87], v[52:55]\n"
    "v_mfma_f32_16x16x32_bf16 v[48:51], a[28:31], a[80:83], v[48:51]\n"
    "v_mfma_f32_16x16x32_bf16 v[48:51], a[24:27], a[64:67], v[48:51]\n"
    "s_waitcnt vmcnt(14) lgkmcnt(0)\n"
    "v_mfma_f32_16x16x32_bf16 v[64:67], a[32:35], a[64:67], v[64:67]\n"
    "s_barrier\n"
    "v_mfma_f32_16x16x32_bf16 v[64:67], a[36:39], a[80:83], v[64:67]\n"
    "s_sub_u32 s60, s60, 1\n"
    "ds_read_b64_tr_b16 a[0:1], v128 offset:0\n"
    "v_mfma_f32_16x16x32_bf16 v[68:71], a[36:39], a[84:87], v[68:71]\n"
    "s_cmp_gt_i32 s60, 0\n"
    "ds_read_b64_tr_b16 a[2:3], v128 offset:4096\n"
    "v_mfma_f32_16x16x32_bf16 v[68:71], a[32:35], a[68:71], v[68:71]\n"
    "s_cselect_b32 s42, s42, 0\n"
    "ds_read_b64_tr_b16 a[4:5], v128 offset:8192\n"
    "v_mfma_f32_16x16x32_bf16 v[72:75], a[32:35], a[72:75], v[72:75]\n"
    "ds_read_b64_tr_b16 a[6:7], v128 offset:12288\n"
    "v_mfma_f32_16x16x32_bf16 v[72:75], a[36:39], a[88:91], v[72:75]\n"
    "buffer_load_dwordx4 a[144:147], %12, s[36:39], s52 offen sc1\n"
    "v_mfma_f32_16x16x32_bf16 v[76:79], a[36:39], a[92:95], v[76:79]\n"
    "ds_read_b64_tr_b16 a[8:9], v129 offset:0\n"
    "v_mfma_f32_16x16x32_bf16 v[76:79], a[32:35], a[76:79], v[76:79]\n"
    "ds_read_b64_tr_b16 a[10:11], v129 offset:4096\n"
    "v_mfma_f32_16x16x32_bf16 v[92:95], a[40:43], a[76:79], v[92:95]\n"
    "s_add_u32 m0, s57, s56\n"
    "ds_read_b64_tr_b16 a[12:13], v129 offset:8192\n"
    "v_mfma_f32_16x16x32_bf16 v[92:95], a[44:47], a[92:95], v[92:95]\n"
    "buffer_load_dwordx4 %15, s[40:43], s53 offen lds\n"
    "v_mfma_f32_16x16x32_bf16 v[88:91], a[44:47], a[88:91], v[88:91]\n"
    "ds_read_b64_tr_b16 a[14:15], v129 offset:12288\n"
    "v_mfma_f32_16x16x32_bf16 v[88:91], a[40:43], a[72:75], v[88:91]\n"
    "ds_read_b64_tr_b16 a[16:17], v128 offset:512\n"
    "v_mfma_f32_16x16x32_bf16 v[84:87], a[40:43], a[68:71], v[84:87]\n"
    "ds_read_b64_tr_b16 a[18:19], v128 offset:4608\n"
    "v_mfma_f32_16x16x32_bf16 v[84:87], a[44:47], a[84:87], v[84:87]\n"
    "buffer_load_dwordx4 a[148:151], %12, s[36:39], s52 offen offset:1024 sc1\n"
    "v_mfma_f32_16x16x32_bf16 v[80:83], a[44:47], a[80:83], v[80:83]\n"
    "ds_read_b64_tr_b16 a[20:21], v128 offset:8704\n"
    "v_mfma_f32_16x16x32_bf16 v[80:83], a[40:43], a[64:67], v[80:83]\n"
    "ds_read_b64_tr_b16 a[22:23], v128 offset:12800\n"
    "v_mfma_f32_16x16x32_bf16 v[96:99], a[48:51], a[64:67], v[96:99]\n"
    "s_add_u32 m0, m0, 1024\n"
    "ds_read_b64_tr_b16 a[24:25], v129 offset:512\n"
    "v_mfma_f32_16x16x32_bf16 v[96:99], a[52:55], a[80:83], v[96:99]\n"
    "buffer_load_dwordx4 %16, s[40:43], s53 offen lds\n"
    "v_mfma_f32_16x16x32_bf16 v[100:103], a[52:55], a[84:87], v[100:103]\n"
    "ds_read_b64_tr_b16 a[26:27], v129 offset:4608\n"
    "v_mfma_f32_16x16x32_bf16 v[100:103], a[48:51], a[68:71], v[100:103]\n"
    "ds_read_b64_tr_b16 a[28:29], v129 offset:8704\n"
    "v_mfma_f32_16x16x32_bf16 v[104:107], a[48:51], a[72:75], v[104:107]\n"
    "ds_read_b64_tr_b16 a[30:31], v129 offset:12800\n"
    "v_mfma_f32_16x16x32_bf16 v[104:107], a[52:55], a[88:91], v[104:107]\n"
    "buffer_load_dwordx4 a[152:155], %12, s[36:39], s52 offen offset:2048 sc1\n"
    "v_mfma_f32_16x16x32_bf16 v[108:111], a[52:55], a[92:95], v[108:111]\n"
    "v_mfma_f32_16x16x32_bf16 v[108:111], a[48:51], a[76:79], v[108:111]\n"
    "v_mfma_f32_16x16x32_bf16 v[124:127], a[56:59], a[76:79], v[124:127]\n"
    "v_mfma_f32_16x16x32_bf16 v[124:127], a[60:63], a[92:95], v[124:127]\n"
    "buffer_load_dwordx4 a[156:159], %12, s[36:39], s52 offen offset:3072 sc1\n"
    "v_mfma_f32_16x16x32_bf16 v[120:123], a[60:63], a[88:91], v[120:123]\n"
    "s_add_u32 s52, s52, 4096\n"
    "v_mfma_f32_16x16x32_bf16 v[120:123], a[56:59], a[72:75], v[120:123]\n"
    "v_mfma_f32_16x16x32_bf16 v[116:119], a[56:59], a[68:71], v[116:119]\n"
    "v_mfma_f32_16x16x32_bf16 v[116:119], a[60:63], a[84:87], v[116:119]\n"
    "v_mfma_f32_16x16x32_bf16 v[112:115], a[60:63], a[80:83], v[112:115]\n"
    "v_mfma_f32_16x16x32_bf16 v[112:115], a[56:59], a[64:67], v[112:115]\n"
    "s_waitcnt vmcnt(12) lgkmcnt(0)\n"
    "v_mfma_f32_16x16x32_bf16 v[0:3], a[0:3], a[96:99], v[0:3]\n"
    "s_sub_u32 s59, s59, 1\n"
    "ds_read_b64_tr_b16 a[32:33], v128 offset:1024\n"
    "v_mfma_f32_16x16x32_bf16 v[0:3], a[4:7], a[112:115], v[0:3]\n"
    "s_cmp_gt_i32 s59, 0\n"
    "ds_read_b64_tr_b16 a[34:35], v128 offset:5120\n"
    "v_mfma_f32_16x16x32_bf16 v[4:7], a[4:7], a[116:119], v[4:7]\n"
    "s_cselect_b32 s38, s38, 0\n"
    "ds_read_b64_tr_b16 a[36:37], v128 offset:9216\n"
    "v_mfma_f32_16x16x32_bf16 v[4:7], a[0:3], a[100:103], v[4:7]\n"
    "ds_read_b64_tr_b16 a[38:39], v128 offset:13312\n"
    "v_mfma_f32_16x16x32_bf16 v[8:11], a[0:3], a[104:107], v[8:11]\n"
    "buffer_load_dwordx4 a[64:67], %12, s[36:39], s52 offen sc1\n"
    "v_mfma_f32_16x16x32_bf16 v[8:11], a[4:7], a[120:123], v[8:11]\n"
    "ds_read_b64_tr_b16 a[40:41], v129 offset:1024\n"
    "v_mfma_f32_16x16x32_bf16 v[12:15], a[4:7], a[124:127], v[12:15]\n"
    "s_add_u32 m0, m0, 1024\n"
    "ds_read_b64_tr_b16 a[42:43], v129 offset:5120\n"
    "v_mfma_f32_16x16x32_bf16 v[12:15], a[0:3], a[108:111], v[12:15]\n"
    "buffer_load_dwordx4 %17, s[40:43], s53 offen lds\n"
    "v_mfma_f32_16x16x32_bf16 v[28:31], a[8:11], a[108:111], v[28:31]\n"
    "ds_read_b64_tr_b16 a[44:45], v129 offset:9216\n"
    "v_mfma_f32_16x16x32_bf16 v[28:31], a[12:15], a[124:127], v[28:31]\n"
    "ds_read_b64_tr_b16 a[46:47], v129 offset:13312\n"
    "v_mfma_f32_16x16x32_bf16 v[24:27], a[12:15], a[120:123], v[24:27]\n"
    "buffer_load_dwordx4 a[68:71], %12, s[36:39], s52 offen offset:1024 sc1\n"
    "v_mfma_f32_16x16x32_bf16 v[24:27], a[8:11], a[104:107], v[24:27]\n"
    "ds_read_b64_tr_b16 a[48:49], v128 offset:1536\n"
    "v_mfma_f32_16x16x32_bf16 v[20:23], a[8:11], a[100:103], v[20:23]\n"
    "s_add_u32 m0, m0, 1024\n"
    "ds_read_b64_tr_b16 a[50:51], v128 offset:5632\n"
    "v_mfma_f32_16x16x32_bf16 v[20:23], a[12:15], a[116:119], v[20:23]\n"
    "buffer_load_dwordx4 %18, s[40:43], s53 offen lds\n"
    "v_mfma_f32_16x16x32_bf16 v[16:19], a[12:15], a[112:115], v[16:19]\n"
    "s_add_u32 s53, s53, 131072\n"
    "ds_read_b64_tr_b16 a[52:53], v128 offset:9728\n"
    "v_mfma_f32_16x16x32_bf16 v[16:19], a[8:11], a[96:99], v[16:19]\n"
    "s_add_u32 s57, s57, 16384\n"
    "ds_read_b64_tr_b16 a[54:55], v128 offset:13824\n"
    "v_mfma_f32_16x16x32_bf16 v[32:35], a[16:19], a[96:99], v[32:35]\n"
    "s_cmp_ge_u32 s57, 49152\n"
    "buffer_load_dwordx4 a[72:75], %12, s[36:39], s52 offen offset:2048 sc1\n"
    "v_mfma_f32_16x16x32_bf16 v[32:35], a[20:23], a[112:115], v[32:35]\n"
    "s_cselect_b32 s57, 0, s57\n"
    "ds_read_b64_tr_b16 a[56:57], v129 offset:1536\n"
    "v_mfma_f32_16x16x32_bf16 v[36:39], a[20:23], a[116:119], v[36:39]\n"
    "ds_read_b64_tr_b16 a[58:59], v129 offset:5632\n"
    "v_mfma_f32_16x16x32_bf16 v[36:39], a[16:19], a[100:103], v[36:39]\n"
    "buffer_load_dwordx4 a[76:79], %12, s[36:39], s52 offen offset:3072 sc1\n"
    "v_mfma_f32_16x16x32_bf16 v[40:43], a[16:19], a[104:107], v[40:43]\n"
    "s_add_u32 s52, s52, 4096\n"
    "ds_read_b64_tr_b16 a[60:61], v129 offset:9728\n"
    "v_mfma_f32_16x16x32_bf16 v[40:43], a[20:23], a[120:123], v[40:43]\n"
    "ds_read_b64_tr_b16 a[62:63], v129 offset:13824\n"
    "v_mfma_f32_16x16x32_bf16 v[44:47], a[20:23], a[124:127], v[44:47]\n"
    "s_add_u32 s58, s58, 16384\n"
    "v_mfma_f32_16x16x32_bf16 v[44:47], a[16:19], a[108:111], v[44:47]\n"
    "s_cmp_ge_u32 s58, 49152\n"
    "v_mfma_f32_16x16x32_bf16 v[60:63], a[24:27], a[108:111], v[60:63]\n"
    "s_cselect_b32 s58, 0, s58\n"
    "v_mfma_f32_16x16x32_bf16 v[60:63], a[28:31], a[124:127], v[60:63]\n"
    "v_add_u32 v128, s58, %13\n"
    "v_add_u32 v129, s58, %14\n"
    "v_mfma_f32_16x16x32_bf16 v[56:59], a[28:31], a[120:123], v[56:59]\n"
    "v_mfma_f32_16x16x32_bf16 v[56:59], a[24:27], a[104:107], v[56:59]\n"
    "v_mfma_f32_16x16x32_bf16 v[52:55], a[24:27], a[100:103], v[52:55]\n"
    "v_mfma_f32_16x16x32_bf16 v[52:55], a[28:31], a[116:119], v[52:55]\n"
    "v_mfma_f32_16x16x32_bf16 v[48:51], a[28:31], a[112:115], v[48:51]\n"
    "v_mfma_f32_16x16x32_bf16 v[48:51], a[24:27], a[96:99], v[48:51]\n"
    "s_waitcnt vmcnt(14) lgkmcnt(0)\n"
    "v_mfma_f32_16x16x32_bf16 v[64:67], a[32:35], a[96:99], v[64:67]\n"
    "s_barrier\n"
    "v_mfma_f32_16x16x32_bf16 v[64:67], a[36:39], a[112:115], v[64:67]\n"
    "s_sub_u32 s60, s60, 1\n"
    "ds_read_b64_tr_b16 a[0:1], v128 offset:0\n"
    "v_mfma_f32_16x16x32_bf16 v[68:71], a[36:39], a[116:119], v[68:71]\n"
    "s_cmp_gt_i32 s60, 0\n"
    "ds_read_b64_tr_b16 a[2:3], v128 offset:4096\n"
    "v_mfma_f32_16x16x32_bf16 v[68:71], a[32:35], a[100:103], v[68:71]\n"
    "s_cselect_b32 s42, s42, 0\n"
    "ds_read_b64_tr_b16 a[4:5], v128 offset:8192\n"
    "v_mfma_f32_16x16x32_bf16 v[72:75], a[32:35], a[104:107], v[72:75]\n"
    "ds_read_b64_tr_b16 a[6:7], v128 offset:12288\n"
    "v_mfma_f32_16x16x32_bf16 v[72:75], a[36:39], a[120:123], v[72:75]\n"
    "buffer_load_dwordx4 a[80:83], %12, s[36:39], s52 offen sc1\n"
    "v_mfma_f32_16x16x32_bf16 v[76:79], a[36:39], a[124:127], v[76:79]\n"
    "ds_read_b64_tr_b16 a[8:9], v129 offset:0\n"
    "v_mfma_f32_16x16x32_bf16 v[76:79], a[32:35], a[108:111], v[76:79]\n"
    "ds_read_b64_tr_b16 a[10:11], v129 offset:4096\n"
    "v_mfma_f32_16x16x32_bf16 v[92:95], a[40:43], a[108:111], v[92:95]\n"
    "s_add_u32 m0, s57, s56\n"
    "ds_read_b64_tr_b16 a[12:13], v129 offset:8192\n"
    "v_mfma_f32_16x16x32_bf16 v[92:95], a[44:47], a[124:127], v[92:95]\n"
    "buffer_load_dwordx4 %15, s[40:43], s53 offen lds\n"
    "v_mfma_f32_16x16x32_bf16 v[88:91], a[44:47], a[120:123], v[88:91]\n"
    "ds_read_b64_tr_b16 a[14:15], v129 offset:12288\n"
    "v_mfma_f32_16x16x32_bf16 v[88:91], a[40:43], a[104:107], v[88:91]\n"
    "ds_read_b64_tr_b16 a[16:17], v128 offset:512\n"
    "v_mfma_f32_16x16x32_bf16 v[84:87], a[40:43], a[100:103], v[84:87]\n"
    "ds_read_b64_tr_b16 a[18:19], v128 offset:4608\n"
    "v_mfma_f32_16x16x32_bf16 v[84:87], a[44:47], a[116:119], v[84:87]\n"
    "buffer_load_dwordx4 a[84:87], %12, s[36:39], s52 offen offset:1024 sc1\n"
    "v_mfma_f32_16x16x32_bf16 v[80:83], a[44:47], a[112:115], v[80:83]\n"
    "ds_read_b64_tr_b16 a[20:21], v128 offset:8704\n"
    "v_mfma_f32_16x16x32_bf16 v[80:83], a[40:43], a[96:99], v[80:83]\n"
    "ds_read_b64_tr_b16 a[22:23], v128 offset:12800\n"
    "v_mfma_f32_16x16x32_bf16 v[96:99], a[48:51], a[96:99], v[96:99]\n"
    "s_add_u32 m0, m0, 1024\n"
    "ds_read_b64_tr_b16 a[24:25], v129 offset:512\n"
    "v_mfma_f32_16x16x32_bf16 v[96:99], a[52:55], a[112:115], v[96:99]\n"
    "buffer_load_dwordx4 %16, s[40:43], s53 offen lds\n"
    "v_mfma_f32_16x16x32_bf16 v[100:103], a[52:55], a[116:119], v[100:103]\n"
    "ds_read_b64_tr_b16 a[26:27], v129 offset:4608\n"
    "v_mfma_f32_16x16x32_bf16 v[100:103], a[48:51], a[100:103], v[100:103]\n"
    "ds_read_b64_tr_b16 a[28:29], v129 offset:8704\n"
    "v_mfma_f32_16x16x32_bf16 v[104:107], a[48:51], a[104:107], v[104:107]\n"
    "ds_read_b64_tr_b16 a[30:31], v129 offset:12800\n"
    "v_mfma_f32_16x16x32_bf16 v[104:107], a[52:55], a[120:123], v[104:107]\n"
    "buffer_load_dwordx4 a[88:91], %12, s[36:39], s52 offen offset:2048 sc1\n"
    "v_mfma_f32_16x16x32_bf16 v[108:111], a[52:55], a[124:127], v[108:111]\n"
    "v_mfma_f32_16x16x32_bf16 v[108:111], a[48:51], a[108:111], v[108:111]\n"
    "v_mfma_f32_16x16x32_bf16 v[124:127], a[56:59], a[108:111], v[124:127]\n"
    "v_mfma_f32_16x16x32_bf16 v[124:127], a[60:63], a[124:127], v[124:127]\n"
    "buffer_load_dwordx4 a[92:95], %12, s[36:39], s52 offen offset:3072 sc1\n"
    "v_mfma_f32_16x16x32_bf16 v[120:123], a[60:63], a[120:123], v[120:123]\n"
    "s_add_u32 s52, s52, 4096\n"
    "v_mfma_f32_16x16x32_bf16 v[120:123], a[56:59], a[104:107], v[120:123]\n"
    "v_mfma_f32_16x16x32_bf16 v[116:119], a[56:59], a[100:103], v[116:119]\n"
    "v_mfma_f32_16x16x32_bf16 v[116:119], a[60:63], a[116:119], v[116:119]\n"
    "v_mfma_f32_16x16x32_bf16 v[112:115], a[60:63], a[112:115], v[112:115]\n"
    "v_mfma_f32_16x16x32_bf16 v[112:115], a[56:59], a[96:99], v[112:115]\n"
    "s_waitcnt vmcnt(12) lgkmcnt(0)\n"
    "v_mfma_f32_16x16x32_bf16 v[0:3], a[0:3], a[128:131], v[0:3]\n"
    "s_sub_u32 s59, s59, 1\n"
    "ds_read_b64_tr_b16 a[32:33], v128 offset:1024\n"
    "v_mfma_f32_16x16x32_bf16 v[0:3], a[4:7], a[144:147], v[0:3]\n"
    "s_cmp_gt_i32 s59, 0\n"
    "ds_read_b64_tr_b16 a[34:35], v128 offset:5120\n"
    "v_mfma_f32_16x16x32_bf16 v[4:7], a[4:7], a[148:151], v[4:7]\n"
    "s_cselect_b32 s38, s38, 0\n"
    "ds_read_b64_tr_b16 a[36:37], v128 offset:9216\n"
    "v_mfma_f32_16x16x32_bf16 v[4:7], a[0:3], a[132:135], v[4:7]\n"
    "ds_read_b64_tr_b16 a[38:39], v128 offset:13312\n"
    "v_mfma_f32_16x16x32_bf16 v[8:11], a[0:3], a[136:139], v[8:11]\n"
    "buffer_load_dwordx4 a[96:99], %12, s[36:39], s52 offen sc1\n"
    "v_mfma_f32_16x16x32_bf16 v[8:11], a[4:7], a[152:155], v[8:11]\n"
    "ds_read_b64_tr_b16 a[40:41], v129 offset:1024\n"
    "v_mfma_f32_16x16x32_bf16 v[12:15], a[4:7], a[156:159], v[12:15]\n"
    "s_add_u32 m0, m0, 1024\n"
    "ds_read_b64_tr_b16 a[42:43], v129 offset:5120\n"
    "v_mfma_f32_16x16x32_bf16 v[12:15], a[0:3], a[140:143], v[12:15]\n"
    "buffer_load_dwordx4 %17, s[40:43], s53 offen lds\n"
    "v_mfma_f32_16x16x32_bf16 v[28:31], a[8:11], a[140:143], v[28:31]\n"
    "ds_read_b64_tr_b16 a[44:45], v129 offset:9216\n"
    "v_mfma_f32_16x16x32_bf16 v[28:31], a[12:15], a[156:159], v[28:31]\n"
    "ds_read_b64_tr_b16 a[46:47], v129 offset:13312\n"
    "v_mfma_f32_16x16x32_bf16 v[24:27], a[12:15], a[152:155], v[24:27]\n"
    "buffer_load_dwordx4 a[100:103], %12, s[36:39], s52 offen offset:1024 sc1\n"
    "v_mfma_f32_16x16x32_bf16 v[24:27], a[8:11], a[136:139], v[24:27]\n"
    "ds_read_b64_tr_b16 a[48:49], v128 offset:1536\n"
    "v_mfma_f32_16x16x32_bf16 v[20:23], a[8:11], a[132:135], v[20:23]\n"
    "s_add_u32 m0, m0, 1024\n"
    "ds_read_b64_tr_b16 a[50:51], v128 offset:5632\n"
    "v_mfma_f32_16x16x32_bf16 v[20:23], a[12:15], a[148:151], v[20:23]\n"
    "buffer_load_dwordx4 %18, s[40:43], s53 offen lds\n"
    "v_mfma_f32_16x16x32_bf16 v[16:19], a[12:15], a[144:147], v[16:19]\n"
    "s_add_u32 s53, s53, 131072\n"
    "ds_read_b64_tr_b16 a[52:53], v128 offset:9728\n"
    "v_mfma_f32_16x16x32_bf16 v[16:19], a[8:11], a[128:131], v[16:19]\n"
    "s_add_u32 s57, s57, 16384\n"
    "ds_read_b64_tr_b16 a[54:55], v128 offset:13824\n"
    "v_mfma_f32_16x16x32_bf16 v[32:35], a[16:19], a[128:131], v[32:35]\n"
    "s_cmp_ge_u32 s57, 49152\n"
    "buffer_load_dwordx4 a[104:107], %12, s[36:39], s52 offen offset:2048 sc1\n"
    "v_mfma_f32_16x16x32_bf16 v[32:35], a[20:23], a[144:147], v[32:35]\n"
    "s_cselect_b32 s57, 0, s57\n"
    "ds_read_b64_tr_b16 a[56:57], v129 offset:1536\n"
    "v_mfma_f32_16x16x32_bf16 v[36:39], a[20:23], a[148:151], v[36:39]\n"
    "ds_read_b64_tr_b16 a[58:59], v129 offset:5632\n"
    "v_mfma_f32_16x16x32_bf16 v[36:39], a[16:19], a[132:135], v[36:39]\n"
    "buffer_load_dwordx4 a[108:111], %12, s[36:39], s52 offen offset:3072 sc1\n"
    "v_mfma_f32_16x16x32_bf16 v[40:43], a[16:19], a[136:139], v[40:43]\n"
    "s_add_u32 s52, s52, 4096\n"
    "ds_read_b64_tr_b16 a[60:61], v129 offset:9728\n"
    "v_mfma_f32_16x16x32_bf16 v[40:43], a[20:23], a[152:155], v[40:43]\n"
    "ds_read_b64_tr_b16 a[62:63], v129 offset:13824\n"
    "v_mfma_f32_16x16x32_bf16 v[44:47], a[20:23], a[156:159], v[44:47]\n"
    "s_add_u32 s58, s58, 16384\n"
    "v_mfma_f32_16x16x32_bf16 v[44:47], a[16:19], a[140:143], v[44:47]\n"
    "s_cmp_ge_u32 s58, 49152\n"
    "v_mfma_f32_16x16x32_bf16 v[60:63], a[24:27], a[140:143], v[60:63]\n"
    "s_cselect_b32 s58, 0, s58\n"
    "v_mfma_f32_16x16x32_bf16 v[60:63], a[28:31], a[156:159], v[60:63]\n"
    "v_add_u32 v128, s58, %13\n"
    "v_add_u32 v129, s58, %14\n"
    "v_mfma_f32_16x16x32_bf16 v[56:59], a[28:31], a[152:155], v[56:59]\n"
    "v_mfma_f32_16x16x32_bf16 v[56:59], a[24:27], a[136:139], v[56:59]\n"
    "v_mfma_f32_16x16x32_bf16 v[52:55], a[24:27], a[132:135], v[52:55]\n"
    "v_mfma_f32_16x16x32_bf16 v[52:55], a[28:31], a[148:151], v[52:55]\n"
    "v_mfma_f32_16x16x32_bf16 v[48:51], a[28:31], a[144:147], v[48:51]\n"
    "v_mfma_f32_16x16x32_bf16 v[48:51], a[24:27], a[128:131], v[48:51]\n"
    "s_waitcnt vmcnt(14) lgkmcnt(0)\n"
    "v_mfma_f32_16x16x32_bf16 v[64:67], a[32:35], a[128:131], v[64:67]\n"
    "s_barrier\n"
    "v_mfma_f32_16x16x32_bf16 v[64:67], a[36:39], a[144:147], v[64:67]\n"
    "s_sub_u32 s60, s60, 1\n"
    "ds_read_b64_tr_b16 a[0:1], v128 offset:0\n"
    "v_mfma_f32_16x16x32_bf16 v[68:71], a[36:39], a[148:151], v[68:71]\n"
    "s_cmp_gt_i32 s60, 0\n"
    "ds_read_b64_tr_b16 a[2:3], v128 offset:4096\n"
    "v_mfma_f32_16x16x32_bf16 v[68:71], a[32:35], a[132:135], v[68:71]\n"
    "s_cselect_b32 s42, s42, 0\n"
    "ds_read_b64_tr_b16 a[4:5], v128 offset:8192\n"
    "v_mfma_f32_16x16x32_bf16 v[72:75], a[32:35], a[136:139], v[72:75]\n"
    "ds_read_b64_tr_b16 a[6:7], v128 offset:12288\n"
    "v_mfma_f32_16x16x32_bf16 v[72:75], a[36:39], a[152:155], v[72:75]\n"
    "buffer_load_dwordx4 a[112:115], %12, s[36:39], s52 offen sc1\n"
    "v_mfma_f32_16x16x32_bf16 v[76:79], a[36:39], a[156:159], v[76:79]\n"
    "ds_read_b64_tr_b16 a[8:9], v129 offset:0\n"
    "v_mfma_f32_16x16x32_bf16 v[76:79], a[32:35], a[140:143], v[76:79]\n"
    "ds_read_b64_tr_b16 a[10:11], v129 offset:4096\n"
    "v_mfma_f32_16x16x32_bf16 v[92:95], a[40:43], a[140:143], v[92:95]\n"
    "s_add_u32 m0, s57, s56\n"
    "ds_read_b64_tr_b16 a[12:13], v129 offset:8192\n"
    "v_mfma_f32_16x16x32_bf16 v[92:95], a[44:47], a[156:159], v[92:95]\n"
    "buffer_load_dwordx4 %15, s[40:43], s53 offen lds\n"
    "v_mfma_f32_16x16x32_bf16 v[88:91], a[44:47], a[152:155], v[88:91]\n"
    "ds_read_b64_tr_b16 a[14:15], v129 offset:12288\n"
    "v_mfma_f32_16x16x32_bf16 v[88:91], a[40:43], a[136:139], v[88:91]\n"
    "ds_read_b64_tr_b16 a[16:17], v128 offset:512\n"
    "v_mfma_f32_16x16x32_bf16 v[84:87], a[40:43], a[132:135], v[84:87]\n"
    "ds_read_b64_tr_b16 a[18:19], v128 offset:4608\n"
    "v_mfma_f32_16x16x32_bf16 v[84:87], a[44:47], a[148:151], v[84:87]\n"
    "buffer_load_dwordx4 a[116:119], %12, s[36:39], s52 offen offset:1024 sc1\n"
    "v_mfma_f32_16x16x32_bf16 v[80:83], a[44:47], a[144:147], v[80:83]\n"
    "ds_read_b64_tr_b16 a[20:21], v128 offset:8704\n"
    "v_mfma_f32_16x16x32_bf16 v[80:83], a[40:43], a[128:131], v[80:83]\n"
    "ds_read_b64_tr_b16 a[22:23], v128 offset:12800\n"
    "v_mfma_f32_16x16x32_bf16 v[96:99], a[48:51], a[128:131], v[96:99]\n"
    "s_add_u32 m0, m0, 1024\n"
    "ds_read_b64_tr_b16 a[24:25], v129 offset:512\n"
    "v_mfma_f32_16x16x32_bf16 v[96:99], a[52:55], a[144:147], v[96:99]\n"
    "buffer_load_dwordx4 %16, s[40:43], s53 offen lds\n"
    "v_mfma_f32_16x16x32_bf16 v[100:103], a[52:55], a[148:151], v[100:103]\n"
    "ds_read_b64_tr_b16 a[26:27], v129 offset:4608\n"
    "v_mfma_f32_16x16x32_bf16 v[100:103], a[48:51], a[132:135], v[100:103]\n"
    "ds_read_b64_tr_b16 a[28:29], v129 offset:8704\n"
    "v_mfma_f32_16x16x32_bf16 v[104:107], a[48:51], a[136:139], v[104:107]\n"
    "ds_read_b64_tr_b16 a[30:31], v129 offset:12800\n"
    "v_mfma_f32_16x16x32_bf16 v[104:107], a[52:55], a[152:155], v[104:107]\n"
    "buffer_load_dwordx4 a[120:123], %12, s[36:39], s52 offen offset:2048 sc1\n"
    "v_mfma_f32_16x16x32_bf16 v[108:111], a[52:55], a[156:159], v[108:111]\n"
    "v_mfma_f32_16x16x32_bf16 v[108:111], a[48:51], a[140:143], v[108:111]\n"
    "v_mfma_f32_16x16x32_bf16 v[124:127], a[56:59], a[140:143], v[124:127]\n"
    "v_mfma_f32_16x16x32_bf16 v[124:127], a[60:63], a[156:159], v[124:127]\n"
    "buffer_load_dwordx4 a[124:127], %12, s[36:39], s52 offen offset:3072 sc1\n"
    "v_mfma_f32_16x16x32_bf16 v[120:123], a[60:63], a[152:155], v[120:123]\n"
    "s_add_u32 s52, s52, 4096\n"
    "v_mfma_f32_16x16x32_bf16 v[120:123], a[56:59], a[136:139], v[120:123]\n"
    "v_mfma_f32_16x16x32_bf16 v[116:119], a[56:59], a[132:135], v[116:119]\n"
    "v_mfma_f32_16x16x32_bf16 v[116:119], a[60:63], a[148:151], v[116:119]\n"
    "v_mfma_f32_16x16x32_bf16 v[112:115], a[60:63], a[144:147], v[112:115]\n"
    "v_mfma_f32_16x16x32_bf16 v[112:115], a[56:59], a[128:131], v[112:115]\n"
    "s_waitcnt vmcnt(12) lgkmcnt(0)\n"
    "s_sub_u32 s55, s55, 1\n"
    "s_cmp_lg_u32 s55, 0\n"
    "s_cbranch_scc1 L_k5_loop\n"
    "v_mfma_f32_16x16x32_bf16 v[0:3], a[0:3], a[64:67], v[0:3]\n"
    "s_sub_u32 s59, s59, 1\n"
    "ds_read_b64_tr_b16 a[32:33], v128 offset:1024\n"
    "v_mfma_f32_16x16x32_bf16 v[0:3], a[4:7], a[80:83], v[0:3]\n"
    "s_cmp_gt_i32 s59, 0\n"
    "ds_read_b64_tr_b16 a[34:35], v128 offset:5120\n"
    "v_mfma_f32_16x16x32_bf16 v[4:7], a[4:7], a[84:87], v[4:7]\n"
    "s_cselect_b32 s38, s38, 0\n"
    "ds_read_b64_tr_b16 a[36:37], v128 offset:9216\n"
    "v_mfma_f32_16x16x32_bf16 v[4:7], a[0:3], a[68:71], v[4:7]\n"
    "ds_read_b64_tr_b16 a[38:39], v128 offset:13312\n"
    "v_mfma_f32_16x16x32_bf16 v[8:11], a[0:3], a[72:75], v[8:11]\n"
    "buffer_load_dwordx4 a[128:131], %12, s[36:39], s52 offen sc1\n"
    "v_mfma_f32_16x16x32_bf16 v[8:11], a[4:7], a[88:91], v[8:11]\n"
    "ds_read_b64_tr_b16 a[40:41], v129 offset:1024\n"
    "v_mfma_f32_16x16x32_bf16 v[12:15], a[4:7], a[92:95], v[12:15]\n"
    "s_add_u32 m0, m0, 1024\n"
    "ds_read_b64_tr_b16 a[42:43], v129 offset:5120\n"
    "v_mfma_f32_16x16x32_bf16 v[12:15], a[0:3], a[76:79], v[12:15]\n"
    "buffer_load_dwordx4 %17, s[40:43], s53 offen lds\n"
    "v_mfma_f32_16x16x32_bf16 v[28:31], a[8:11], a[76:79], v[28:31]\n"
    "ds_read_b64_tr_b16 a[44:45], v129 offset:9216\n"
    "v_mfma_f32_16x16x32_bf16 v[28:31], a[12:15], a[92:95], v[28:31]\n"
    "ds_read_b64_tr_b16 a[46:47], v129 offset:13312\n"
    "v_mfma_f32_16x16x32_bf16 v[24:27], a[12:15], a[88:91], v[24:27]\n"
    "buffer_load_dwordx4 a[132:135], %12, s[36:39], s52 offen offset:1024 sc1\n"
    "v_mfma_f32_16x16x32_bf16 v[24:27], a[8:11], a[72:75], v[24:27]\n"
    "ds_read_b64_tr_b16 a[48:49], v128 offset:1536\n"
    "v_mfma_f32_16x16x32_bf16 v[20:23], a[8:11], a[68:71], v[20:23]\n"
    "s_add_u32 m0, m0, 1024\n"
    "ds_read_b64_tr_b16 a[50:51], v128 offset:5632\n"
    "v_mfma_f32_16x16x32_bf16 v[20:23], a[12:15], a[84:87], v[20:23]\n"
    "buffer_load_dwordx4 %18, s[40:43], s53 offen lds\n"
    "v_mfma_f32_16x16x32_bf16 v[16:19], a[12:15], a[80:83], v[16:19]\n"
    "s_add_u32 s53, s53, 131072\n"
    "ds_read_b64_tr_b16 a[52:53], v128 offset:9728\n"
    "v_mfma_f32_16x16x32_bf16 v[16:19], a[8:11], a[64:67], v[16:19]\n"
    "s_add_u32 s57, s57, 16384\n"
    "ds_read_b64_tr_b16 a[54:55], v128 offset:13824\n"
    "v_mfma_f32_16x16x32_bf16 v[32:35], a[16:19], a[64:67], v[32:35]\n"
    "s_cmp_ge_u32 s57, 49152\n"
    "buffer_load_dwordx4 a[136:139], %12, s[36:39], s52 offen offset:2048 sc1\n"
    "v_mfma_f32_16x16x32_bf16 v[32:35], a[20:23], a[80:83], v[32:35]\n"
    "s_cselect_b32 s57, 0, s57\n"
    "ds_read_b64_tr_b16 a[56:57], v129 offset:1536\n"
    "v_mfma_f32_16x16x32_bf16 v[36:39], a[20:23], a[84:87], v[36:39]\n"
    "ds_read_b64_tr_b16 a[58:59], v129 offset:5632\n"
    "v_mfma_f32_16x16x32_bf16 v[36:39], a[16:19], a[68:71], v[36:39]\n"
    "buffer_load_dwordx4 a[140:143], %12, s[36:39], s52 offen offset:3072 sc1\n"
    "v_mfma_f32_16x16x32_bf16 v[40:43], a[16:19], a[72:75], v[40:43]\n"
    "s_add_u32 s52, s52, 4096\n"
    "ds_read_b64_tr_b16 a[60:61], v129 offset:9728\n"
    "v_mfma_f32_16x16x32_bf16 v[40:43], a[20:23], a[88:91], v[40:43]\n"
    "ds_read_b64_tr_b16 a[62:63], v129 offset:13824\n"
    "v_mfma_f32_16x16x32_bf16 v[44:47], a[20:23], a[92:95], v[44:47]\n"
    "s_add_u32 s58, s58, 16384\n"
    "v_mfma_f32_16x16x32_bf16 v[44:47], a[16:19], a[76:79], v[44:47]\n"
    "s_cmp_ge_u32 s58, 49152\n"
    "v_mfma_f32_16x16x32_bf16 v[60:63], a[24:27], a[76:79], v[60:63]\n"
    "s_cselect_b32 s58, 0, s58\n"
    "v_mfma_f32_16x16x32_bf16 v[60:63], a[28:31], a[92:95], v[60:63]\n"
    "v_add_u32 v128, s58, %13\n"
    "v_add_u32 v129, s58, %14\n"
    "v_mfma_f32_16x16x32_bf16 v[56:59], a[28:31], a[88:91], v[56:59]\n"
    "v_mfma_f32_16x16x32_bf16 v[56:59], a[24:27], a[72:75], v[56:59]\n"
    "v_mfma_f32_16x16x32_bf16 v[52:55], a[24:27], a[68:71], v[52:55]\n"
    "v_mfma_f32_16x16x32_bf16 v[52:55], a[28:31], a[84:87], v[52:55]\n"
    "v_mfma_f32_16x16x32_bf16 v[48:51], a[28:31], a[80:83], v[48:51]\n"
    "v_mfma_f32_16x16x32_bf16 v[48:51], a[24:27], a[64:67], v[48:51]\n"
    "s_waitcnt vmcnt(14) lgkmcnt(0)\n"
    "v_mfma_f32_16x16x32_bf16 v[64:67], a[32:35], a[64:67], v[64:67]\n"
    "s_barrier\n"
    "v_mfma_f32_16x16x32_bf16 v[64:67], a[36:39], a[80:83], v[64:67]\n"
    "s_sub_u32 s60, s60, 1\n"
    "ds_read_b64_tr_b16 a[0:1], v128 offset:0\n"
    "v_mfma_f32_16x16x32_bf16 v[68:71], a[36:39], a[84:87], v[68:71]\n"
    "s_cmp_gt_i32 s60, 0\n"
    "ds_read_b64_tr_b16 a[2:3], v128 offset:4096\n"
    "v_mfma_f32_16x16x32_bf16 v[68:71], a[32:35], a[68:71], v[68:71]\n"
    "s_cselect_b32 s42, s42, 0\n"
    "ds_read_b64_tr_b16 a[4:5], v128 offset:8192\n"
    "v_mfma_f32_16x16x32_bf16 v[72:75], a[32:35], a[72:75], v[72:75]\n"
    "ds_read_b64_tr_b16 a[6:7], v128 offset:12288\n"
    "v_mfma_f32_16x16x32_bf16 v[72:75], a[36:39], a[88:91], v[72:75]\n"
    "buffer_load_dwordx4 a[144:147], %12, s[36:39], s52 offen sc1\n"
    "v_mfma_f32_16x16x32_bf16 v[76:79], a[36:39], a[92:95], v[76:79]\n"
    "ds_read_b64_tr_b16 a[8:9], v129 offset:0\n"
    "v_mfma_f32_16x16x32_bf16 v[76:79], a[32:35], a[76:79], v[76:79]\n"
    "ds_read_b64_tr_b16 a[10:11], v129 offset:4096\n"
    "v_mfma_f32_16x16x32_bf16 v[92:95], a[40:43], a[76:79], v[92:95]\n"
    "s_add_u32 m0, s57, s56\n"
    "ds_read_b64_tr_b16 a[12:13], v129 offset:8192\n"
    "v_mfma_f32_16x16x32_bf16 v[92:95], a[44:47], a[92:95], v[92:95]\n"
    "buffer_load_dwordx4 %15, s[40:43], s53 offen lds\n"
    "v_mfma_f32_16x16x32_bf16 v[88:91], a[44:47], a[88:91], v[88:91]\n"
    "ds_read_b64_tr_b16 a[14:15], v129 offset:12288\n"
    "v_mfma_f32_16x16x32_bf16 v[88:91], a[40:43], a[72:75], v[88:91]\n"
    "ds_read_b64_tr_b16 a[16:17], v128 offset:512\n"
    "v_mfma_f32_16x16x32_bf16 v[84:87], a[40:43], a[68:71], v[84:87]\n"
    "ds_read_b64_tr_b16 a[18:19], v128 offset:4608\n"
    "v_mfma_f32_16x16x32_bf16 v[84:87], a[44:47], a[84:87], v[84:87]\n"
    "buffer_load_dwordx4 a[148:151], %12, s[36:39], s52 offen offset:1024 sc1\n"
    "v_mfma_f32_16x16x32_bf16 v[80:83], a[44:47], a[80:83], v[80:83]\n"
    "ds_read_b64_tr_b16 a[20:21], v128 offset:8704\n"
    "v_mfma_f32_16x16x32_bf16 v[80:83], a[40:43], a[64:67], v[80:83]\n"
    "ds_read_b64_tr_b16 a[22:23], v128 offset:12800\n"
    "v_mfma_f32_16x16x32_bf16 v[96:99], a[48:51], a[64:67], v[96:99]\n"
    "s_add_u32 m0, m0, 1024\n"
    "ds_read_b64_tr_b16 a[24:25], v129 offset:512\n"
    "v_mfma_f32_16x16x32_bf16 v[96:99], a[52:55], a[80:83], v[96:99]\n"
    "buffer_load_dwordx4 %16, s[40:43], s53 offen lds\n"
    "v_mfma_f32_16x16x32_bf16 v[100:103], a[52:55], a[84:87], v[100:103]\n"
    "ds_read_b64_tr_b16 a[26:27], v129 offset:4608\n"
    "v_mfma_f32_16x16x32_bf16 v[100:103], a[48:51], a[68:71], v[100:103]\n"
    "ds_read_b64_tr_b16 a[28:29], v129 offset:8704\n"
    "v_mfma_f32_16x16x32_bf16 v[104:107], a[48:51], a[72:75], v[104:107]\n"
    "ds_read_b64_tr_b16 a[30:31], v129 offset:12800\n"
    "v_mfma_f32_16x16x32_bf16 v[104:107], a[52:55], a[88:91], v[104:107]\n"
    "buffer_load_dwordx4 a[152:155], %12, s[36:39], s52 offen offset:2048 sc1\n"
    "v_mfma_f32_16x16x32_bf16 v[108:111], a[52:55], a[92:95], v[108:111]\n"
    "v_mfma_f32_16x16x32_bf16 v[108:111], a[48:51], a[76:79], v[108:111]\n"
    "v_mfma_f32_16x16x32_bf16 v[124:127], a[56:59], a[76:79], v[124:127]\n"
    "v_mfma_f32_16x16x32_bf16 v[124:127], a[60:63], a[92:95], v[124:127]\n"
    "buffer_load_dwordx4 a[156:159], %12, s[36:39], s52 offen offset:3072 sc1\n"
    "v_mfma_f32_16x16x32_bf16 v[120:123], a[60:63], a[88:91], v[120:123]\n"
    "s_add_u32 s52, s52, 4096\n"
    "v_mfma_f32_16x16x32_bf16 v[120:123], a[56:59], a[72:75], v[120:123]\n"
    "v_mfma_f32_16x16x32_bf16 v[116:119], a[56:59], a[68:71], v[116:119]\n"
    "v_mfma_f32_16x16x32_bf16 v[116:119], a[60:63], a[84:87], v[116:119]\n"
    "v_mfma_f32_16x16x32_bf16 v[112:115], a[60:63], a[80:83], v[112:115]\n"
    "v_mfma_f32_16x16x32_bf16 v[112:115], a[56:59], a[64:67], v[112:115]\n"
    "s_waitcnt vmcnt(12) lgkmcnt(0)\n"
    "v_mfma_f32_16x16x32_bf16 v[0:3], a[0:3], a[96:99], v[0:3]\n"
    "s_sub_u32 s59, s59, 1\n"
    "ds_read_b64_tr_b16 a[32:33], v128 offset:1024\n"
    "v_mfma_f32_16x16x32_bf16 v[0:3], a[4:7], a[112:115], v[0:3]\n"
    "s_cmp_gt_i32 s59, 0\n"
    "ds_read_b64_tr_b16 a[34:35], v128 offset:5120\n"
    "v_mfma_f32_16x16x32_bf16 v[4:7], a[4:7], a[116:119], v[4:7]\n"
    "s_cselect_b32 s38, s38, 0\n"
    "ds_read_b64_tr_b16 a[36:37], v128 offset:9216\n"
    "v_mfma_f32_16x16x32_bf16 v[4:7], a[0:3], a[100:103], v[4:7]\n"
    "ds_read_b64_tr_b16 a[38:39], v128 offset:13312\n"
    "v_mfma_f32_16x16x32_bf16 v[8:11], a[0:3], a[104:107], v[8:11]\n"
    "buffer_load_dwordx4 a[64:67], %12, s[36:39], s52 offen sc1\n"
    "v_mfma_f32_16x16x32_bf16 v[8:11], a[4:7], a[120:123], v[8:11]\n"
    "ds_read_b64_tr_b16 a[40:41], v129 offset:1024\n"
    "v_mfma_f32_16x16x32_bf16 v[12:15], a[4:7], a[124:127], v[12:15]\n"
    "s_add_u32 m0, m0, 1024\n"
    "ds_read_b64_tr_b16 a[42:43], v129 offset:5120\n"
    "v_mfma_f32_16x16x32_bf16 v[12:15], a[0:3], a[108:111], v[12:15]\n"
    "buffer_load_dwordx4 %17, s[40:43], s53 offen lds\n"
    "v_mfma_f32_16x16x32_bf16 v[28:31], a[8:11], a[108:111], v[28:31]\n"
    "ds_read_b64_tr_b16 a[44:45], v129 offset:9216\n"
    "v_mfma_f32_16x16x32_bf16 v[28:31], a[12:15], a[124:127], v[28:31]\n"
    "ds_read_b64_tr_b16 a[46:47], v129 offset:13312\n"
    "v_mfma_f32_16x16x32_bf16 v[24:27], a[12:15], a[120:123], v[24:27]\n"
    "buffer_load_dwordx4 a[68:71], %12, s[36:39], s52 offen offset:1024 sc1\n"
    "v_mfma_f32_16x16x32_bf16 v[24:27], a[8:11], a[104:107], v[24:27]\n"
    "ds_read_b64_tr_b16 a[48:49], v128 offset:1536\n"
    "v_mfma_f32_16x16x32_bf16 v[20:23], a[8:11], a[100:103], v[20:23]\n"
    "s_add_u32 m0, m0, 1024\n"
    "ds_read_b64_tr_b16 a[50:51], v128 offset:5632\n"
    "v_mfma_f32_16x16x32_bf16 v[20:23], a[12:15], a[116:119], v[20:23]\n"
    "buffer_load_dwordx4 %18, s[40:43], s53 offen lds\n"
    "v_mfma_f32_16x16x32_bf16 v[16:19], a[12:15], a[112:115], v[16:19]\n"
    "s_add_u32 s53, s53, 131072\n"
    "ds_read_b64_tr_b16 a[52:53], v128 offset:9728\n"
    "v_mfma_f32_16x16x32_bf16 v[16:19], a[8:11], a[96:99], v[16:19]\n"
    "s_add_u32 s57, s57, 16384\n"
    "ds_read_b64_tr_b16 a[54:55], v128 offset:13824\n"
    "v_mfma_f32_16x16x32_bf16 v[32:35], a[16:19], a[96:99], v[32:35]\n"
    "s_cmp_ge_u32 s57, 49152\n"
    "buffer_load_dwordx4 a[72:75], %12, s[36:39], s52 offen offset:2048 sc1\n"
    "v_mfma_f32_16x16x32_bf16 v[32:35], a[20:23], a[112:115], v[32:35]\n"
    "s_cselect_b32 s57, 0, s57\n"
    "ds_read_b64_tr_b16 a[56:57], v129 offset:1536\n"
    "v_mfma_f32_16x16x32_bf16 v[36:39], a[20:23], a[116:119], v[36:39]\n"
    "ds_read_b64_tr_b16 a[58:59], v129 offset:5632\n"
    "v_mfma_f32_16x16x32_bf16 v[36:39], a[16:19], a[100:103], v[36:39]\n"
    "buffer_load_dwordx4 a[76:79], %12, s[36:39], s52 offen offset:3072 sc1\n"
    "v_mfma_f32_16x16x32_bf16 v[40:43], a[16:19], a[104:107], v[40:43]\n"
    "s_add_u32 s52, s52, 4096\n"
    "ds_read_b64_tr_b16 a[60:61], v129 offset:9728\n"
    "v_mfma_f32_16x16x32_bf16 v[40:43], a[20:23], a[120:123], v[40:43]\n"
    "ds_read_b64_tr_b16 a[62:63], v129 offset:13824\n"
    "v_mfma_f32_16x16x32_bf16 v[44:47], a[20:23], a[124:127], v[44:47]\n"
    "s_add_u32 s58, s58, 16384\n"
    "v_mfma_f32_16x16x32_bf16 v[44:47], a[16:19], a[108:111], v[44:47]\n"
    "s_cmp_ge_u32 s58, 49152\n"
    "v_mfma_f32_16x16x32_bf16 v[60:63], a[24:27], a[108:111], v[60:63]\n"
    "s_cselect_b32 s58, 0, s58\n"
    "v_mfma_f32_16x16x32_bf16 v[60:63], a[28:31], a[124:127], v[60:63]\n"
    "v_add_u32 v128, s58, %13\n"
    "v_add_u32 v129, s58, %14\n"
    "v_mfma_f32_16x16x32_bf16 v[56:59], a[28:31], a[120:123], v[56:59]\n"
    "v_mfma_f32_16x16x32_bf16 v[56:59], a[24:27], a[104:107], v[56:59]\n"
    "v_mfma_f32_16x16x32_bf16 v[52:55], a[24:27], a[100:103], v[52:55]\n"
    "v_mfma_f32_16x16x32_bf16 v[52:55], a[28:31], a[116:119], v[52:55]\n"
    "v_mfma_f32_16x16x32_bf16 v[48:51], a[28:31], a[112:115], v[48:51]\n"
    "v_mfma_f32_16x16x32_bf16 v[48:51], a[24:27], a[96:99], v[48:51]\n"
    "s_waitcnt vmcnt(14) lgkmcnt(0)\n"
    "v_mfma_f32_16x16x32_bf16 v[64:67], a[32:35], a[96:99], v[64:67]\n"
    "s_barrier\n"
    "v_mfma_f32_16x16x32_bf16 v[64:67], a[36:39], a[112:115], v[64:67]\n"
    "s_sub_u32 s60, s60, 1\n"
    "ds_read_b64_tr_b16 a[0:1], v128 offset:0\n"
    "v_mfma_f32_16x16x32_bf16 v[68:71], a[36:39], a[116:119], v[68:71]\n"
    "s_cmp_gt_i32 s60, 0\n"
    "ds_read_b64_tr_b16 a[2:3], v128 offset:4096\n"
    "v_mfma_f32_16x16x32_bf16 v[68:71], a[32:35], a[100:103], v[68:71]\n"
    "s_cselect_b32 s42, s42, 0\n"
    "ds_read_b64_tr_b16 a[4:5], v128 offset:8192\n"
    "v_mfma_f32_16x16x32_bf16 v[72:75], a[32:35], a[104:107], v[72:75]\n"
    "ds_read_b64_tr_b16 a[6:7], v128 offset:12288\n"
    "v_mfma_f32_16x16x32_bf16 v[72:75], a[36:39], a[120:123], v[72:75]\n"
    "buffer_load_dwordx4 a[80:83], %12, s[36:39], s52 offen sc1\n"
    "v_mfma_f32_16x16x32_bf16 v[76:79], a[36:39], a[124:127], v[76:79]\n"
    "ds_read_b64_tr_b16 a[8:9], v129 offset:0\n"
    "v_mfma_f32_16x16x32_bf16 v[76:79], a[32:35], a[108:111], v[76:79]\n"
    "ds_read_b64_tr_b16 a[10:11], v129 offset:4096\n"
    "v_mfma_f32_16x16x32_bf16 v[92:95], a[40:43], a[108:111], v[92:95]\n"
    "s_add_u32 m0, s57, s56\n"
    "ds_read_b64_tr_b16 a[12:13], v129 offset:8192\n"
    "v_mfma_f32_16x16x32_bf16 v[92:95], a[44:47], a[124:127], v[92:95]\n"
    "buffer_load_dwordx4 %15, s[40:43], s53 offen lds\n"
    "v_mfma_f32_16x16x32_bf16 v[88:91], a[44:47], a[120:123], v[88:91]\n"
    "ds_read_b64_tr_b16 a[14:15], v129 offset:12288\n"
    "v_mfma_f32_16x16x32_bf16 v[88:91], a[40:43], a[104:107], v[88:91]\n"
    "ds_read_b64_tr_b16 a[16:17], v128 offset:512\n"
    "v_mfma_f32_16x16x32_bf16 v[84:87], a[40:43], a[100:103], v[84:87]\n"
    "ds_read_b64_tr_b16 a[18:19], v128 offset:4608\n"
    "v_mfma_f32_16x16x32_bf16 v[84:87], a[44:47], a[116:119], v[84:87]\n"
    "buffer_load_dwordx4 a[84:87], %12, s[36:39], s52 offen offset:1024 sc1\n"
    "v_mfma_f32_16x16x32_bf16 v[80:83], a[44:47], a[112:115], v[80:83]\n"
    "ds_read_b64_tr_b16 a[20:21], v128 offset:8704\n"
    "v_mfma_f32_16x16x32_bf16 v[80:83], a[40:43], a[96:99], v[80:83]\n"
    "ds_read_b64_tr_b16 a[22:23], v128 offset:12800\n"
    "v_mfma_f32_16x16x32_bf16 v[96:99], a[48:51], a[96:99], v[96:99]\n"
    "s_add_u32 m0, m0, 1024\n"
    "ds_read_b64_tr_b16 a[24:25], v129 offset:512\n"
    "v_mfma_f32_16x16x32_bf16 v[96:99], a[52:55], a[112:115], v[96:99]\n"
    "buffer_load_dwordx4 %16, s[40:43], s53 offen lds\n"
    "v_mfma_f32_16x16x32_bf16 v[100:103], a[52:55], a[116:119], v[100:103]\n"
    "ds_read_b64_tr_b16 a[26:27], v129 offset:4608\n"
    "v_mfma_f32_16x16x32_bf16 v[100:103], a[48:51], a[100:103], v[100:103]\n"
    "ds_read_b64_tr_b16 a[28:29], v129 offset:8704\n"
    "v_mfma_f32_16x16x32_bf16 v[104:107], a[48:51], a[104:107], v[104:107]\n"
    "ds_read_b64_tr_b16 a[30:31], v129 offset:12800\n"
    "v_mfma_f32_16x16x32_bf16 v[104:107], a[52:55], a[120:123], v[104:107]\n"
    "buffer_load_dwordx4 a[88:91], %12, s[36:39], s52 offen offset:2048 sc1\n"
    "v_mfma_f32_16x16x32_bf16 v[108:111], a[52:55], a[124:127], v[108:111]\n"
    "v_mfma_f32_16x16x32_bf16 v[108:111], a[48:51], a[108:111], v[108:111]\n"
    "v_mfma_f32_16x16x32_bf16 v[124:127], a[56:59], a[108:111], v[124:127]\n"
    "v_mfma_f32_16x16x32_bf16 v[124:127], a[60:63], a[124:127], v[124:127]\n"
    "buffer_load_dwordx4 a[92:95], %12, s[36:39], s52 offen offset:3072 sc1\n"
    "v_mfma_f32_16x16x32_bf16 v[120:123], a[60:63], a[120:123], v[120:123]\n"
    "s_add_u32 s52, s52, 4096\n"
    "v_mfma_f32_16x16x32_bf16 v[120:123], a[56:59], a[104:107], v[120:123]\n"
    "v_mfma_f32_16x16x32_bf16 v[116:119], a[56:59], a[100:103], v[116:119]\n"
    "v_mfma_f32_16x16x32_bf16 v[116:119], a[60:63], a[116:119], v[116:119]\n"
    "v_mfma_f32_16x16x32_bf16 v[112:115], a[60:63], a[112:115], v[112:115]\n"
    "v_mfma_f32_16x16x32_bf16 v[112:115], a[56:59], a[96:99], v[112:115]\n"
    "s_waitcnt vmcnt(12) lgkmcnt(0)\n"
    "s_waitcnt vmcnt(0)\n"
    "s_nop 7\n"
    "s_nop 7\n"
      : "=&{v[0:31]}"(a0), "=&{v[32:63]}"(a1), "=&{v[64:95]}"(a2), "=&{v[96:127]}"(a3), "=&{v[132:147]}"(xr)
      : "s"(ws+WS_ACT), "s"(M_*7168u), "s"(Wd), "s"(3584u*2048u), "s"(offX), "s"(offW), "s"(wdst),
        "v"(vXl), "v"(vWr0), "v"(vWr1), "v"(vWd[0]), "v"(vWd[1]), "v"(vWd[2]), "v"(vWd[3]), "s"(ws+WS_X1), "v"(vRes), "s"(cofs), "s"(CNT(rt,C_A+sp))
      : "memory", "vcc", "scc", "m0", "s36", "s37", "s38", "s39", "s40", "s41", "s42", "s43", "s44", "s45", "s46", "s47", "s48", "s49", "s50", "s51", "s52", "s53", "s54", "s55", "s56", "s57", "s58", "s59", "s60", "s61", "s62", "s63", "s64", "s65", "s66", "s67", "s68", "s69", "s70", "s71", "s72", "s73", "s74", "s75", "s76", "s77", "s78", "s79", "s80", "s81", "s82", "s83", "s84", "s85", "s86", "s87", "s88", "s89", "s90", "s91", "s92", "s93", "s94", "s95", "s96", "s97", "s98", "s99", "v128", "v129", "v130", "v131", "a0", "a1", "a2", "a3", "a4", "a5", "a6", "a7", "a8", "a9", "a10", "a11", "a12", "a13", "a14", "a15", "a16", "a17", "a18", "a19", "a20", "a21", "a22", "a23", "a24", "a25", "a26", "a27", "a28", "a29", "a30", "a31", "a32", "a33", "a34", "a35", "a36", "a37", "a38", "a39", "a40", "a41", "a42", "a43", "a44", "a45", "a46", "a47", "a48", "a49", "a50", "a51", "a52", "a53", "a54", "a55", "a56", "a57", "a58", "a59", "a60", "a61", "a62", "a63", "a64", "a65", "a66", "a67", "a68", "a69", "a70", "a71", "a72", "a73", "a74", "a75", "a76", "a77", "a78", "a79", "a80", "a81", "a82", "a83", "a84", "a85", "a86", "a87", "a88", "a89", "a90", "a91", "a92", "a93", "a94", "a95", "a96", "a97", "a98", "a99", "a100", "a101", "a102", "a103", "a104", "a105", "a106", "a107", "a108", "a109", "a110", "a111", "a112", "a113", "a114", "a115", "a116", "a117", "a118", "a119", "a120", "a121", "a122", "a123", "a124", "a125", "a126", "a127", "a128", "a129", "a130", "a131", "a132", "a133", "a134", "a135", "a136", "a137", "a138", "a139", "a140", "a141", "a142", "a143", "a144", "a145", "a146", "a147", "a148", "a149", "a150", "a151", "a152", "a153", "a154", "a155", "a156", "a157", "a158", "a159");
   { PHASE() LANE() (void)q;(void)p;(void)vXl;
    { const unsigned rt=rt5, sp=sp5, To=rt*4+sp; const unsigned T=rt*4+w, col0=ct*128;

    EXCHANGE4(C_X5)

    const __amdgpu_buffer_rsrc_t rOut=mkrsrc(out,M_*2048u);
    #pragma unroll
    for(int m=0;m<4;m++){ unsigned o[4]; const u32x4 gb=*(const u32x4*)(smem_+65536+3*2048+(ct*4+m)*64+g*16);
      #pragma unroll
      for(int e=0;e<4;e++) o[e]=cvtpk(bflo(xr[m*4+e])+bflo(gb[e])*y[m][2*e], bfhi(xr[m*4+e])+bfhi(gb[e])*y[m][2*e+1]);
      { auto r=__builtin_amdgcn_permlane32_swap(o[0],o[2],false,false); o[0]=r[0]; o[2]=r[1]; r=__builtin_amdgcn_permlane32_swap(o[1],o[3],false,false); o[1]=r[0]; o[3]=r[1];
        r=__builtin_amdgcn_permlane16_swap(o[0],o[2],false,false); o[0]=r[0]; o[2]=r[1]; r=__builtin_amdgcn_permlane16_swap(o[1],o[3],false,false); o[1]=r[0]; o[3]=r[1]; }
      OUTST(rOut,((64*To+16*w+i16)*1024+col0+32*m+8*g)*2,((u32x4){o[0],o[1],o[2],o[3]})); }

    }
   }
   }
  }
}

__global__ void k_text_end(){ asm volatile("L_text_end:\n s_nop 0"); }
extern "C" void kernel_launch(void* const* d_in, const int* in_sizes, int n_in, void* d_out, int out_size, void* d_ws, size_t ws_size, hipStream_t stream) {
  const u16* x=(const u16*)d_in[0]; const u16* kc=(const u16*)d_in[1]; const u16* vc=(const u16*)d_in[2]; const u16* wq=(const u16*)d_in[3]; const u16* wo=(const u16*)d_in[4];
  const u16* wg=(const u16*)d_in[6]; const u16* wu=(const u16*)d_in[7]; const u16* wd=(const u16*)d_in[8]; const u16* c=(const u16*)d_in[9]; const u16* wmod=(const u16*)d_in[10];
  (void)hipFuncSetAttribute((const void*)mega, hipFuncAttributeMaxDynamicSharedMemorySize, 163840);
  mega<<<256,256,163840,stream>>>(x,kc,vc,wq,wo,wg,wu,wd,c,wmod,(char*)d_ws,(u16*)d_out);
  (void)k_text_end;
}
```
